```python
import torch

def _softmax(x, axis=-1):
    return torch.softmax(x, dim=axis)
def _gelu(x):
    return torch.nn.functional.gelu(x, approximate="tanh")
def _silu(x):
    return torch.nn.functional.silu(x)
def _sigmoid(x):
    return torch.sigmoid(x)
def _layernorm(x, axis=-1, eps=1e-5):
    m = x.mean(dim=axis, keepdim=True)
    v = x.var(dim=axis, keepdim=True, unbiased=False)
    return (x - m) / torch.sqrt(v + eps)
def _scatter_add(tgt, idx, src, axis=0):
    return tgt.clone().index_add_(axis, idx, src)
def _segment_sum(src, seg, n):
    out = torch.zeros((n,) + src.shape[1:], dtype=src.dtype)
    return out.index_add_(0, seg, src)
def _pad(x, pw):
    flat = tuple(v for pair in reversed(pw) for v in pair)
    return torch.nn.functional.pad(x, flat)

kernel_name = 'concat_heads__mha_block_cached__gqa_block_cached__near_ridge'

def setup_inputs():

    _w = (lambda a: (lambda t: t.to(torch.bfloat16) if t.is_floating_point() else t)(torch.as_tensor(a)))
    def _A_setup():
        B, Sq, D = 8, 144, 640
        H, Sk, Dh = 32, 1024, 128
        return {
            "x": _w((lambda *s: torch.randn(*s, dtype=torch.bfloat16) * 0.8)(B, Sq, D)),
            "k_cache": _w((lambda *s: torch.randn(*s, dtype=torch.bfloat16) * 0.8)(B, H, Sk, Dh)),
            "v_cache": _w((lambda *s: torch.randn(*s, dtype=torch.bfloat16) * 0.05)(B, H, Sk, Dh)),
            "wq": _w((lambda *s: torch.randn(*s, dtype=torch.bfloat16) * 0.8)(D, H * Dh) * (D ** -0.5)),
            "wo": _w((lambda *s: torch.randn(*s, dtype=torch.bfloat16) * 0.05)(H * Dh, D) * ((H * Dh) ** -0.5)),
        }
    _A_in = _A_setup()

    def _B_setup():
        B, Sq, D = 8, 144, 640
        Hq, Hkv, Sk, Dh = 32, 32, 1024, 128
        assert Hq % Hkv == 0, (Hq, Hkv)
        return {
            "x": _w((lambda *s: torch.randn(*s, dtype=torch.bfloat16) * 0.8)(B, Sq, D)),
            "k_cache": _w((lambda *s: torch.randn(*s, dtype=torch.bfloat16) * 0.8)(B, Hkv, Sk, Dh)),
            "v_cache": _w((lambda *s: torch.randn(*s, dtype=torch.bfloat16) * 0.05)(B, Hkv, Sk, Dh)),
            "wq": _w((lambda *s: torch.randn(*s, dtype=torch.bfloat16) * 0.8)(D, Hq * Dh) * (D ** -0.5)),
            "wo": _w((lambda *s: torch.randn(*s, dtype=torch.bfloat16) * 0.05)(Hq * Dh, D) * ((Hq * Dh) ** -0.5)),
        }
    _B_in = _B_setup()

    return {
        "A_x": _A_in["x"],
        "A_k_cache": _A_in["k_cache"],
        "A_v_cache": _A_in["v_cache"],
        "A_wq": _A_in["wq"],
        "A_wo": _A_in["wo"],
        "B_x": _B_in["x"],
        "B_k_cache": _B_in["k_cache"],
        "B_v_cache": _B_in["v_cache"],
        "B_wq": _B_in["wq"],
        "B_wo": _B_in["wo"],
    }

def reference(A_x, A_k_cache, A_v_cache, A_wq, A_wo, B_x, B_k_cache, B_v_cache, B_wq, B_wo):
    def _A_ref(x, k_cache, v_cache, wq, wo):
        B, Sq, D = x.shape
        H, Dh = 32, 128
        q = (lambda x, *d: x.permute(*d))((x @ wq).reshape(B, Sq, H, Dh), 0, 2, 1, 3)
        scores = torch.einsum("bhqd,bhkd->bhqk", q, k_cache) * (Dh ** -0.5)
        p = _softmax(scores, axis=-1)
        o = torch.einsum("bhqk,bhkd->bhqd", p, v_cache)
        return (lambda x, *d: x.permute(*d))(o, 0, 2, 1, 3).reshape(B, Sq, H * Dh) @ wo

    def _B_ref(x, k_cache, v_cache, wq, wo):
        B, Sq, D = x.shape
        Hq, Dh = 32, 128
        Hkv = k_cache.shape[1]
        rep = Hq // Hkv
        q = (lambda x, *d: x.permute(*d))((x @ wq).reshape(B, Sq, Hq, Dh), 0, 2, 1, 3)
        k = (lambda x, n, axis: torch.repeat_interleave(x, n, dim=axis))(k_cache, rep, axis=1)
        v = (lambda x, n, axis: torch.repeat_interleave(x, n, dim=axis))(v_cache, rep, axis=1)
        scores = torch.einsum("bhqd,bhkd->bhqk", q, k) * (Dh ** -0.5)
        p = _softmax(scores, axis=-1)
        o = torch.einsum("bhqk,bhkd->bhqd", p, v)
        return (lambda x, *d: x.permute(*d))(o, 0, 2, 1, 3).reshape(B, Sq, Hq * Dh) @ wo

    a = _A_ref(A_x, A_k_cache, A_v_cache, A_wq, A_wo)
    b = _B_ref(B_x, B_k_cache, B_v_cache, B_wq, B_wo)
    return torch.concatenate([a, b], axis=-1)
```

```cpp
#include <hip/hip_runtime.h>
#include <hip/hip_bf16.h>
typedef unsigned short u16;
__device__ __forceinline__ float b2f(u16 x){ return __uint_as_float(((unsigned)x)<<16); }
__device__ __forceinline__ u16 f2b(float f){ unsigned u=__float_as_uint(f); u += 0x7fff + ((u>>16)&1); return (u16)(u>>16); }
__global__ void gemm_naive(const u16* A, const u16* Bm, u16* C, int M, int N, int K, int ldc){
  __shared__ float As[32][33], Bs[32][33];
  int tx=threadIdx.x&31, ty=threadIdx.x>>5;
  int m0=blockIdx.y*32, n0=blockIdx.x*32;
  float acc[4]={0,0,0,0};
  for(int k0=0;k0<K;k0+=32){
    for(int i=0;i<4;i++){ int r=ty+8*i; As[r][tx]=b2f(A[(size_t)(m0+r)*K+k0+tx]); Bs[r][tx]=b2f(Bm[(size_t)(k0+r)*N+n0+tx]); }
    __syncthreads();
    for(int k=0;k<32;k++){ float b=Bs[k][tx]; for(int i=0;i<4;i++) acc[i]+=As[ty+8*i][k]*b; }
    __syncthreads();
  }
  for(int i=0;i<4;i++) C[(size_t)(m0+ty+8*i)*ldc+n0+tx]=f2b(acc[i]);
}
__global__ void attn_naive(const u16* Q, const u16* K, const u16* V, u16* O){
  __shared__ float qs[16][128]; __shared__ float s[16][1024];
  int h=blockIdx.x&31, b=blockIdx.x>>5, q0=blockIdx.y*16; int t=threadIdx.x;
  for(int i=t;i<16*128;i+=256) qs[i>>7][i&127]=b2f(Q[(size_t)(b*144+q0+(i>>7))*4096+h*128+(i&127)]);
  __syncthreads();
  const u16* Kp=K+(size_t)(b*32+h)*1024*128; const u16* Vp=V+(size_t)(b*32+h)*1024*128;
  for(int k=t;k<1024;k+=256){ float a[16]; for(int i=0;i<16;i++)a[i]=0; for(int d=0;d<128;d++){ float kv=b2f(Kp[k*128+d]); for(int i=0;i<16;i++)a[i]+=qs[i][d]*kv;} for(int i=0;i<16;i++) s[i][k]=a[i]*0.08838834764831845f; }
  __syncthreads();
  { int r=t>>4, c=t&15; float m=-1e30f; for(int k=c;k<1024;k+=16) m=fmaxf(m,s[r][k]); for(int o=1;o<16;o<<=1) m=fmaxf(m,__shfl_xor(m,o)); float l=0; for(int k=c;k<1024;k+=16){ float p=__expf(s[r][k]-m); s[r][k]=p; l+=p;} for(int o=1;o<16;o<<=1) l+=__shfl_xor(l,o); float il=1.f/l; for(int k=c;k<1024;k+=16) s[r][k]*=il; }
  __syncthreads();
  { int d=t&127, half=t>>7; float a[8]; for(int i=0;i<8;i++)a[i]=0; for(int k=0;k<1024;k++){ float v=b2f(Vp[k*128+d]); for(int i=0;i<8;i++) a[i]+=s[half*8+i][k]*v; } for(int i=0;i<8;i++) O[(size_t)(b*144+q0+half*8+i)*4096+h*128+d]=f2b(a[i]); }
}
extern "C" void kernel_launch(void* const* d_in, const int* in_sizes, int n_in, void* d_out, int out_size, void* d_ws, size_t ws_size, hipStream_t stream){
  u16* ws=(u16*)d_ws; u16* out=(u16*)d_out;
  for(int br=0;br<2;br++){
    const u16* x=(const u16*)d_in[br*5+0]; const u16* k=(const u16*)d_in[br*5+1]; const u16* v=(const u16*)d_in[br*5+2]; const u16* wq=(const u16*)d_in[br*5+3]; const u16* wo=(const u16*)d_in[br*5+4];
    u16* q=ws+(size_t)br*2*1152*4096; u16* o=q+(size_t)1152*4096;
    gemm_naive<<<dim3(4096/32,1152/32),256,0,stream>>>(x,wq,q,1152,4096,640,4096);
    attn_naive<<<dim3(256,9),256,0,stream>>>(q,k,v,o);
    gemm_naive<<<dim3(640/32,1152/32),256,0,stream>>>(o,wo,out+br*640,1152,640,4096,1280);
  }
}
```

```cpp
#include <hip/hip_runtime.h>
#include <hip/hip_bf16.h>

__global__ __launch_bounds__(256, 1) void fused_k(const char* __restrict__ xA, const char* __restrict__ kA, const char* __restrict__ vA, const char* __restrict__ wqA, const char* __restrict__ woA,
                                                  const char* __restrict__ xB, const char* __restrict__ kB, const char* __restrict__ vB, const char* __restrict__ wqB, const char* __restrict__ woB,
                                                  char* __restrict__ ws, char* __restrict__ out) {
  __shared__ __attribute__((aligned(16))) char lds[163840];
  const unsigned long long ka = (unsigned long long)__builtin_amdgcn_kernarg_segment_ptr();
  const unsigned id = blockIdx.x;
  const unsigned wave = __builtin_amdgcn_readfirstlane(threadIdx.x >> 6);
  const unsigned ldsb = (unsigned)(size_t)lds;
  asm volatile(
    "; ---- HPRE\n"
    "s_bfe_u32 s6, %1, 0x10002\n"
    "s_mul_i32 s6, s6, 40\n"
    "s_load_dwordx4 s[20:23], %0, 0x50\n"
    "s_load_dwordx8 s[8:15], %0, s6\n"
    "s_add_u32 s7, s6, 32\n"
    "s_load_dwordx2 s[16:17], %0, s7\n"
    "s_mov_b32 s30, %1\n"
    "s_lshr_b32 s31, %1, 3\n"
    "s_lshr_b32 s33, %1, 5\n"
    "s_getpc_b64 s[68:69]\n"
    "Lpcpf:\n"
    "v_mbcnt_lo_u32_b32 v184, -1, 0\n"
    "v_mbcnt_hi_u32_b32 v184, -1, v184\n"
    "v_lshlrev_b32 v184, 7, v184\n"
    "s_mul_i32 s74, s31, ((Lend-Lpcpf)/4096+1)*128\n"
    "v_add_u32 v184, s74, v184\n"
    "v_min_u32 v184, Lend-Lpcpf-4, v184\n"
    "global_load_dword a255, v184, s[68:69]\n"
    "s_mov_b32 s56, %2\n"
    "s_mov_b32 s74, %3\n"
    "s_lshl_b32 s61, s56, 12\n"
    "s_lshl_b32 s62, s56, 11\n"
    "s_add_u32 s62, s62, 117760\n"
    "s_and_b32 s77, s56, 1\n"
    "s_lshr_b32 s76, s56, 1\n"
    "s_lshl_b32 s76, s76, 13\n"
    "s_lshr_b32 s74, s31, 2\n"
    "s_mul_i32 s74, s74, 5\n"
    "s_lshr_b32 s74, s74, 1\n"
    "s_and_b32 s75, s31, 3\n"
    "s_mul_i32 s75, s75, 5\n"
    "s_add_u32 s74, s74, s75\n"
    "s_sub_u32 s75, s74, 20\n"
    "s_cmp_ge_u32 s74, 20\n"
    "s_cselect_b32 s74, s75, s74\n"
    "s_lshl_b32 s57, s74, 18\n"
    "s_lshl_b32 s58, s74, 6\n"
    "s_lshl_b32 s66, s56, 8\n"
    "s_add_u32 s66, s66, 125952\n"
    "v_mbcnt_lo_u32_b32 v164, -1, 0\n"
    "v_mbcnt_hi_u32_b32 v164, -1, v164\n"
    "v_lshrrev_b32 v165, 4, v164\n"
    "v_and_b32 v166, 15, v164\n"
    "v_lshlrev_b32 v167, 2, v165\n"
    "v_xor_b32 v167, v166, v167\n"
    "v_lshlrev_b32 v167, 4, v167\n"
    "v_lshl_add_u32 v168, v165, 13, v167\n"
    "s_lshl_b32 s74, s77, 17\n"
    "s_lshr_b32 s75, s76, 5\n"
    "s_add_u32 s74, s74, s75\n"
    "v_add_u32 v154, s74, v168\n"
    "v_add_u32 v155, 31744, v154\n"
    "v_add_u32 v156, 63488, v154\n"
    "v_add_u32 v157, 95232, v154\n"
    "v_sub_u32 v168, 0, v165\n"
    "v_xor_b32 v168, v164, v168\n"
    "v_and_b32 v168, 3, v168\n"
    "v_lshlrev_b32 v168, 4, v168\n"
    "v_lshrrev_b32 v169, 2, v164\n"
    "v_mul_u32_u24 v169, 1280, v169\n"
    "v_add_u32 v168, v169, v168\n"
    "s_mul_i32 s74, s56, 40960\n"
    "v_add_u32 v158, s74, v168\n"
    "v_add_u32 v159, 19456, v158\n"
    "s_sub_u32 s74, 0, s56\n"
    "v_lshrrev_b32 v169, 2, v164\n"
    "v_xor_b32 v169, s74, v169\n"
    "v_and_b32 v169, 3, v169\n"
    "v_and_b32 v168, 3, v164\n"
    "v_lshlrev_b32 v168, 2, v168\n"
    "v_lshl_add_u32 v168, v169, 4, v168\n"
    "v_mul_u32_u24 v169, 1280, v165\n"
    "v_add_u32 v168, v169, v168\n"
    "s_mul_i32 s74, s56, 5120\n"
    "s_add_u32 s74, s74, 163840\n"
    "v_add_u32 v160, s74, v168\n"
    "s_and_b32 s6, s30, 3\n"
    "s_lshl_b32 s6, s6, 3\n"
    "s_and_b32 s7, s31, 3\n"
    "s_lshl_b32 s7, s7, 1\n"
    "s_add_u32 s5, s6, s7\n"
    "s_lshl_b32 s6, s5, 8\n"
    "s_sub_u32 s32, 5242880, s6\n"
    "s_mul_i32 s7, s33, 184320\n"
    "s_waitcnt lgkmcnt(0)\n"
    "s_add_u32 s14, s14, s6\n"
    "s_addc_u32 s15, s15, 0\n"
    "s_add_u32 s8, s8, s7\n"
    "s_addc_u32 s9, s9, 0\n"
    "s_mov_b64 s[40:41], s[14:15]\n"
    "s_and_b32 s41, s41, 0xffff\n"
    "s_mov_b32 s42, s32\n"
    "s_mov_b32 s43, 0x20000\n"
    "s_mov_b64 s[36:37], s[8:9]\n"
    "s_and_b32 s37, s37, 0xffff\n"
    "s_mov_b32 s38, 184320\n"
    "s_mov_b32 s39, 0x20000\n"
    "s_add_u32 m0, s61, 32768\n"
    "s_nop 0\n"
    "buffer_load_dwordx4 v154, s[40:43], s57 offen lds\n"
    "buffer_load_dwordx4 v155, s[40:43], s57 offen offset:1024 lds\n"
    "buffer_load_dwordx4 v156, s[40:43], s57 offen offset:2048 lds\n"
    "buffer_load_dwordx4 v157, s[40:43], s57 offen offset:3072 lds\n"
    "s_add_u32 m0, s62, 0\n"
    "s_nop 0\n"
    "buffer_load_dwordx4 v158, s[36:39], s58 offen lds\n"
    "buffer_load_dwordx4 v159, s[36:39], s58 offen offset:1024 lds\n"
    "s_add_u32 m0, s66, 0\n"
    "s_nop 0\n"
    "buffer_load_dword v160, s[36:39], s58 offen lds\n"
    "s_add_u32 s58, s58, 64\n"
    "s_cmp_eq_u32 s58, 1280\n"
    "s_cselect_b32 s58, 0, s58\n"
    "s_lshl_b32 s57, s58, 12\n"
    "s_add_u32 m0, s61, 98304\n"
    "s_nop 0\n"
    "buffer_load_dwordx4 v154, s[40:43], s57 offen lds\n"
    "buffer_load_dwordx4 v155, s[40:43], s57 offen offset:1024 lds\n"
    "buffer_load_dwordx4 v156, s[40:43], s57 offen offset:2048 lds\n"
    "buffer_load_dwordx4 v157, s[40:43], s57 offen offset:3072 lds\n"
    "s_add_u32 m0, s62, 9216\n"
    "s_nop 0\n"
    "buffer_load_dwordx4 v158, s[36:39], s58 offen lds\n"
    "buffer_load_dwordx4 v159, s[36:39], s58 offen offset:1024 lds\n"
    "s_add_u32 m0, s66, 9216\n"
    "s_nop 0\n"
    "buffer_load_dword v160, s[36:39], s58 offen lds\n"
    "s_add_u32 s58, s58, 64\n"
    "s_cmp_eq_u32 s58, 1280\n"
    "s_cselect_b32 s58, 0, s58\n"
    "s_lshl_b32 s57, s58, 12\n"
    "s_bfe_u32 s90, s30, 0x10002\n"
    "s_and_b32 s74, s30, 3\n"
    "s_and_b32 s75, s31, 3\n"
    "s_lshl_b32 s6, s33, 5\n"
    "s_add_u32 s6, s6, s5\n"
    "s_lshr_b32 s7, s56, 1\n"
    "s_add_u32 s6, s6, s7\n"
    "s_lshl_b32 s6, s6, 18\n"
    "s_add_u32 s10, s10, s6\n"
    "s_addc_u32 s11, s11, 0\n"
    "s_add_u32 s12, s12, s6\n"
    "s_addc_u32 s13, s13, 0\n"
    "s_lshl_b32 s6, s90, 3\n"
    "s_add_u32 s6, s6, s33\n"
    "s_lshl_b32 s7, s6, 2\n"
    "s_add_u32 s7, s7, s74\n"
    "s_mul_i32 s7, s7, 294912\n"
    "s_add_u32 s24, s20, s7\n"
    "s_addc_u32 s25, s21, 0\n"
    "s_lshl_b32 s7, s6, 2\n"
    "s_add_u32 s7, s7, s75\n"
    "s_mul_i32 s7, s7, 147456\n"
    "s_add_u32 s7, s7, 33554432\n"
    "s_add_u32 s26, s20, s7\n"
    "s_addc_u32 s27, s21, 0\n"
    "s_add_u32 s28, s20, 62914560\n"
    "s_addc_u32 s29, s21, 0\n"
    "s_lshl_b32 s7, s30, 9\n"
    "s_add_u32 s7, s7, 67108864\n"
    "s_add_u32 s34, s20, s7\n"
    "s_addc_u32 s35, s21, 0\n"
    "s_mul_i32 s6, s74, 1310720\n"
    "s_add_u32 s16, s16, s6\n"
    "s_addc_u32 s17, s17, 0\n"
    "s_mul_i32 s6, s31, 40960\n"
    "s_add_u32 s18, s16, s6\n"
    "s_addc_u32 s19, s17, 0\n"
    "s_mul_i32 s6, s75, 320\n"
    "s_add_u32 s16, s16, s6\n"
    "s_addc_u32 s17, s17, 0\n"
    "s_add_u32 s22, s22, s6\n"
    "s_addc_u32 s23, s23, 0\n"
    "s_sub_u32 s16, s16, 4096\n"
    "s_subb_u32 s17, s17, 0\n"
    "s_mul_i32 s6, s33, 368640\n"
    "s_mul_i32 s7, s90, 1280\n"
    "s_add_u32 s6, s6, s7\n"
    "s_add_u32 s22, s22, s6\n"
    "s_addc_u32 s23, s23, 0\n"
    "s_mov_b64 s[44:45], s[10:11]\n"
    "s_and_b32 s45, s45, 0xffff\n"
    "s_mov_b32 s46, 262144\n"
    "s_mov_b32 s47, 0x20000\n"
    "s_mov_b64 s[48:49], s[12:13]\n"
    "s_and_b32 s49, s49, 0xffff\n"
    "s_mov_b32 s50, 262144\n"
    "s_mov_b32 s51, 0x20000\n"
    "s_mov_b64 s[52:53], s[24:25]\n"
    "s_and_b32 s53, s53, 0xffff\n"
    "s_mov_b32 s54, 294912\n"
    "s_mov_b32 s55, 0x20000\n"
    "v_mbcnt_lo_u32_b32 v164, -1, 0\n"
    "v_mbcnt_hi_u32_b32 v164, -1, v164\n"
    "v_lshrrev_b32 v165, 4, v164\n"
    "v_and_b32 v166, 15, v164\n"
    "v_lshrrev_b32 v167, 2, v166\n"
    "v_and_b32 v168, 3, v164\n"
    "v_and_b32 v171, 1, v165\n"
    "v_lshl_add_u32 v169, v165, 3, v167\n"
    "v_lshlrev_b32 v169, 8, v169\n"
    "v_lshl_add_u32 v170, v167, 2, v168\n"
    "v_lshl_add_u32 v169, v170, 4, v169\n"
    "s_lshl_b32 s75, s77, 7\n"
    "v_xor_b32 v169, s75, v169\n"
    "v_add_u32 v144, s76, v169\n"
    "v_xor_b32 v145, 64, v144\n"
    "v_xor_b32 v146, 128, v144\n"
    "v_xor_b32 v147, 192, v144\n"
    "v_add_u32 v148, 65536, v144\n"
    "v_add_u32 v149, 65536, v145\n"
    "v_add_u32 v150, 65536, v146\n"
    "v_add_u32 v151, 65536, v147\n"
    "v_sub_u32 v170, 0, v167\n"
    "v_xor_b32 v170, v165, v170\n"
    "v_and_b32 v170, 3, v170\n"
    "v_lshlrev_b32 v170, 4, v170\n"
    "v_lshl_add_u32 v169, v166, 6, v170\n"
    "v_add_u32 v153, 125952, v169\n"
    "s_lshl_b32 s74, s77, 12\n"
    "s_add_u32 s74, s74, 117760\n"
    "v_add_u32 v152, s74, v169\n"
    "v_lshl_add_u32 v170, v167, 10, v170\n"
    "v_lshl_add_u32 v170, v168, 6, v170\n"
    "s_lshl_b32 s74, s77, 9\n"
    "s_add_u32 s74, s74, s76\n"
    "v_add_u32 v246, s74, v170\n"
    "v_xor_b32 v247, 512, v246\n"
    "v_lshlrev_b32 v169, 10, v165\n"
    "v_lshl_add_u32 v169, v167, 6, v169\n"
    "v_lshrrev_b32 v170, 1, v168\n"
    "v_lshl_add_u32 v170, v171, 1, v170\n"
    "v_lshl_add_u32 v169, v170, 4, v169\n"
    "v_and_b32 v170, 1, v168\n"
    "v_lshl_add_u32 v169, v170, 3, v169\n"
    "s_add_u32 s74, s76, 65536\n"
    "v_add_u32 v248, s74, v169\n"
    "v_xor_b32 v249, 32, v248\n"
    "v_lshlrev_b32 v169, 8, v167\n"
    "v_lshl_add_u32 v169, v165, 6, v169\n"
    "v_lshl_add_u32 v250, v168, 4, v169\n"
    "v_xor_b32 v251, 48, v250\n"
    "v_xor_b32 v252, 32, v250\n"
    "v_xor_b32 v253, 16, v250\n"
    "s_lshl_b32 s67, s30, 1\n"
    "s_lshr_b32 s74, s56, 1\n"
    "s_add_u32 s67, s67, s74\n"
    "s_mul_i32 s67, s67, 0x9e3779b1\n"
    "s_lshr_b32 s67, s67, 27\n"
    "s_lshl_b32 s67, s67, 13\n"
    "s_and_b32 s82, s30, 3\n"
    "s_lshr_b32 s83, s56, 1\n"
    "s_and_b32 s84, s31, 3\n"
    "s_mov_b64 s[80:81], s[28:29]\n"
    "s_mul_i32 s85, s56, 5120\n"
    "s_mul_i32 s88, s56, 20480\n"
    "s_lshl_b32 s86, s56, 12\n"
    "s_lshl_b32 s87, s56, 9\n"
    "s_add_u32 s87, s87, 16384\n"
    "v_mbcnt_lo_u32_b32 v174, -1, 0\n"
    "v_mbcnt_hi_u32_b32 v174, -1, v174\n"
    "v_lshrrev_b32 v175, 4, v174\n"
    "v_and_b32 v176, 15, v174\n"
    "v_lshrrev_b32 v176, 2, v176\n"
    "v_and_b32 v177, 3, v174\n"
    "v_and_b32 v178, 1, v175\n"
    "v_lshrrev_b32 v179, 1, v175\n"
    "v_mul_u32_u24 v179, 2560, v179\n"
    "v_lshl_add_u32 v180, v178, 2, v176\n"
    "v_lshl_add_u32 v179, v180, 6, v179\n"
    "v_lshrrev_b32 v180, 1, v177\n"
    "v_lshl_add_u32 v180, v178, 1, v180\n"
    "v_lshl_add_u32 v179, v180, 4, v179\n"
    "v_and_b32 v180, 1, v177\n"
    "v_lshl_add_u32 v179, v180, 3, v179\n"
    "s_mul_i32 s74, s77, 3\n"
    "s_add_u32 s74, s74, 0\n"
    "s_sub_u32 s75, s74, 5\n"
    "s_cmp_ge_u32 s74, 5\n"
    "s_cselect_b32 s74, s75, s74\n"
    "s_mul_i32 s75, s83, 5\n"
    "s_add_u32 s74, s74, s75\n"
    "s_and_b32 s75, s74, 1\n"
    "s_lshl_b32 s75, s75, 5\n"
    "v_xor_b32 v180, s75, v179\n"
    "s_lshr_b32 s75, s74, 1\n"
    "s_lshl_b32 s75, s75, 9\n"
    "v_add_u32 v180, s75, v180\n"
    "v_accvgpr_write_b32 a244, v180\n"
    "s_mul_i32 s74, s77, 3\n"
    "s_add_u32 s74, s74, 1\n"
    "s_sub_u32 s75, s74, 5\n"
    "s_cmp_ge_u32 s74, 5\n"
    "s_cselect_b32 s74, s75, s74\n"
    "s_mul_i32 s75, s83, 5\n"
    "s_add_u32 s74, s74, s75\n"
    "s_and_b32 s75, s74, 1\n"
    "s_lshl_b32 s75, s75, 5\n"
    "v_xor_b32 v180, s75, v179\n"
    "s_lshr_b32 s75, s74, 1\n"
    "s_lshl_b32 s75, s75, 9\n"
    "v_add_u32 v180, s75, v180\n"
    "v_accvgpr_write_b32 a245, v180\n"
    "s_mul_i32 s74, s77, 3\n"
    "s_add_u32 s74, s74, 2\n"
    "s_sub_u32 s75, s74, 5\n"
    "s_cmp_ge_u32 s74, 5\n"
    "s_cselect_b32 s74, s75, s74\n"
    "s_mul_i32 s75, s83, 5\n"
    "s_add_u32 s74, s74, s75\n"
    "s_and_b32 s75, s74, 1\n"
    "s_lshl_b32 s75, s75, 5\n"
    "v_xor_b32 v180, s75, v179\n"
    "s_lshr_b32 s75, s74, 1\n"
    "s_lshl_b32 s75, s75, 9\n"
    "v_add_u32 v180, s75, v180\n"
    "v_accvgpr_write_b32 a246, v180\n"
    "s_mul_i32 s74, s77, 3\n"
    "s_add_u32 s74, s74, 3\n"
    "s_sub_u32 s75, s74, 5\n"
    "s_cmp_ge_u32 s74, 5\n"
    "s_cselect_b32 s74, s75, s74\n"
    "s_mul_i32 s75, s83, 5\n"
    "s_add_u32 s74, s74, s75\n"
    "s_and_b32 s75, s74, 1\n"
    "s_lshl_b32 s75, s75, 5\n"
    "v_xor_b32 v180, s75, v179\n"
    "s_lshr_b32 s75, s74, 1\n"
    "s_lshl_b32 s75, s75, 9\n"
    "v_add_u32 v180, s75, v180\n"
    "v_accvgpr_write_b32 a247, v180\n"
    "s_mul_i32 s74, s77, 3\n"
    "s_add_u32 s74, s74, 4\n"
    "s_sub_u32 s75, s74, 5\n"
    "s_cmp_ge_u32 s74, 5\n"
    "s_cselect_b32 s74, s75, s74\n"
    "s_mul_i32 s75, s83, 5\n"
    "s_add_u32 s74, s74, s75\n"
    "s_and_b32 s75, s74, 1\n"
    "s_lshl_b32 s75, s75, 5\n"
    "v_xor_b32 v180, s75, v179\n"
    "s_lshr_b32 s75, s74, 1\n"
    "s_lshl_b32 s75, s75, 9\n"
    "v_add_u32 v180, s75, v180\n"
    "v_accvgpr_write_b32 a248, v180\n"
    "v_lshlrev_b32 v179, 4, v174\n"
    "s_add_u32 s74, s82, 0\n"
    "s_and_b32 s74, s74, 3\n"
    "s_lshl_b32 s75, s77, 2\n"
    "s_add_u32 s74, s74, s75\n"
    "s_lshl_b32 s74, s74, 11\n"
    "v_add_u32 v180, s74, v179\n"
    "v_accvgpr_write_b32 a249, v180\n"
    "s_add_u32 s74, s82, 1\n"
    "s_and_b32 s74, s74, 3\n"
    "s_lshl_b32 s75, s77, 2\n"
    "s_add_u32 s74, s74, s75\n"
    "s_lshl_b32 s74, s74, 11\n"
    "v_add_u32 v180, s74, v179\n"
    "v_accvgpr_write_b32 a250, v180\n"
    "s_add_u32 s74, s82, 2\n"
    "s_and_b32 s74, s74, 3\n"
    "s_lshl_b32 s75, s77, 2\n"
    "s_add_u32 s74, s74, s75\n"
    "s_lshl_b32 s74, s74, 11\n"
    "v_add_u32 v180, s74, v179\n"
    "v_accvgpr_write_b32 a251, v180\n"
    "s_add_u32 s74, s82, 3\n"
    "s_and_b32 s74, s74, 3\n"
    "s_lshl_b32 s75, s77, 2\n"
    "s_add_u32 s74, s74, s75\n"
    "s_lshl_b32 s74, s74, 11\n"
    "v_add_u32 v180, s74, v179\n"
    "v_accvgpr_write_b32 a252, v180\n"
    "v_add_u32 v180, 16384, v179\n"
    "v_accvgpr_write_b32 a253, v180\n"
    "s_lshl_b32 s74, s56, 13\n"
    "v_add_u32 v241, s74, v179\n"
    "v_lshlrev_b32 v179, 2, v174\n"
    "s_lshl_b32 s74, s56, 9\n"
    "s_add_u32 s74, s74, 32768\n"
    "v_add_u32 v242, s74, v179\n"
    "v_lshrrev_b32 v181, 2, v174\n"
    "v_and_b32 v181, 7, v181\n"
    "v_lshrrev_b32 v182, 5, v174\n"
    "v_lshrrev_b32 v183, 2, v181\n"
    "v_lshlrev_b32 v183, 1, v183\n"
    "v_xor_b32 v183, v174, v183\n"
    "v_and_b32 v183, 3, v183\n"
    "v_lshlrev_b32 v183, 4, v183\n"
    "v_mul_u32_u24 v179, 1280, v181\n"
    "v_add_u32 v183, v179, v183\n"
    "v_mul_u32_u24 v179, 64, v182\n"
    "v_add_u32 v179, v179, v183\n"
    "v_add_u32 v236, 4096, v179\n"
    "v_mul_u32_u24 v179, 64, v182\n"
    "v_add_u32 v179, v179, v183\n"
    "v_add_u32 v237, 3200, v179\n"
    "v_mul_u32_u24 v179, 9984, v182\n"
    "v_add_u32 v179, v179, v183\n"
    "v_add_u32 v238, 2304, v179\n"
    "v_mul_u32_u24 v179, 64, v182\n"
    "v_add_u32 v179, v179, v183\n"
    "v_add_u32 v239, 11328, v179\n"
    "v_mul_u32_u24 v179, 64, v182\n"
    "v_add_u32 v179, v179, v183\n"
    "v_add_u32 v240, 14528, v179\n"
    "v_min_u32 v175, 5, v174\n"
    "v_cmp_lt_u32 vcc, 2, v175\n"
    "v_add_u32 v176, -3, v175\n"
    "s_nop 0\n"
    "v_cndmask_b32 v176, v175, v176, vcc\n"
    "v_cndmask_b32 v177, 0, 3, vcc\n"
    "v_lshrrev_b32 v179, v177, s30\n"
    "v_add3_u32 v179, v179, v176, 1\n"
    "v_and_b32 v179, 3, v179\n"
    "v_lshlrev_b32 v179, v177, v179\n"
    "v_lshlrev_b32 v180, v177, 3\n"
    "v_not_b32 v180, v180\n"
    "v_and_b32 v180, s30, v180\n"
    "v_or_b32 v179, v179, v180\n"
    "v_lshlrev_b32 v179, 8, v179\n"
    "s_lshl_b32 s74, s56, 2\n"
    "s_add_u32 s74, s74, 4\n"
    "v_mov_b32 v180, s74\n"
    "v_cndmask_b32 v180, v180, 0, vcc\n"
    "v_sub_u32 v178, 2, v176\n"
    "v_add_u32 v178, v178, v180\n"
    "v_lshl_add_u32 v244, v178, 3, v179\n"
    "v_add_u32 v180, v180, v176\n"
    "s_lshl_b32 s75, s30, 8\n"
    "v_lshlrev_b32 v180, 3, v180\n"
    "v_add_u32 v243, s75, v180\n"
    "s_cmp_eq_u32 s56, 0\n"
    "s_cselect_b32 s93, 63, 7\n"
    "s_mov_b32 s72, 0x3e0293ee\n"
    "s_mov_b32 s73, 0x3e0293ee\n"
    "s_waitcnt vmcnt(7)\n"
    "s_barrier\n"
    "ds_read_b128 a[32:35], v152\n"
    "ds_read_b128 a[36:39], v152 offset:1024\n"
    "ds_read_b128 a[40:43], v152 offset:2048\n"
    "ds_read_b128 a[44:47], v152 offset:3072\n"
    "ds_read_b128 a[48:51], v153\n"
    "ds_read_b64_tr_b16 a[0:1], v144 offset:32768\n"
    "ds_read_b64_tr_b16 a[2:3], v144 offset:33792\n"
    "ds_read_b64_tr_b16 a[4:5], v144 offset:32776\n"
    "ds_read_b64_tr_b16 a[6:7], v144 offset:33800\n"
    "; ---- H0\n"
    "s_waitcnt lgkmcnt(2)\n"
    "v_mfma_f32_16x16x32_bf16 v[0:3], a[0:3], a[32:35], 0\n"
    "ds_read_b64_tr_b16 a[8:9], v145 offset:32768\n"
    "v_mfma_f32_16x16x32_bf16 v[4:7], a[0:3], a[36:39], 0\n"
    "ds_read_b64_tr_b16 a[10:11], v145 offset:33792\n"
    "s_add_u32 m0, s61, 49152\n"
    "v_mfma_f32_16x16x32_bf16 v[8:11], a[0:3], a[40:43], 0\n"
    "buffer_load_dwordx4 v154, s[40:43], s57 offen lds\n"
    "v_mfma_f32_16x16x32_bf16 v[12:15], a[0:3], a[44:47], 0\n"
    "buffer_load_dwordx4 v155, s[40:43], s57 offen offset:1024 lds\n"
    "v_mfma_f32_16x16x32_bf16 v[128:131], a[0:3], a[48:51], 0\n"
    "s_waitcnt lgkmcnt(2)\n"
    "ds_read_b64_tr_b16 a[12:13], v145 offset:32776\n"
    "v_mfma_f32_16x16x32_bf16 v[16:19], a[4:7], a[32:35], 0\n"
    "buffer_load_dwordx4 v156, s[40:43], s57 offen offset:2048 lds\n"
    "v_mfma_f32_16x16x32_bf16 v[20:23], a[4:7], a[36:39], 0\n"
    "ds_read_b64_tr_b16 a[14:15], v145 offset:33800\n"
    "v_mfma_f32_16x16x32_bf16 v[24:27], a[4:7], a[40:43], 0\n"
    "buffer_load_dwordx4 v157, s[40:43], s57 offen offset:3072 lds\n"
    "v_mfma_f32_16x16x32_bf16 v[28:31], a[4:7], a[44:47], 0\n"
    "s_add_u32 m0, s62, 18432\n"
    "v_mfma_f32_16x16x32_bf16 v[132:135], a[4:7], a[48:51], 0\n"
    "s_waitcnt lgkmcnt(2)\n"
    "buffer_load_dwordx4 v158, s[36:39], s58 offen lds\n"
    "v_mfma_f32_16x16x32_bf16 v[32:35], a[8:11], a[32:35], 0\n"
    "buffer_load_dwordx4 v159, s[36:39], s58 offen offset:1024 lds\n"
    "v_mfma_f32_16x16x32_bf16 v[36:39], a[8:11], a[36:39], 0\n"
    "s_add_u32 m0, s66, 18432\n"
    "ds_read_b64_tr_b16 a[16:17], v146 offset:32768\n"
    "v_mfma_f32_16x16x32_bf16 v[40:43], a[8:11], a[40:43], 0\n"
    "buffer_load_dword v160, s[36:39], s58 offen lds\n"
    "s_add_u32 s58, s58, 64\n"
    "s_cmp_eq_u32 s58, 1280\n"
    "s_cselect_b32 s58, 0, s58\n"
    "s_lshl_b32 s57, s58, 12\n"
    "s_add_u32 m0, s61, 16384\n"
    "v_mfma_f32_16x16x32_bf16 v[44:47], a[8:11], a[44:47], 0\n"
    "buffer_load_dwordx4 v154, s[40:43], s57 offen lds\n"
    "v_mfma_f32_16x16x32_bf16 v[136:139], a[8:11], a[48:51], 0\n"
    "s_waitcnt lgkmcnt(1)\n"
    "buffer_load_dwordx4 v155, s[40:43], s57 offen offset:1024 lds\n"
    "v_mfma_f32_16x16x32_bf16 v[48:51], a[12:15], a[32:35], 0\n"
    "ds_read_b64_tr_b16 a[18:19], v146 offset:33792\n"
    "ds_read_b64_tr_b16 a[20:21], v146 offset:32776\n"
    "v_mfma_f32_16x16x32_bf16 v[52:55], a[12:15], a[36:39], 0\n"
    "buffer_load_dwordx4 v156, s[40:43], s57 offen offset:2048 lds\n"
    "v_mfma_f32_16x16x32_bf16 v[56:59], a[12:15], a[40:43], 0\n"
    "buffer_load_dwordx4 v157, s[40:43], s57 offen offset:3072 lds\n"
    "s_add_u32 m0, s62, 27648\n"
    "v_mfma_f32_16x16x32_bf16 v[60:63], a[12:15], a[44:47], 0\n"
    "buffer_load_dwordx4 v158, s[36:39], s58 offen lds\n"
    "v_mfma_f32_16x16x32_bf16 v[140:143], a[12:15], a[48:51], 0\n"
    "s_waitcnt lgkmcnt(1)\n"
    "buffer_load_dwordx4 v159, s[36:39], s58 offen offset:1024 lds\n"
    "s_add_u32 m0, s66, 27648\n"
    "s_nop 0\n"
    "buffer_load_dword v160, s[36:39], s58 offen lds\n"
    "s_add_u32 s58, s58, 64\n"
    "s_cmp_eq_u32 s58, 1280\n"
    "s_cselect_b32 s58, 0, s58\n"
    "s_lshl_b32 s57, s58, 12\n"
    "v_mfma_f32_16x16x32_bf16 v[64:67], a[16:19], a[32:35], 0\n"
    "s_waitcnt vmcnt(14)\n"
    "s_barrier\n"
    "ds_read_b64_tr_b16 a[22:23], v146 offset:33800\n"
    "v_mfma_f32_16x16x32_bf16 v[68:71], a[16:19], a[36:39], 0\n"
    "ds_read_b64_tr_b16 a[24:25], v147 offset:32768\n"
    "ds_read_b64_tr_b16 a[26:27], v147 offset:33792\n"
    "v_mfma_f32_16x16x32_bf16 v[72:75], a[16:19], a[40:43], 0\n"
    "ds_read_b128 a[52:55], v152 offset:9216\n"
    "s_add_u32 m0, s61, 81920\n"
    "v_mfma_f32_16x16x32_bf16 v[76:79], a[16:19], a[44:47], 0\n"
    "s_waitcnt lgkmcnt(3)\n"
    "ds_read_b64_tr_b16 a[28:29], v147 offset:32776\n"
    "v_mfma_f32_16x16x32_bf16 v[80:83], a[20:23], a[32:35], 0\n"
    "ds_read_b64_tr_b16 a[30:31], v147 offset:33800\n"
    "ds_read_b128 a[56:59], v152 offset:10240\n"
    "v_mfma_f32_16x16x32_bf16 v[84:87], a[20:23], a[36:39], 0\n"
    "buffer_load_dwordx4 v154, s[40:43], s57 offen lds\n"
    "v_mfma_f32_16x16x32_bf16 v[88:91], a[20:23], a[40:43], 0\n"
    "ds_read_b128 a[60:63], v152 offset:11264\n"
    "ds_read_b128 a[64:67], v152 offset:12288\n"
    "v_mfma_f32_16x16x32_bf16 v[92:95], a[20:23], a[44:47], 0\n"
    "s_waitcnt lgkmcnt(6)\n"
    "ds_read_b128 a[68:71], v153 offset:9216\n"
    "v_mfma_f32_16x16x32_bf16 v[96:99], a[24:27], a[32:35], 0\n"
    "ds_read_b64_tr_b16 a[0:1], v148 offset:32768\n"
    "ds_read_b64_tr_b16 a[2:3], v148 offset:33792\n"
    "v_mfma_f32_16x16x32_bf16 v[100:103], a[24:27], a[36:39], 0\n"
    "buffer_load_dwordx4 v155, s[40:43], s57 offen offset:1024 lds\n"
    "v_mfma_f32_16x16x32_bf16 v[104:107], a[24:27], a[40:43], 0\n"
    "ds_read_b64_tr_b16 a[4:5], v148 offset:32776\n"
    "ds_read_b64_tr_b16 a[6:7], v148 offset:33800\n"
    "v_mfma_f32_16x16x32_bf16 v[108:111], a[24:27], a[44:47], 0\n"
    "s_waitcnt lgkmcnt(8)\n"
    "v_mfma_f32_16x16x32_bf16 v[112:115], a[28:31], a[32:35], 0\n"
    "buffer_load_dwordx4 v156, s[40:43], s57 offen offset:2048 lds\n"
    "v_mfma_f32_16x16x32_bf16 v[116:119], a[28:31], a[36:39], 0\n"
    "v_mfma_f32_16x16x32_bf16 v[120:123], a[28:31], a[40:43], 0\n"
    "v_mfma_f32_16x16x32_bf16 v[124:127], a[28:31], a[44:47], 0\n"
    "s_waitcnt lgkmcnt(2)\n"
    "ds_read_b64_tr_b16 a[8:9], v149 offset:32768\n"
    "; ---- H1\n"
    "v_mfma_f32_16x16x32_bf16 v[0:3], a[0:3], a[52:55], v[0:3]\n"
    "ds_read_b64_tr_b16 a[10:11], v149 offset:33792\n"
    "v_mfma_f32_16x16x32_bf16 v[4:7], a[0:3], a[56:59], v[4:7]\n"
    "buffer_load_dwordx4 v157, s[40:43], s57 offen offset:3072 lds\n"
    "v_mfma_f32_16x16x32_bf16 v[8:11], a[0:3], a[60:63], v[8:11]\n"
    "s_add_u32 m0, s62, 36864\n"
    "v_mfma_f32_16x16x32_bf16 v[12:15], a[0:3], a[64:67], v[12:15]\n"
    "v_mfma_f32_16x16x32_bf16 v[128:131], a[0:3], a[68:71], v[128:131]\n"
    "s_waitcnt lgkmcnt(2)\n"
    "ds_read_b64_tr_b16 a[12:13], v149 offset:32776\n"
    "v_mfma_f32_16x16x32_bf16 v[16:19], a[4:7], a[52:55], v[16:19]\n"
    "ds_read_b64_tr_b16 a[14:15], v149 offset:33800\n"
    "v_mfma_f32_16x16x32_bf16 v[20:23], a[4:7], a[56:59], v[20:23]\n"
    "buffer_load_dwordx4 v158, s[36:39], s58 offen lds\n"
    "v_mfma_f32_16x16x32_bf16 v[24:27], a[4:7], a[60:63], v[24:27]\n"
    "v_mfma_f32_16x16x32_bf16 v[28:31], a[4:7], a[64:67], v[28:31]\n"
    "v_mfma_f32_16x16x32_bf16 v[132:135], a[4:7], a[68:71], v[132:135]\n"
    "s_waitcnt lgkmcnt(2)\n"
    "ds_read_b64_tr_b16 a[16:17], v150 offset:32768\n"
    "v_mfma_f32_16x16x32_bf16 v[32:35], a[8:11], a[52:55], v[32:35]\n"
    "ds_read_b64_tr_b16 a[18:19], v150 offset:33792\n"
    "v_mfma_f32_16x16x32_bf16 v[36:39], a[8:11], a[56:59], v[36:39]\n"
    "buffer_load_dwordx4 v159, s[36:39], s58 offen offset:1024 lds\n"
    "v_mfma_f32_16x16x32_bf16 v[40:43], a[8:11], a[60:63], v[40:43]\n"
    "s_add_u32 m0, s66, 36864\n"
    "v_mfma_f32_16x16x32_bf16 v[44:47], a[8:11], a[64:67], v[44:47]\n"
    "ds_read_b64_tr_b16 a[20:21], v150 offset:32776\n"
    "ds_read_b64_tr_b16 a[22:23], v150 offset:33800\n"
    "v_mfma_f32_16x16x32_bf16 v[136:139], a[8:11], a[68:71], v[136:139]\n"
    "s_waitcnt lgkmcnt(4)\n"
    "v_mfma_f32_16x16x32_bf16 v[48:51], a[12:15], a[52:55], v[48:51]\n"
    "buffer_load_dword v160, s[36:39], s58 offen lds\n"
    "v_mfma_f32_16x16x32_bf16 v[52:55], a[12:15], a[56:59], v[52:55]\n"
    "s_add_u32 s58, s58, 64\n"
    "v_mfma_f32_16x16x32_bf16 v[56:59], a[12:15], a[60:63], v[56:59]\n"
    "s_cmp_eq_u32 s58, 1280\n"
    "s_cselect_b32 s58, 0, s58\n"
    "v_mfma_f32_16x16x32_bf16 v[60:63], a[12:15], a[64:67], v[60:63]\n"
    "s_lshl_b32 s57, s58, 12\n"
    "ds_read_b64_tr_b16 a[24:25], v151 offset:32768\n"
    "v_mfma_f32_16x16x32_bf16 v[140:143], a[12:15], a[68:71], v[140:143]\n"
    "s_waitcnt lgkmcnt(3)\n"
    "ds_read_b64_tr_b16 a[26:27], v151 offset:33792\n"
    "v_mfma_f32_16x16x32_bf16 v[64:67], a[16:19], a[52:55], v[64:67]\n"
    "s_waitcnt vmcnt(14)\n"
    "s_barrier\n"
    "v_mfma_f32_16x16x32_bf16 v[68:71], a[16:19], a[56:59], v[68:71]\n"
    "ds_read_b128 a[32:35], v152 offset:18432\n"
    "s_add_u32 m0, s61, 32768\n"
    "v_mfma_f32_16x16x32_bf16 v[72:75], a[16:19], a[60:63], v[72:75]\n"
    "ds_read_b64_tr_b16 a[28:29], v151 offset:32776\n"
    "ds_read_b64_tr_b16 a[30:31], v151 offset:33800\n"
    "v_mfma_f32_16x16x32_bf16 v[76:79], a[16:19], a[64:67], v[76:79]\n"
    "s_waitcnt lgkmcnt(5)\n"
    "ds_read_b128 a[36:39], v152 offset:19456\n"
    "v_mfma_f32_16x16x32_bf16 v[80:83], a[20:23], a[52:55], v[80:83]\n"
    "buffer_load_dwordx4 v154, s[40:43], s57 offen lds\n"
    "v_mfma_f32_16x16x32_bf16 v[84:87], a[20:23], a[56:59], v[84:87]\n"
    "ds_read_b128 a[40:43], v152 offset:20480\n"
    "ds_read_b128 a[44:47], v152 offset:21504\n"
    "v_mfma_f32_16x16x32_bf16 v[88:91], a[20:23], a[60:63], v[88:91]\n"
    "ds_read_b128 a[48:51], v153 offset:18432\n"
    "ds_read_b64_tr_b16 a[0:1], v144 offset:49152\n"
    "v_mfma_f32_16x16x32_bf16 v[92:95], a[20:23], a[64:67], v[92:95]\n"
    "s_waitcnt lgkmcnt(8)\n"
    "ds_read_b64_tr_b16 a[2:3], v144 offset:50176\n"
    "v_mfma_f32_16x16x32_bf16 v[96:99], a[24:27], a[52:55], v[96:99]\n"
    "buffer_load_dwordx4 v155, s[40:43], s57 offen offset:1024 lds\n"
    "v_mfma_f32_16x16x32_bf16 v[100:103], a[24:27], a[56:59], v[100:103]\n"
    "v_mfma_f32_16x16x32_bf16 v[104:107], a[24:27], a[60:63], v[104:107]\n"
    "ds_read_b64_tr_b16 a[4:5], v144 offset:49160\n"
    "ds_read_b64_tr_b16 a[6:7], v144 offset:50184\n"
    "v_mfma_f32_16x16x32_bf16 v[108:111], a[24:27], a[64:67], v[108:111]\n"
    "s_waitcnt lgkmcnt(8)\n"
    "v_mfma_f32_16x16x32_bf16 v[112:115], a[28:31], a[52:55], v[112:115]\n"
    "buffer_load_dwordx4 v156, s[40:43], s57 offen offset:2048 lds\n"
    "v_mfma_f32_16x16x32_bf16 v[116:119], a[28:31], a[56:59], v[116:119]\n"
    "v_mfma_f32_16x16x32_bf16 v[120:123], a[28:31], a[60:63], v[120:123]\n"
    "v_mfma_f32_16x16x32_bf16 v[124:127], a[28:31], a[64:67], v[124:127]\n"
    "s_waitcnt lgkmcnt(2)\n"
    "ds_read_b64_tr_b16 a[8:9], v145 offset:49152\n"
    "; ---- H2\n"
    "v_mfma_f32_16x16x32_bf16 v[0:3], a[0:3], a[32:35], v[0:3]\n"
    "ds_read_b64_tr_b16 a[10:11], v145 offset:50176\n"
    "v_mfma_f32_16x16x32_bf16 v[4:7], a[0:3], a[36:39], v[4:7]\n"
    "buffer_load_dwordx4 v157, s[40:43], s57 offen offset:3072 lds\n"
    "v_mfma_f32_16x16x32_bf16 v[8:11], a[0:3], a[40:43], v[8:11]\n"
    "s_add_u32 m0, s62, 0\n"
    "v_mfma_f32_16x16x32_bf16 v[12:15], a[0:3], a[44:47], v[12:15]\n"
    "v_mfma_f32_16x16x32_bf16 v[128:131], a[0:3], a[48:51], v[128:131]\n"
    "s_waitcnt lgkmcnt(2)\n"
    "ds_read_b64_tr_b16 a[12:13], v145 offset:49160\n"
    "v_mfma_f32_16x16x32_bf16 v[16:19], a[4:7], a[32:35], v[16:19]\n"
    "ds_read_b64_tr_b16 a[14:15], v145 offset:50184\n"
    "v_mfma_f32_16x16x32_bf16 v[20:23], a[4:7], a[36:39], v[20:23]\n"
    "buffer_load_dwordx4 v158, s[36:39], s58 offen lds\n"
    "v_mfma_f32_16x16x32_bf16 v[24:27], a[4:7], a[40:43], v[24:27]\n"
    "v_mfma_f32_16x16x32_bf16 v[28:31], a[4:7], a[44:47], v[28:31]\n"
    "v_mfma_f32_16x16x32_bf16 v[132:135], a[4:7], a[48:51], v[132:135]\n"
    "s_waitcnt lgkmcnt(2)\n"
    "ds_read_b64_tr_b16 a[16:17], v146 offset:49152\n"
    "v_mfma_f32_16x16x32_bf16 v[32:35], a[8:11], a[32:35], v[32:35]\n"
    "ds_read_b64_tr_b16 a[18:19], v146 offset:50176\n"
    "v_mfma_f32_16x16x32_bf16 v[36:39], a[8:11], a[36:39], v[36:39]\n"
    "buffer_load_dwordx4 v159, s[36:39], s58 offen offset:1024 lds\n"
    "v_mfma_f32_16x16x32_bf16 v[40:43], a[8:11], a[40:43], v[40:43]\n"
    "s_add_u32 m0, s66, 0\n"
    "v_mfma_f32_16x16x32_bf16 v[44:47], a[8:11], a[44:47], v[44:47]\n"
    "ds_read_b64_tr_b16 a[20:21], v146 offset:49160\n"
    "ds_read_b64_tr_b16 a[22:23], v146 offset:50184\n"
    "v_mfma_f32_16x16x32_bf16 v[136:139], a[8:11], a[48:51], v[136:139]\n"
    "s_waitcnt lgkmcnt(4)\n"
    "v_mfma_f32_16x16x32_bf16 v[48:51], a[12:15], a[32:35], v[48:51]\n"
    "buffer_load_dword v160, s[36:39], s58 offen lds\n"
    "v_mfma_f32_16x16x32_bf16 v[52:55], a[12:15], a[36:39], v[52:55]\n"
    "s_add_u32 s58, s58, 64\n"
    "v_mfma_f32_16x16x32_bf16 v[56:59], a[12:15], a[40:43], v[56:59]\n"
    "s_cmp_eq_u32 s58, 1280\n"
    "s_cselect_b32 s58, 0, s58\n"
    "v_mfma_f32_16x16x32_bf16 v[60:63], a[12:15], a[44:47], v[60:63]\n"
    "s_lshl_b32 s57, s58, 12\n"
    "ds_read_b64_tr_b16 a[24:25], v147 offset:49152\n"
    "v_mfma_f32_16x16x32_bf16 v[140:143], a[12:15], a[48:51], v[140:143]\n"
    "s_waitcnt lgkmcnt(3)\n"
    "ds_read_b64_tr_b16 a[26:27], v147 offset:50176\n"
    "v_mfma_f32_16x16x32_bf16 v[64:67], a[16:19], a[32:35], v[64:67]\n"
    "s_waitcnt vmcnt(14)\n"
    "s_barrier\n"
    "v_mfma_f32_16x16x32_bf16 v[68:71], a[16:19], a[36:39], v[68:71]\n"
    "ds_read_b128 a[52:55], v152 offset:27648\n"
    "s_add_u32 m0, s61, 98304\n"
    "v_mfma_f32_16x16x32_bf16 v[72:75], a[16:19], a[40:43], v[72:75]\n"
    "ds_read_b64_tr_b16 a[28:29], v147 offset:49160\n"
    "ds_read_b64_tr_b16 a[30:31], v147 offset:50184\n"
    "v_mfma_f32_16x16x32_bf16 v[76:79], a[16:19], a[44:47], v[76:79]\n"
    "s_waitcnt lgkmcnt(5)\n"
    "ds_read_b128 a[56:59], v152 offset:28672\n"
    "v_mfma_f32_16x16x32_bf16 v[80:83], a[20:23], a[32:35], v[80:83]\n"
    "buffer_load_dwordx4 v154, s[40:43], s57 offen lds\n"
    "v_mfma_f32_16x16x32_bf16 v[84:87], a[20:23], a[36:39], v[84:87]\n"
    "ds_read_b128 a[60:63], v152 offset:29696\n"
    "ds_read_b128 a[64:67], v152 offset:30720\n"
    "v_mfma_f32_16x16x32_bf16 v[88:91], a[20:23], a[40:43], v[88:91]\n"
    "buffer_load_dwordx4 v155, s[40:43], s57 offen offset:1024 lds\n"
    "v_mfma_f32_16x16x32_bf16 v[92:95], a[20:23], a[44:47], v[92:95]\n"
    "s_waitcnt lgkmcnt(6)\n"
    "ds_read_b128 a[68:71], v153 offset:27648\n"
    "v_mfma_f32_16x16x32_bf16 v[96:99], a[24:27], a[32:35], v[96:99]\n"
    "buffer_load_dwordx4 v156, s[40:43], s57 offen offset:2048 lds\n"
    "v_mfma_f32_16x16x32_bf16 v[100:103], a[24:27], a[36:39], v[100:103]\n"
    "ds_read_b64_tr_b16 a[0:1], v144 offset:16384\n"
    "ds_read_b64_tr_b16 a[2:3], v144 offset:17408\n"
    "v_mfma_f32_16x16x32_bf16 v[104:107], a[24:27], a[40:43], v[104:107]\n"
    "buffer_load_dwordx4 v157, s[40:43], s57 offen offset:3072 lds\n"
    "v_mfma_f32_16x16x32_bf16 v[108:111], a[24:27], a[44:47], v[108:111]\n"
    "s_waitcnt lgkmcnt(6)\n"
    "s_add_u32 m0, s62, 9216\n"
    "v_mfma_f32_16x16x32_bf16 v[112:115], a[28:31], a[32:35], v[112:115]\n"
    "buffer_load_dwordx4 v158, s[36:39], s58 offen lds\n"
    "v_mfma_f32_16x16x32_bf16 v[116:119], a[28:31], a[36:39], v[116:119]\n"
    "buffer_load_dwordx4 v159, s[36:39], s58 offen offset:1024 lds\n"
    "v_mfma_f32_16x16x32_bf16 v[120:123], a[28:31], a[40:43], v[120:123]\n"
    "s_add_u32 m0, s66, 9216\n"
    "ds_read_b64_tr_b16 a[4:5], v144 offset:16392\n"
    "v_mfma_f32_16x16x32_bf16 v[124:127], a[28:31], a[44:47], v[124:127]\n"
    "s_waitcnt lgkmcnt(1)\n"
    "buffer_load_dword v160, s[36:39], s58 offen lds\n"
    "; ---- H3\n"
    "v_mfma_f32_16x16x32_bf16 v[0:3], a[0:3], a[52:55], v[0:3]\n"
    "s_add_u32 s58, s58, 64\n"
    "s_cmp_eq_u32 s58, 1280\n"
    "s_cselect_b32 s58, 0, s58\n"
    "s_lshl_b32 s57, s58, 12\n"
    "v_mov_b32 v185, 0\n"
    "v_mov_b32 v186, 366080\n"
    "v_mfma_f32_16x16x32_bf16 v[4:7], a[0:3], a[56:59], v[4:7]\n"
    "ds_read_b64_tr_b16 a[6:7], v144 offset:17416\n"
    "global_load_dword a255, v185, s[24:25]\n"
    "v_mfma_f32_16x16x32_bf16 v[8:11], a[0:3], a[60:63], v[8:11]\n"
    "ds_read_b64_tr_b16 a[8:9], v145 offset:16384\n"
    "ds_read_b64_tr_b16 a[10:11], v145 offset:17408\n"
    "v_mfma_f32_16x16x32_bf16 v[12:15], a[0:3], a[64:67], v[12:15]\n"
    "global_load_dword a255, v185, s[26:27]\n"
    "v_mfma_f32_16x16x32_bf16 v[128:131], a[0:3], a[68:71], v[128:131]\n"
    "s_waitcnt lgkmcnt(2)\n"
    "ds_read_b64_tr_b16 a[12:13], v145 offset:16392\n"
    "v_mfma_f32_16x16x32_bf16 v[16:19], a[4:7], a[52:55], v[16:19]\n"
    "global_load_dword a255, v185, s[22:23]\n"
    "v_mfma_f32_16x16x32_bf16 v[20:23], a[4:7], a[56:59], v[20:23]\n"
    "global_load_dword a255, v186, s[22:23]\n"
    "v_mfma_f32_16x16x32_bf16 v[24:27], a[4:7], a[60:63], v[24:27]\n"
    "s_add_u32 s59, s67, 0\n"
    "s_and_b32 s59, s59, 0x3ffff\n"
    "s_lshl_b32 s60, s77, 12\n"
    "s_add_u32 s59, s59, s60\n"
    "s_add_u32 m0, s61, 0\n"
    "v_mfma_f32_16x16x32_bf16 v[28:31], a[4:7], a[64:67], v[28:31]\n"
    "buffer_load_dwordx4 v250, s[44:47], s59 offen nt lds\n"
    "v_mfma_f32_16x16x32_bf16 v[132:135], a[4:7], a[68:71], v[132:135]\n"
    "s_waitcnt lgkmcnt(1)\n"
    "ds_read_b64_tr_b16 a[14:15], v145 offset:17416\n"
    "v_mfma_f32_16x16x32_bf16 v[32:35], a[8:11], a[52:55], v[32:35]\n"
    "buffer_load_dwordx4 v251, s[44:47], s59 offen offset:1024 nt lds\n"
    "v_mfma_f32_16x16x32_bf16 v[36:39], a[8:11], a[56:59], v[36:39]\n"
    "buffer_load_dwordx4 v252, s[44:47], s59 offen offset:2048 nt lds\n"
    "v_mfma_f32_16x16x32_bf16 v[40:43], a[8:11], a[60:63], v[40:43]\n"
    "ds_read_b64_tr_b16 a[16:17], v146 offset:16384\n"
    "ds_read_b64_tr_b16 a[18:19], v146 offset:17408\n"
    "v_mfma_f32_16x16x32_bf16 v[44:47], a[8:11], a[64:67], v[44:47]\n"
    "buffer_load_dwordx4 v253, s[44:47], s59 offen offset:3072 nt lds\n"
    "v_mfma_f32_16x16x32_bf16 v[136:139], a[8:11], a[68:71], v[136:139]\n"
    "s_waitcnt lgkmcnt(2)\n"
    "s_add_u32 s59, s67, 0\n"
    "s_and_b32 s59, s59, 0x3ffff\n"
    "s_lshl_b32 s60, s77, 12\n"
    "s_add_u32 s59, s59, s60\n"
    "s_add_u32 m0, s61, 65536\n"
    "v_mfma_f32_16x16x32_bf16 v[48:51], a[12:15], a[52:55], v[48:51]\n"
    "buffer_load_dwordx4 v250, s[48:51], s59 offen nt lds\n"
    "v_mfma_f32_16x16x32_bf16 v[52:55], a[12:15], a[56:59], v[52:55]\n"
    "ds_read_b64_tr_b16 a[20:21], v146 offset:16392\n"
    "ds_read_b64_tr_b16 a[22:23], v146 offset:17416\n"
    "v_mfma_f32_16x16x32_bf16 v[56:59], a[12:15], a[60:63], v[56:59]\n"
    "buffer_load_dwordx4 v252, s[48:51], s59 offen offset:1024 nt lds\n"
    "v_mfma_f32_16x16x32_bf16 v[60:63], a[12:15], a[64:67], v[60:63]\n"
    "buffer_load_dwordx4 v250, s[48:51], s59 offen offset:2048 nt lds\n"
    "v_mfma_f32_16x16x32_bf16 v[140:143], a[12:15], a[68:71], v[140:143]\n"
    "s_waitcnt lgkmcnt(2)\n"
    "buffer_load_dwordx4 v252, s[48:51], s59 offen offset:3072 nt lds\n"
    "v_mfma_f32_16x16x32_bf16 v[64:67], a[16:19], a[52:55], v[64:67]\n"
    "s_waitcnt vmcnt(26)\n"
    "s_barrier\n"
    "v_mfma_f32_16x16x32_bf16 v[68:71], a[16:19], a[56:59], v[68:71]\n"
    "ds_read_b64_tr_b16 a[24:25], v147 offset:16384\n"
    "ds_read_b64_tr_b16 a[26:27], v147 offset:17408\n"
    "v_mfma_f32_16x16x32_bf16 v[72:75], a[16:19], a[60:63], v[72:75]\n"
    "ds_read_b128 a[32:35], v152 offset:36864\n"
    "s_add_u32 m0, s61, 49152\n"
    "v_mfma_f32_16x16x32_bf16 v[76:79], a[16:19], a[64:67], v[76:79]\n"
    "s_waitcnt lgkmcnt(3)\n"
    "ds_read_b64_tr_b16 a[28:29], v147 offset:16392\n"
    "v_mfma_f32_16x16x32_bf16 v[80:83], a[20:23], a[52:55], v[80:83]\n"
    "ds_read_b64_tr_b16 a[30:31], v147 offset:17416\n"
    "ds_read_b128 a[36:39], v152 offset:37888\n"
    "v_mfma_f32_16x16x32_bf16 v[84:87], a[20:23], a[56:59], v[84:87]\n"
    "buffer_load_dwordx4 v154, s[40:43], s57 offen lds\n"
    "v_mfma_f32_16x16x32_bf16 v[88:91], a[20:23], a[60:63], v[88:91]\n"
    "ds_read_b128 a[40:43], v152 offset:38912\n"
    "ds_read_b128 a[44:47], v152 offset:39936\n"
    "v_mfma_f32_16x16x32_bf16 v[92:95], a[20:23], a[64:67], v[92:95]\n"
    "s_waitcnt lgkmcnt(6)\n"
    "ds_read_b128 a[48:51], v153 offset:36864\n"
    "v_mfma_f32_16x16x32_bf16 v[96:99], a[24:27], a[52:55], v[96:99]\n"
    "ds_read_b64_tr_b16 a[0:1], v148 offset:16384\n"
    "ds_read_b64_tr_b16 a[2:3], v148 offset:17408\n"
    "v_mfma_f32_16x16x32_bf16 v[100:103], a[24:27], a[56:59], v[100:103]\n"
    "buffer_load_dwordx4 v155, s[40:43], s57 offen offset:1024 lds\n"
    "v_mfma_f32_16x16x32_bf16 v[104:107], a[24:27], a[60:63], v[104:107]\n"
    "ds_read_b64_tr_b16 a[4:5], v148 offset:16392\n"
    "ds_read_b64_tr_b16 a[6:7], v148 offset:17416\n"
    "v_mfma_f32_16x16x32_bf16 v[108:111], a[24:27], a[64:67], v[108:111]\n"
    "s_waitcnt lgkmcnt(8)\n"
    "v_mfma_f32_16x16x32_bf16 v[112:115], a[28:31], a[52:55], v[112:115]\n"
    "buffer_load_dwordx4 v156, s[40:43], s57 offen offset:2048 lds\n"
    "v_mfma_f32_16x16x32_bf16 v[116:119], a[28:31], a[56:59], v[116:119]\n"
    "v_mfma_f32_16x16x32_bf16 v[120:123], a[28:31], a[60:63], v[120:123]\n"
    "v_mfma_f32_16x16x32_bf16 v[124:127], a[28:31], a[64:67], v[124:127]\n"
    "s_waitcnt lgkmcnt(2)\n"
    "ds_read_b64_tr_b16 a[8:9], v149 offset:16384\n"
    "; ---- H4\n"
    "v_mfma_f32_16x16x32_bf16 v[0:3], a[0:3], a[32:35], v[0:3]\n"
    "ds_read_b64_tr_b16 a[10:11], v149 offset:17408\n"
    "v_mfma_f32_16x16x32_bf16 v[4:7], a[0:3], a[36:39], v[4:7]\n"
    "buffer_load_dwordx4 v157, s[40:43], s57 offen offset:3072 lds\n"
    "v_mfma_f32_16x16x32_bf16 v[8:11], a[0:3], a[40:43], v[8:11]\n"
    "s_add_u32 m0, s62, 18432\n"
    "v_mfma_f32_16x16x32_bf16 v[12:15], a[0:3], a[44:47], v[12:15]\n"
    "v_mfma_f32_16x16x32_bf16 v[128:131], a[0:3], a[48:51], v[128:131]\n"
    "s_waitcnt lgkmcnt(2)\n"
    "ds_read_b64_tr_b16 a[12:13], v149 offset:16392\n"
    "v_mfma_f32_16x16x32_bf16 v[16:19], a[4:7], a[32:35], v[16:19]\n"
    "ds_read_b64_tr_b16 a[14:15], v149 offset:17416\n"
    "v_mfma_f32_16x16x32_bf16 v[20:23], a[4:7], a[36:39], v[20:23]\n"
    "buffer_load_dwordx4 v158, s[36:39], s58 offen lds\n"
    "v_mfma_f32_16x16x32_bf16 v[24:27], a[4:7], a[40:43], v[24:27]\n"
    "v_mfma_f32_16x16x32_bf16 v[28:31], a[4:7], a[44:47], v[28:31]\n"
    "v_mfma_f32_16x16x32_bf16 v[132:135], a[4:7], a[48:51], v[132:135]\n"
    "s_waitcnt lgkmcnt(2)\n"
    "ds_read_b64_tr_b16 a[16:17], v150 offset:16384\n"
    "v_mfma_f32_16x16x32_bf16 v[32:35], a[8:11], a[32:35], v[32:35]\n"
    "ds_read_b64_tr_b16 a[18:19], v150 offset:17408\n"
    "v_mfma_f32_16x16x32_bf16 v[36:39], a[8:11], a[36:39], v[36:39]\n"
    "buffer_load_dwordx4 v159, s[36:39], s58 offen offset:1024 lds\n"
    "v_mfma_f32_16x16x32_bf16 v[40:43], a[8:11], a[40:43], v[40:43]\n"
    "s_add_u32 m0, s66, 18432\n"
    "v_mfma_f32_16x16x32_bf16 v[44:47], a[8:11], a[44:47], v[44:47]\n"
    "ds_read_b64_tr_b16 a[20:21], v150 offset:16392\n"
    "ds_read_b64_tr_b16 a[22:23], v150 offset:17416\n"
    "v_mfma_f32_16x16x32_bf16 v[136:139], a[8:11], a[48:51], v[136:139]\n"
    "s_waitcnt lgkmcnt(4)\n"
    "v_mfma_f32_16x16x32_bf16 v[48:51], a[12:15], a[32:35], v[48:51]\n"
    "buffer_load_dword v160, s[36:39], s58 offen lds\n"
    "v_mfma_f32_16x16x32_bf16 v[52:55], a[12:15], a[36:39], v[52:55]\n"
    "s_add_u32 s58, s58, 64\n"
    "v_mfma_f32_16x16x32_bf16 v[56:59], a[12:15], a[40:43], v[56:59]\n"
    "s_cmp_eq_u32 s58, 1280\n"
    "s_cselect_b32 s58, 0, s58\n"
    "v_mfma_f32_16x16x32_bf16 v[60:63], a[12:15], a[44:47], v[60:63]\n"
    "s_lshl_b32 s57, s58, 12\n"
    "ds_read_b64_tr_b16 a[24:25], v151 offset:16384\n"
    "v_mfma_f32_16x16x32_bf16 v[140:143], a[12:15], a[48:51], v[140:143]\n"
    "s_waitcnt lgkmcnt(3)\n"
    "ds_read_b64_tr_b16 a[26:27], v151 offset:17408\n"
    "v_mfma_f32_16x16x32_bf16 v[64:67], a[16:19], a[32:35], v[64:67]\n"
    "s_waitcnt vmcnt(26)\n"
    "s_barrier\n"
    "v_mfma_f32_16x16x32_bf16 v[68:71], a[16:19], a[36:39], v[68:71]\n"
    "ds_read_b128 a[52:55], v152\n"
    "s_add_u32 m0, s61, 16384\n"
    "v_mfma_f32_16x16x32_bf16 v[72:75], a[16:19], a[40:43], v[72:75]\n"
    "ds_read_b64_tr_b16 a[28:29], v151 offset:16392\n"
    "ds_read_b64_tr_b16 a[30:31], v151 offset:17416\n"
    "v_mfma_f32_16x16x32_bf16 v[76:79], a[16:19], a[44:47], v[76:79]\n"
    "s_waitcnt lgkmcnt(5)\n"
    "ds_read_b128 a[56:59], v152 offset:1024\n"
    "v_mfma_f32_16x16x32_bf16 v[80:83], a[20:23], a[32:35], v[80:83]\n"
    "buffer_load_dwordx4 v154, s[40:43], s57 offen lds\n"
    "v_mfma_f32_16x16x32_bf16 v[84:87], a[20:23], a[36:39], v[84:87]\n"
    "ds_read_b128 a[60:63], v152 offset:2048\n"
    "ds_read_b128 a[64:67], v152 offset:3072\n"
    "v_mfma_f32_16x16x32_bf16 v[88:91], a[20:23], a[40:43], v[88:91]\n"
    "ds_read_b128 a[68:71], v153\n"
    "ds_read_b64_tr_b16 a[0:1], v144 offset:32768\n"
    "v_mfma_f32_16x16x32_bf16 v[92:95], a[20:23], a[44:47], v[92:95]\n"
    "s_waitcnt lgkmcnt(8)\n"
    "ds_read_b64_tr_b16 a[2:3], v144 offset:33792\n"
    "v_mfma_f32_16x16x32_bf16 v[96:99], a[24:27], a[32:35], v[96:99]\n"
    "buffer_load_dwordx4 v155, s[40:43], s57 offen offset:1024 lds\n"
    "v_mfma_f32_16x16x32_bf16 v[100:103], a[24:27], a[36:39], v[100:103]\n"
    "v_mfma_f32_16x16x32_bf16 v[104:107], a[24:27], a[40:43], v[104:107]\n"
    "ds_read_b64_tr_b16 a[4:5], v144 offset:32776\n"
    "ds_read_b64_tr_b16 a[6:7], v144 offset:33800\n"
    "v_mfma_f32_16x16x32_bf16 v[108:111], a[24:27], a[44:47], v[108:111]\n"
    "s_waitcnt lgkmcnt(8)\n"
    "v_mfma_f32_16x16x32_bf16 v[112:115], a[28:31], a[32:35], v[112:115]\n"
    "buffer_load_dwordx4 v156, s[40:43], s57 offen offset:2048 lds\n"
    "v_mfma_f32_16x16x32_bf16 v[116:119], a[28:31], a[36:39], v[116:119]\n"
    "v_mfma_f32_16x16x32_bf16 v[120:123], a[28:31], a[40:43], v[120:123]\n"
    "v_mfma_f32_16x16x32_bf16 v[124:127], a[28:31], a[44:47], v[124:127]\n"
    "s_waitcnt lgkmcnt(2)\n"
    "ds_read_b64_tr_b16 a[8:9], v145 offset:32768\n"
    "; ---- H5\n"
    "v_mfma_f32_16x16x32_bf16 v[0:3], a[0:3], a[52:55], v[0:3]\n"
    "ds_read_b64_tr_b16 a[10:11], v145 offset:33792\n"
    "v_mfma_f32_16x16x32_bf16 v[4:7], a[0:3], a[56:59], v[4:7]\n"
    "buffer_load_dwordx4 v157, s[40:43], s57 offen offset:3072 lds\n"
    "v_mfma_f32_16x16x32_bf16 v[8:11], a[0:3], a[60:63], v[8:11]\n"
    "s_add_u32 m0, s62, 27648\n"
    "v_mfma_f32_16x16x32_bf16 v[12:15], a[0:3], a[64:67], v[12:15]\n"
    "v_mfma_f32_16x16x32_bf16 v[128:131], a[0:3], a[68:71], v[128:131]\n"
    "s_waitcnt lgkmcnt(2)\n"
    "ds_read_b64_tr_b16 a[12:13], v145 offset:32776\n"
    "v_mfma_f32_16x16x32_bf16 v[16:19], a[4:7], a[52:55], v[16:19]\n"
    "ds_read_b64_tr_b16 a[14:15], v145 offset:33800\n"
    "v_mfma_f32_16x16x32_bf16 v[20:23], a[4:7], a[56:59], v[20:23]\n"
    "buffer_load_dwordx4 v158, s[36:39], s58 offen lds\n"
    "v_mfma_f32_16x16x32_bf16 v[24:27], a[4:7], a[60:63], v[24:27]\n"
    "v_mfma_f32_16x16x32_bf16 v[28:31], a[4:7], a[64:67], v[28:31]\n"
    "v_mfma_f32_16x16x32_bf16 v[132:135], a[4:7], a[68:71], v[132:135]\n"
    "s_waitcnt lgkmcnt(2)\n"
    "ds_read_b64_tr_b16 a[16:17], v146 offset:32768\n"
    "v_mfma_f32_16x16x32_bf16 v[32:35], a[8:11], a[52:55], v[32:35]\n"
    "ds_read_b64_tr_b16 a[18:19], v146 offset:33792\n"
    "v_mfma_f32_16x16x32_bf16 v[36:39], a[8:11], a[56:59], v[36:39]\n"
    "buffer_load_dwordx4 v159, s[36:39], s58 offen offset:1024 lds\n"
    "v_mfma_f32_16x16x32_bf16 v[40:43], a[8:11], a[60:63], v[40:43]\n"
    "s_add_u32 m0, s66, 27648\n"
    "v_mfma_f32_16x16x32_bf16 v[44:47], a[8:11], a[64:67], v[44:47]\n"
    "ds_read_b64_tr_b16 a[20:21], v146 offset:32776\n"
    "ds_read_b64_tr_b16 a[22:23], v146 offset:33800\n"
    "v_mfma_f32_16x16x32_bf16 v[136:139], a[8:11], a[68:71], v[136:139]\n"
    "s_waitcnt lgkmcnt(4)\n"
    "v_mfma_f32_16x16x32_bf16 v[48:51], a[12:15], a[52:55], v[48:51]\n"
    "buffer_load_dword v160, s[36:39], s58 offen lds\n"
    "v_mfma_f32_16x16x32_bf16 v[52:55], a[12:15], a[56:59], v[52:55]\n"
    "s_add_u32 s58, s58, 64\n"
    "v_mfma_f32_16x16x32_bf16 v[56:59], a[12:15], a[60:63], v[56:59]\n"
    "s_cmp_eq_u32 s58, 1280\n"
    "s_cselect_b32 s58, 0, s58\n"
    "v_mfma_f32_16x16x32_bf16 v[60:63], a[12:15], a[64:67], v[60:63]\n"
    "s_lshl_b32 s57, s58, 12\n"
    "ds_read_b64_tr_b16 a[24:25], v147 offset:32768\n"
    "v_mfma_f32_16x16x32_bf16 v[140:143], a[12:15], a[68:71], v[140:143]\n"
    "s_waitcnt lgkmcnt(3)\n"
    "ds_read_b64_tr_b16 a[26:27], v147 offset:33792\n"
    "v_mfma_f32_16x16x32_bf16 v[64:67], a[16:19], a[52:55], v[64:67]\n"
    "s_waitcnt vmcnt(26)\n"
    "s_barrier\n"
    "v_mfma_f32_16x16x32_bf16 v[68:71], a[16:19], a[56:59], v[68:71]\n"
    "ds_read_b128 a[32:35], v152 offset:9216\n"
    "s_add_u32 m0, s61, 81920\n"
    "v_mfma_f32_16x16x32_bf16 v[72:75], a[16:19], a[60:63], v[72:75]\n"
    "ds_read_b64_tr_b16 a[28:29], v147 offset:32776\n"
    "ds_read_b64_tr_b16 a[30:31], v147 offset:33800\n"
    "v_mfma_f32_16x16x32_bf16 v[76:79], a[16:19], a[64:67], v[76:79]\n"
    "s_waitcnt lgkmcnt(5)\n"
    "ds_read_b128 a[36:39], v152 offset:10240\n"
    "v_mfma_f32_16x16x32_bf16 v[80:83], a[20:23], a[52:55], v[80:83]\n"
    "buffer_load_dwordx4 v154, s[40:43], s57 offen lds\n"
    "v_mfma_f32_16x16x32_bf16 v[84:87], a[20:23], a[56:59], v[84:87]\n"
    "ds_read_b128 a[40:43], v152 offset:11264\n"
    "ds_read_b128 a[44:47], v152 offset:12288\n"
    "v_mfma_f32_16x16x32_bf16 v[88:91], a[20:23], a[60:63], v[88:91]\n"
    "ds_read_b128 a[48:51], v153 offset:9216\n"
    "ds_read_b64_tr_b16 a[0:1], v148 offset:32768\n"
    "v_mfma_f32_16x16x32_bf16 v[92:95], a[20:23], a[64:67], v[92:95]\n"
    "s_waitcnt lgkmcnt(8)\n"
    "ds_read_b64_tr_b16 a[2:3], v148 offset:33792\n"
    "v_mfma_f32_16x16x32_bf16 v[96:99], a[24:27], a[52:55], v[96:99]\n"
    "buffer_load_dwordx4 v155, s[40:43], s57 offen offset:1024 lds\n"
    "v_mfma_f32_16x16x32_bf16 v[100:103], a[24:27], a[56:59], v[100:103]\n"
    "v_mfma_f32_16x16x32_bf16 v[104:107], a[24:27], a[60:63], v[104:107]\n"
    "ds_read_b64_tr_b16 a[4:5], v148 offset:32776\n"
    "ds_read_b64_tr_b16 a[6:7], v148 offset:33800\n"
    "v_mfma_f32_16x16x32_bf16 v[108:111], a[24:27], a[64:67], v[108:111]\n"
    "s_waitcnt lgkmcnt(8)\n"
    "v_mfma_f32_16x16x32_bf16 v[112:115], a[28:31], a[52:55], v[112:115]\n"
    "buffer_load_dwordx4 v156, s[40:43], s57 offen offset:2048 lds\n"
    "v_mfma_f32_16x16x32_bf16 v[116:119], a[28:31], a[56:59], v[116:119]\n"
    "v_mfma_f32_16x16x32_bf16 v[120:123], a[28:31], a[60:63], v[120:123]\n"
    "v_mfma_f32_16x16x32_bf16 v[124:127], a[28:31], a[64:67], v[124:127]\n"
    "s_waitcnt lgkmcnt(2)\n"
    "ds_read_b64_tr_b16 a[8:9], v149 offset:32768\n"
    "; ---- H6\n"
    "v_mfma_f32_16x16x32_bf16 v[0:3], a[0:3], a[32:35], v[0:3]\n"
    "ds_read_b64_tr_b16 a[10:11], v149 offset:33792\n"
    "v_mfma_f32_16x16x32_bf16 v[4:7], a[0:3], a[36:39], v[4:7]\n"
    "buffer_load_dwordx4 v157, s[40:43], s57 offen offset:3072 lds\n"
    "v_mfma_f32_16x16x32_bf16 v[8:11], a[0:3], a[40:43], v[8:11]\n"
    "s_add_u32 m0, s62, 36864\n"
    "v_mfma_f32_16x16x32_bf16 v[12:15], a[0:3], a[44:47], v[12:15]\n"
    "v_mfma_f32_16x16x32_bf16 v[128:131], a[0:3], a[48:51], v[128:131]\n"
    "s_waitcnt lgkmcnt(2)\n"
    "ds_read_b64_tr_b16 a[12:13], v149 offset:32776\n"
    "v_mfma_f32_16x16x32_bf16 v[16:19], a[4:7], a[32:35], v[16:19]\n"
    "ds_read_b64_tr_b16 a[14:15], v149 offset:33800\n"
    "v_mfma_f32_16x16x32_bf16 v[20:23], a[4:7], a[36:39], v[20:23]\n"
    "buffer_load_dwordx4 v158, s[36:39], s58 offen lds\n"
    "v_mfma_f32_16x16x32_bf16 v[24:27], a[4:7], a[40:43], v[24:27]\n"
    "v_mfma_f32_16x16x32_bf16 v[28:31], a[4:7], a[44:47], v[28:31]\n"
    "v_mfma_f32_16x16x32_bf16 v[132:135], a[4:7], a[48:51], v[132:135]\n"
    "s_waitcnt lgkmcnt(2)\n"
    "ds_read_b64_tr_b16 a[16:17], v150 offset:32768\n"
    "v_mfma_f32_16x16x32_bf16 v[32:35], a[8:11], a[32:35], v[32:35]\n"
    "ds_read_b64_tr_b16 a[18:19], v150 offset:33792\n"
    "v_mfma_f32_16x16x32_bf16 v[36:39], a[8:11], a[36:39], v[36:39]\n"
    "buffer_load_dwordx4 v159, s[36:39], s58 offen offset:1024 lds\n"
    "v_mfma_f32_16x16x32_bf16 v[40:43], a[8:11], a[40:43], v[40:43]\n"
    "s_add_u32 m0, s66, 36864\n"
    "v_mfma_f32_16x16x32_bf16 v[44:47], a[8:11], a[44:47], v[44:47]\n"
    "ds_read_b64_tr_b16 a[20:21], v150 offset:32776\n"
    "ds_read_b64_tr_b16 a[22:23], v150 offset:33800\n"
    "v_mfma_f32_16x16x32_bf16 v[136:139], a[8:11], a[48:51], v[136:139]\n"
    "s_waitcnt lgkmcnt(4)\n"
    "v_mfma_f32_16x16x32_bf16 v[48:51], a[12:15], a[32:35], v[48:51]\n"
    "buffer_load_dword v160, s[36:39], s58 offen lds\n"
    "v_mfma_f32_16x16x32_bf16 v[52:55], a[12:15], a[36:39], v[52:55]\n"
    "s_add_u32 s58, s58, 64\n"
    "v_mfma_f32_16x16x32_bf16 v[56:59], a[12:15], a[40:43], v[56:59]\n"
    "s_cmp_eq_u32 s58, 1280\n"
    "s_cselect_b32 s58, 0, s58\n"
    "v_mfma_f32_16x16x32_bf16 v[60:63], a[12:15], a[44:47], v[60:63]\n"
    "s_lshl_b32 s57, s58, 12\n"
    "ds_read_b64_tr_b16 a[24:25], v151 offset:32768\n"
    "v_mfma_f32_16x16x32_bf16 v[140:143], a[12:15], a[48:51], v[140:143]\n"
    "s_waitcnt lgkmcnt(3)\n"
    "ds_read_b64_tr_b16 a[26:27], v151 offset:33792\n"
    "v_mfma_f32_16x16x32_bf16 v[64:67], a[16:19], a[32:35], v[64:67]\n"
    "s_waitcnt vmcnt(14)\n"
    "s_barrier\n"
    "v_mfma_f32_16x16x32_bf16 v[68:71], a[16:19], a[36:39], v[68:71]\n"
    "ds_read_b128 a[52:55], v152 offset:18432\n"
    "s_add_u32 m0, s61, 32768\n"
    "v_mfma_f32_16x16x32_bf16 v[72:75], a[16:19], a[40:43], v[72:75]\n"
    "ds_read_b64_tr_b16 a[28:29], v151 offset:32776\n"
    "ds_read_b64_tr_b16 a[30:31], v151 offset:33800\n"
    "v_mfma_f32_16x16x32_bf16 v[76:79], a[16:19], a[44:47], v[76:79]\n"
    "s_waitcnt lgkmcnt(5)\n"
    "ds_read_b128 a[56:59], v152 offset:19456\n"
    "v_mfma_f32_16x16x32_bf16 v[80:83], a[20:23], a[32:35], v[80:83]\n"
    "buffer_load_dwordx4 v154, s[40:43], s57 offen lds\n"
    "v_mfma_f32_16x16x32_bf16 v[84:87], a[20:23], a[36:39], v[84:87]\n"
    "ds_read_b128 a[60:63], v152 offset:20480\n"
    "ds_read_b128 a[64:67], v152 offset:21504\n"
    "v_mfma_f32_16x16x32_bf16 v[88:91], a[20:23], a[40:43], v[88:91]\n"
    "ds_read_b128 a[68:71], v153 offset:18432\n"
    "ds_read_b64_tr_b16 a[0:1], v144 offset:49152\n"
    "v_mfma_f32_16x16x32_bf16 v[92:95], a[20:23], a[44:47], v[92:95]\n"
    "s_waitcnt lgkmcnt(8)\n"
    "ds_read_b64_tr_b16 a[2:3], v144 offset:50176\n"
    "v_mfma_f32_16x16x32_bf16 v[96:99], a[24:27], a[32:35], v[96:99]\n"
    "buffer_load_dwordx4 v155, s[40:43], s57 offen offset:1024 lds\n"
    "v_mfma_f32_16x16x32_bf16 v[100:103], a[24:27], a[36:39], v[100:103]\n"
    "v_mfma_f32_16x16x32_bf16 v[104:107], a[24:27], a[40:43], v[104:107]\n"
    "ds_read_b64_tr_b16 a[4:5], v144 offset:49160\n"
    "ds_read_b64_tr_b16 a[6:7], v144 offset:50184\n"
    "v_mfma_f32_16x16x32_bf16 v[108:111], a[24:27], a[44:47], v[108:111]\n"
    "s_waitcnt lgkmcnt(8)\n"
    "v_mfma_f32_16x16x32_bf16 v[112:115], a[28:31], a[32:35], v[112:115]\n"
    "buffer_load_dwordx4 v156, s[40:43], s57 offen offset:2048 lds\n"
    "v_mfma_f32_16x16x32_bf16 v[116:119], a[28:31], a[36:39], v[116:119]\n"
    "v_mfma_f32_16x16x32_bf16 v[120:123], a[28:31], a[40:43], v[120:123]\n"
    "v_mfma_f32_16x16x32_bf16 v[124:127], a[28:31], a[44:47], v[124:127]\n"
    "s_waitcnt lgkmcnt(2)\n"
    "ds_read_b64_tr_b16 a[8:9], v145 offset:49152\n"
    "; ---- H7\n"
    "v_mfma_f32_16x16x32_bf16 v[0:3], a[0:3], a[52:55], v[0:3]\n"
    "ds_read_b64_tr_b16 a[10:11], v145 offset:50176\n"
    "v_mfma_f32_16x16x32_bf16 v[4:7], a[0:3], a[56:59], v[4:7]\n"
    "buffer_load_dwordx4 v157, s[40:43], s57 offen offset:3072 lds\n"
    "v_mfma_f32_16x16x32_bf16 v[8:11], a[0:3], a[60:63], v[8:11]\n"
    "s_add_u32 m0, s62, 0\n"
    "v_mfma_f32_16x16x32_bf16 v[12:15], a[0:3], a[64:67], v[12:15]\n"
    "v_mfma_f32_16x16x32_bf16 v[128:131], a[0:3], a[68:71], v[128:131]\n"
    "s_waitcnt lgkmcnt(2)\n"
    "ds_read_b64_tr_b16 a[12:13], v145 offset:49160\n"
    "v_mfma_f32_16x16x32_bf16 v[16:19], a[4:7], a[52:55], v[16:19]\n"
    "ds_read_b64_tr_b16 a[14:15], v145 offset:50184\n"
    "v_mfma_f32_16x16x32_bf16 v[20:23], a[4:7], a[56:59], v[20:23]\n"
    "buffer_load_dwordx4 v158, s[36:39], s58 offen lds\n"
    "v_mfma_f32_16x16x32_bf16 v[24:27], a[4:7], a[60:63], v[24:27]\n"
    "v_mfma_f32_16x16x32_bf16 v[28:31], a[4:7], a[64:67], v[28:31]\n"
    "v_mfma_f32_16x16x32_bf16 v[132:135], a[4:7], a[68:71], v[132:135]\n"
    "s_waitcnt lgkmcnt(2)\n"
    "ds_read_b64_tr_b16 a[16:17], v146 offset:49152\n"
    "v_mfma_f32_16x16x32_bf16 v[32:35], a[8:11], a[52:55], v[32:35]\n"
    "ds_read_b64_tr_b16 a[18:19], v146 offset:50176\n"
    "v_mfma_f32_16x16x32_bf16 v[36:39], a[8:11], a[56:59], v[36:39]\n"
    "buffer_load_dwordx4 v159, s[36:39], s58 offen offset:1024 lds\n"
    "v_mfma_f32_16x16x32_bf16 v[40:43], a[8:11], a[60:63], v[40:43]\n"
    "s_add_u32 m0, s66, 0\n"
    "v_mfma_f32_16x16x32_bf16 v[44:47], a[8:11], a[64:67], v[44:47]\n"
    "ds_read_b64_tr_b16 a[20:21], v146 offset:49160\n"
    "ds_read_b64_tr_b16 a[22:23], v146 offset:50184\n"
    "v_mfma_f32_16x16x32_bf16 v[136:139], a[8:11], a[68:71], v[136:139]\n"
    "s_waitcnt lgkmcnt(4)\n"
    "v_mfma_f32_16x16x32_bf16 v[48:51], a[12:15], a[52:55], v[48:51]\n"
    "buffer_load_dword v160, s[36:39], s58 offen lds\n"
    "v_mfma_f32_16x16x32_bf16 v[52:55], a[12:15], a[56:59], v[52:55]\n"
    "s_add_u32 s58, s58, 64\n"
    "v_mfma_f32_16x16x32_bf16 v[56:59], a[12:15], a[60:63], v[56:59]\n"
    "s_cmp_eq_u32 s58, 1280\n"
    "s_cselect_b32 s58, 0, s58\n"
    "v_mfma_f32_16x16x32_bf16 v[60:63], a[12:15], a[64:67], v[60:63]\n"
    "s_lshl_b32 s57, s58, 12\n"
    "ds_read_b64_tr_b16 a[24:25], v147 offset:49152\n"
    "v_mfma_f32_16x16x32_bf16 v[140:143], a[12:15], a[68:71], v[140:143]\n"
    "s_waitcnt lgkmcnt(3)\n"
    "ds_read_b64_tr_b16 a[26:27], v147 offset:50176\n"
    "v_mfma_f32_16x16x32_bf16 v[64:67], a[16:19], a[52:55], v[64:67]\n"
    "s_waitcnt vmcnt(14)\n"
    "s_barrier\n"
    "v_mfma_f32_16x16x32_bf16 v[68:71], a[16:19], a[56:59], v[68:71]\n"
    "ds_read_b128 a[32:35], v152 offset:27648\n"
    "s_add_u32 m0, s61, 98304\n"
    "v_mfma_f32_16x16x32_bf16 v[72:75], a[16:19], a[60:63], v[72:75]\n"
    "ds_read_b64_tr_b16 a[28:29], v147 offset:49160\n"
    "ds_read_b64_tr_b16 a[30:31], v147 offset:50184\n"
    "v_mfma_f32_16x16x32_bf16 v[76:79], a[16:19], a[64:67], v[76:79]\n"
    "s_waitcnt lgkmcnt(5)\n"
    "ds_read_b128 a[36:39], v152 offset:28672\n"
    "v_mfma_f32_16x16x32_bf16 v[80:83], a[20:23], a[52:55], v[80:83]\n"
    "buffer_load_dwordx4 v154, s[40:43], s57 offen lds\n"
    "v_mfma_f32_16x16x32_bf16 v[84:87], a[20:23], a[56:59], v[84:87]\n"
    "ds_read_b128 a[40:43], v152 offset:29696\n"
    "ds_read_b128 a[44:47], v152 offset:30720\n"
    "v_mfma_f32_16x16x32_bf16 v[88:91], a[20:23], a[60:63], v[88:91]\n"
    "ds_read_b128 a[48:51], v153 offset:27648\n"
    "ds_read_b64_tr_b16 a[0:1], v144 offset:16384\n"
    "v_mfma_f32_16x16x32_bf16 v[92:95], a[20:23], a[64:67], v[92:95]\n"
    "s_waitcnt lgkmcnt(8)\n"
    "ds_read_b64_tr_b16 a[2:3], v144 offset:17408\n"
    "v_mfma_f32_16x16x32_bf16 v[96:99], a[24:27], a[52:55], v[96:99]\n"
    "buffer_load_dwordx4 v155, s[40:43], s57 offen offset:1024 lds\n"
    "v_mfma_f32_16x16x32_bf16 v[100:103], a[24:27], a[56:59], v[100:103]\n"
    "ds_read_b128 a[80:83], v246\n"
    "ds_read_b128 a[84:87], v246 offset:256\n"
    "v_mfma_f32_16x16x32_bf16 v[104:107], a[24:27], a[60:63], v[104:107]\n"
    "ds_read_b128 a[88:91], v247\n"
    "ds_read_b128 a[92:95], v247 offset:256\n"
    "v_mfma_f32_16x16x32_bf16 v[108:111], a[24:27], a[64:67], v[108:111]\n"
    "s_waitcnt lgkmcnt(10)\n"
    "ds_read_b128 a[96:99], v246 offset:4096\n"
    "v_mfma_f32_16x16x32_bf16 v[112:115], a[28:31], a[52:55], v[112:115]\n"
    "ds_read_b64_tr_b16 a[4:5], v144 offset:16392\n"
    "ds_read_b64_tr_b16 a[6:7], v144 offset:17416\n"
    "v_mfma_f32_16x16x32_bf16 v[116:119], a[28:31], a[56:59], v[116:119]\n"
    "buffer_load_dwordx4 v156, s[40:43], s57 offen offset:2048 lds\n"
    "v_mfma_f32_16x16x32_bf16 v[120:123], a[28:31], a[60:63], v[120:123]\n"
    "ds_read_b128 a[100:103], v246 offset:4352\n"
    "ds_read_b128 a[104:107], v247 offset:4096\n"
    "v_mfma_f32_16x16x32_bf16 v[124:127], a[28:31], a[64:67], v[124:127]\n"
    "s_waitcnt lgkmcnt(9)\n"
    "ds_read_b128 a[108:111], v247 offset:4352\n"
    "; ---- H8\n"
    "v_mfma_f32_16x16x32_bf16 v[0:3], a[0:3], a[32:35], v[0:3]\n"
    "ds_read_b64_tr_b16 a[144:145], v248\n"
    "ds_read_b64_tr_b16 a[146:147], v248 offset:4096\n"
    "v_mfma_f32_16x16x32_bf16 v[4:7], a[0:3], a[36:39], v[4:7]\n"
    "ds_read_b64_tr_b16 a[148:149], v249\n"
    "ds_read_b64_tr_b16 a[8:9], v145 offset:16384\n"
    "v_mfma_f32_16x16x32_bf16 v[8:11], a[0:3], a[40:43], v[8:11]\n"
    "ds_read_b64_tr_b16 a[10:11], v145 offset:17408\n"
    "ds_read_b64_tr_b16 a[150:151], v249 offset:4096\n"
    "v_mfma_f32_16x16x32_bf16 v[12:15], a[0:3], a[44:47], v[12:15]\n"
    "buffer_load_dwordx4 v157, s[40:43], s57 offen offset:3072 lds\n"
    "v_mfma_f32_16x16x32_bf16 v[128:131], a[0:3], a[48:51], v[128:131]\n"
    "s_waitcnt lgkmcnt(9)\n"
    "s_add_u32 m0, s62, 9216\n"
    "v_mfma_f32_16x16x32_bf16 v[16:19], a[4:7], a[32:35], v[16:19]\n"
    "ds_read_b64_tr_b16 a[152:153], v248 offset:256\n"
    "ds_read_b64_tr_b16 a[154:155], v248 offset:4352\n"
    "v_mfma_f32_16x16x32_bf16 v[20:23], a[4:7], a[36:39], v[20:23]\n"
    "ds_read_b64_tr_b16 a[156:157], v249 offset:256\n"
    "ds_read_b64_tr_b16 a[158:159], v249 offset:4352\n"
    "v_mfma_f32_16x16x32_bf16 v[24:27], a[4:7], a[40:43], v[24:27]\n"
    "ds_read_b64_tr_b16 a[12:13], v145 offset:16392\n"
    "ds_read_b64_tr_b16 a[14:15], v145 offset:17416\n"
    "v_mfma_f32_16x16x32_bf16 v[28:31], a[4:7], a[44:47], v[28:31]\n"
    "buffer_load_dwordx4 v158, s[36:39], s58 offen lds\n"
    "v_mfma_f32_16x16x32_bf16 v[132:135], a[4:7], a[48:51], v[132:135]\n"
    "s_waitcnt lgkmcnt(7)\n"
    "ds_read_b64_tr_b16 a[160:161], v248 offset:512\n"
    "v_mfma_f32_16x16x32_bf16 v[32:35], a[8:11], a[32:35], v[32:35]\n"
    "ds_read_b64_tr_b16 a[162:163], v248 offset:4608\n"
    "ds_read_b64_tr_b16 a[164:165], v249 offset:512\n"
    "v_mfma_f32_16x16x32_bf16 v[36:39], a[8:11], a[36:39], v[36:39]\n"
    "ds_read_b64_tr_b16 a[166:167], v249 offset:4608\n"
    "ds_read_b64_tr_b16 a[168:169], v248 offset:768\n"
    "v_mfma_f32_16x16x32_bf16 v[40:43], a[8:11], a[40:43], v[40:43]\n"
    "ds_read_b64_tr_b16 a[170:171], v248 offset:4864\n"
    "ds_read_b64_tr_b16 a[16:17], v146 offset:16384\n"
    "v_mfma_f32_16x16x32_bf16 v[44:47], a[8:11], a[44:47], v[44:47]\n"
    "buffer_load_dwordx4 v159, s[36:39], s58 offen offset:1024 lds\n"
    "v_mfma_f32_16x16x32_bf16 v[136:139], a[8:11], a[48:51], v[136:139]\n"
    "s_waitcnt lgkmcnt(7)\n"
    "ds_read_b64_tr_b16 a[172:173], v249 offset:768\n"
    "ds_read_b64_tr_b16 a[174:175], v249 offset:4864\n"
    "v_mfma_f32_16x16x32_bf16 v[48:51], a[12:15], a[32:35], v[48:51]\n"
    "ds_read_b64_tr_b16 a[18:19], v146 offset:17408\n"
    "s_add_u32 m0, s66, 9216\n"
    "v_mfma_f32_16x16x32_bf16 v[52:55], a[12:15], a[36:39], v[52:55]\n"
    "ds_read_b64_tr_b16 a[20:21], v146 offset:16392\n"
    "ds_read_b64_tr_b16 a[22:23], v146 offset:17416\n"
    "v_mfma_f32_16x16x32_bf16 v[56:59], a[12:15], a[40:43], v[56:59]\n"
    "buffer_load_dword v160, s[36:39], s58 offen lds\n"
    "v_mfma_f32_16x16x32_bf16 v[60:63], a[12:15], a[44:47], v[60:63]\n"
    "s_add_u32 s58, s58, 64\n"
    "ds_read_b64_tr_b16 a[24:25], v147 offset:16384\n"
    "v_mfma_f32_16x16x32_bf16 v[140:143], a[12:15], a[48:51], v[140:143]\n"
    "s_waitcnt lgkmcnt(3)\n"
    "s_cmp_eq_u32 s58, 1280\n"
    "s_cselect_b32 s58, 0, s58\n"
    "s_lshl_b32 s57, s58, 12\n"
    "v_mfma_f32_16x16x32_bf16 v[64:67], a[16:19], a[32:35], v[64:67]\n"
    "s_waitcnt vmcnt(14)\n"
    "s_barrier\n"
    "v_mfma_f32_16x16x32_bf16 v[68:71], a[16:19], a[36:39], v[68:71]\n"
    "ds_read_b64_tr_b16 a[26:27], v147 offset:17408\n"
    "ds_read_b128 a[52:55], v152 offset:36864\n"
    "v_mfma_f32_16x16x32_bf16 v[72:75], a[16:19], a[40:43], v[72:75]\n"
    "s_add_u32 m0, s61, 49152\n"
    "ds_read_b64_tr_b16 a[28:29], v147 offset:16392\n"
    "v_mfma_f32_16x16x32_bf16 v[76:79], a[16:19], a[44:47], v[76:79]\n"
    "s_waitcnt lgkmcnt(4)\n"
    "ds_read_b64_tr_b16 a[30:31], v147 offset:17416\n"
    "v_mfma_f32_16x16x32_bf16 v[80:83], a[20:23], a[32:35], v[80:83]\n"
    "ds_read_b128 a[56:59], v152 offset:37888\n"
    "ds_read_b128 a[60:63], v152 offset:38912\n"
    "v_mfma_f32_16x16x32_bf16 v[84:87], a[20:23], a[36:39], v[84:87]\n"
    "buffer_load_dwordx4 v154, s[40:43], s57 offen lds\n"
    "v_mfma_f32_16x16x32_bf16 v[88:91], a[20:23], a[40:43], v[88:91]\n"
    "ds_read_b128 a[64:67], v152 offset:39936\n"
    "ds_read_b128 a[68:71], v153 offset:36864\n"
    "v_mfma_f32_16x16x32_bf16 v[92:95], a[20:23], a[44:47], v[92:95]\n"
    "s_waitcnt lgkmcnt(7)\n"
    "buffer_load_dwordx4 v155, s[40:43], s57 offen offset:1024 lds\n"
    "v_mfma_f32_16x16x32_bf16 v[96:99], a[24:27], a[32:35], v[96:99]\n"
    "ds_read_b64_tr_b16 a[0:1], v148 offset:16384\n"
    "ds_read_b64_tr_b16 a[2:3], v148 offset:17408\n"
    "v_mfma_f32_16x16x32_bf16 v[100:103], a[24:27], a[36:39], v[100:103]\n"
    "buffer_load_dwordx4 v156, s[40:43], s57 offen offset:2048 lds\n"
    "v_mfma_f32_16x16x32_bf16 v[104:107], a[24:27], a[40:43], v[104:107]\n"
    "buffer_load_dwordx4 v157, s[40:43], s57 offen offset:3072 lds\n"
    "v_mfma_f32_16x16x32_bf16 v[108:111], a[24:27], a[44:47], v[108:111]\n"
    "s_waitcnt lgkmcnt(6)\n"
    "s_add_u32 m0, s62, 18432\n"
    "v_mfma_f32_16x16x32_bf16 v[112:115], a[28:31], a[32:35], v[112:115]\n"
    "ds_read_b64_tr_b16 a[4:5], v148 offset:16392\n"
    "ds_read_b64_tr_b16 a[6:7], v148 offset:17416\n"
    "v_mfma_f32_16x16x32_bf16 v[116:119], a[28:31], a[36:39], v[116:119]\n"
    "buffer_load_dwordx4 v158, s[36:39], s58 offen lds\n"
    "v_mfma_f32_16x16x32_bf16 v[120:123], a[28:31], a[40:43], v[120:123]\n"
    "buffer_load_dwordx4 v159, s[36:39], s58 offen offset:1024 lds\n"
    "v_mfma_f32_16x16x32_bf16 v[124:127], a[28:31], a[44:47], v[124:127]\n"
    "s_waitcnt lgkmcnt(2)\n"
    "s_add_u32 m0, s66, 18432\n"
    "; ---- H9\n"
    "v_mfma_f32_16x16x32_bf16 v[0:3], a[0:3], a[52:55], v[0:3]\n"
    "buffer_load_dword v160, s[36:39], s58 offen lds\n"
    "v_mfma_f32_16x16x32_bf16 v[4:7], a[0:3], a[56:59], v[4:7]\n"
    "s_add_u32 s58, s58, 64\n"
    "ds_read_b64_tr_b16 a[8:9], v149 offset:16384\n"
    "v_mfma_f32_16x16x32_bf16 v[8:11], a[0:3], a[60:63], v[8:11]\n"
    "s_cmp_eq_u32 s58, 1280\n"
    "s_cselect_b32 s58, 0, s58\n"
    "v_mfma_f32_16x16x32_bf16 v[12:15], a[0:3], a[64:67], v[12:15]\n"
    "s_lshl_b32 s57, s58, 12\n"
    "s_add_u32 s59, s67, 8192\n"
    "s_and_b32 s59, s59, 0x3ffff\n"
    "s_lshl_b32 s60, s77, 12\n"
    "s_add_u32 s59, s59, s60\n"
    "s_add_u32 m0, s61, 0\n"
    "v_mfma_f32_16x16x32_bf16 v[128:131], a[0:3], a[68:71], v[128:131]\n"
    "s_waitcnt lgkmcnt(1)\n"
    "buffer_load_dwordx4 v250, s[44:47], s59 offen nt lds\n"
    "v_mfma_f32_16x16x32_bf16 v[16:19], a[4:7], a[52:55], v[16:19]\n"
    "ds_read_b64_tr_b16 a[10:11], v149 offset:17408\n"
    "ds_read_b64_tr_b16 a[12:13], v149 offset:16392\n"
    "v_mfma_f32_16x16x32_bf16 v[20:23], a[4:7], a[56:59], v[20:23]\n"
    "buffer_load_dwordx4 v251, s[44:47], s59 offen offset:1024 nt lds\n"
    "v_mfma_f32_16x16x32_bf16 v[24:27], a[4:7], a[60:63], v[24:27]\n"
    "ds_read_b64_tr_b16 a[14:15], v149 offset:17416\n"
    "v_mfma_f32_16x16x32_bf16 v[28:31], a[4:7], a[64:67], v[28:31]\n"
    "buffer_load_dwordx4 v252, s[44:47], s59 offen offset:2048 nt lds\n"
    "v_mfma_f32_16x16x32_bf16 v[132:135], a[4:7], a[68:71], v[132:135]\n"
    "s_waitcnt lgkmcnt(2)\n"
    "ds_read_b64_tr_b16 a[16:17], v150 offset:16384\n"
    "v_mfma_f32_16x16x32_bf16 v[32:35], a[8:11], a[52:55], v[32:35]\n"
    "buffer_load_dwordx4 v253, s[44:47], s59 offen offset:3072 nt lds\n"
    "v_mfma_f32_16x16x32_bf16 v[36:39], a[8:11], a[56:59], v[36:39]\n"
    "s_add_u32 s59, s67, 8192\n"
    "s_and_b32 s59, s59, 0x3ffff\n"
    "v_mfma_f32_16x16x32_bf16 v[40:43], a[8:11], a[60:63], v[40:43]\n"
    "s_lshl_b32 s60, s77, 12\n"
    "s_add_u32 s59, s59, s60\n"
    "s_add_u32 m0, s61, 65536\n"
    "v_mfma_f32_16x16x32_bf16 v[44:47], a[8:11], a[64:67], v[44:47]\n"
    "buffer_load_dwordx4 v250, s[48:51], s59 offen nt lds\n"
    "v_mfma_f32_16x16x32_bf16 v[136:139], a[8:11], a[68:71], v[136:139]\n"
    "s_waitcnt lgkmcnt(1)\n"
    "ds_read_b64_tr_b16 a[18:19], v150 offset:17408\n"
    "v_mfma_f32_16x16x32_bf16 v[48:51], a[12:15], a[52:55], v[48:51]\n"
    "buffer_load_dwordx4 v252, s[48:51], s59 offen offset:1024 nt lds\n"
    "v_mfma_f32_16x16x32_bf16 v[52:55], a[12:15], a[56:59], v[52:55]\n"
    "ds_read_b64_tr_b16 a[20:21], v150 offset:16392\n"
    "ds_read_b64_tr_b16 a[22:23], v150 offset:17416\n"
    "v_mfma_f32_16x16x32_bf16 v[56:59], a[12:15], a[60:63], v[56:59]\n"
    "buffer_load_dwordx4 v250, s[48:51], s59 offen offset:2048 nt lds\n"
    "v_mfma_f32_16x16x32_bf16 v[60:63], a[12:15], a[64:67], v[60:63]\n"
    "buffer_load_dwordx4 v252, s[48:51], s59 offen offset:3072 nt lds\n"
    "v_mfma_f32_16x16x32_bf16 v[140:143], a[12:15], a[68:71], v[140:143]\n"
    "s_waitcnt lgkmcnt(2)\n"
    "ds_read_b64_tr_b16 a[24:25], v151 offset:16384\n"
    "v_mfma_f32_16x16x32_bf16 v[64:67], a[16:19], a[52:55], v[64:67]\n"
    "s_waitcnt vmcnt(22)\n"
    "s_barrier\n"
    "v_mfma_f32_16x16x32_bf16 v[68:71], a[16:19], a[56:59], v[68:71]\n"
    "ds_read_b64_tr_b16 a[26:27], v151 offset:17408\n"
    "ds_read_b128 a[32:35], v152\n"
    "v_mfma_f32_16x16x32_bf16 v[72:75], a[16:19], a[60:63], v[72:75]\n"
    "s_add_u32 m0, s61, 16384\n"
    "ds_read_b64_tr_b16 a[28:29], v151 offset:16392\n"
    "v_mfma_f32_16x16x32_bf16 v[76:79], a[16:19], a[64:67], v[76:79]\n"
    "s_waitcnt lgkmcnt(4)\n"
    "ds_read_b64_tr_b16 a[30:31], v151 offset:17416\n"
    "v_mfma_f32_16x16x32_bf16 v[80:83], a[20:23], a[52:55], v[80:83]\n"
    "ds_read_b128 a[36:39], v152 offset:1024\n"
    "ds_read_b128 a[40:43], v152 offset:2048\n"
    "v_mfma_f32_16x16x32_bf16 v[84:87], a[20:23], a[56:59], v[84:87]\n"
    "buffer_load_dwordx4 v154, s[40:43], s57 offen lds\n"
    "v_mfma_f32_16x16x32_bf16 v[88:91], a[20:23], a[60:63], v[88:91]\n"
    "ds_read_b128 a[44:47], v152 offset:3072\n"
    "ds_read_b128 a[48:51], v153\n"
    "v_mfma_f32_16x16x32_bf16 v[92:95], a[20:23], a[64:67], v[92:95]\n"
    "s_waitcnt lgkmcnt(7)\n"
    "ds_read_b64_tr_b16 a[0:1], v144 offset:32768\n"
    "v_mfma_f32_16x16x32_bf16 v[96:99], a[24:27], a[52:55], v[96:99]\n"
    "ds_read_b64_tr_b16 a[2:3], v144 offset:33792\n"
    "v_mfma_f32_16x16x32_bf16 v[100:103], a[24:27], a[56:59], v[100:103]\n"
    "buffer_load_dwordx4 v155, s[40:43], s57 offen offset:1024 lds\n"
    "v_mfma_f32_16x16x32_bf16 v[104:107], a[24:27], a[60:63], v[104:107]\n"
    "ds_read_b64_tr_b16 a[4:5], v144 offset:32776\n"
    "ds_read_b64_tr_b16 a[6:7], v144 offset:33800\n"
    "v_mfma_f32_16x16x32_bf16 v[108:111], a[24:27], a[64:67], v[108:111]\n"
    "s_waitcnt lgkmcnt(8)\n"
    "v_mfma_f32_16x16x32_bf16 v[112:115], a[28:31], a[52:55], v[112:115]\n"
    "buffer_load_dwordx4 v156, s[40:43], s57 offen offset:2048 lds\n"
    "v_mfma_f32_16x16x32_bf16 v[116:119], a[28:31], a[56:59], v[116:119]\n"
    "v_mfma_f32_16x16x32_bf16 v[120:123], a[28:31], a[60:63], v[120:123]\n"
    "v_mfma_f32_16x16x32_bf16 v[124:127], a[28:31], a[64:67], v[124:127]\n"
    "s_waitcnt lgkmcnt(2)\n"
    "ds_read_b64_tr_b16 a[8:9], v145 offset:32768\n"
    "; ---- H10\n"
    "v_mfma_f32_16x16x32_bf16 v[0:3], a[0:3], a[32:35], v[0:3]\n"
    "ds_read_b64_tr_b16 a[10:11], v145 offset:33792\n"
    "v_mfma_f32_16x16x32_bf16 v[4:7], a[0:3], a[36:39], v[4:7]\n"
    "buffer_load_dwordx4 v157, s[40:43], s57 offen offset:3072 lds\n"
    "v_mfma_f32_16x16x32_bf16 v[8:11], a[0:3], a[40:43], v[8:11]\n"
    "s_add_u32 m0, s62, 27648\n"
    "v_mfma_f32_16x16x32_bf16 v[12:15], a[0:3], a[44:47], v[12:15]\n"
    "v_mfma_f32_16x16x32_bf16 v[128:131], a[0:3], a[48:51], v[128:131]\n"
    "s_waitcnt lgkmcnt(2)\n"
    "ds_read_b64_tr_b16 a[12:13], v145 offset:32776\n"
    "v_mfma_f32_16x16x32_bf16 v[16:19], a[4:7], a[32:35], v[16:19]\n"
    "ds_read_b64_tr_b16 a[14:15], v145 offset:33800\n"
    "v_mfma_f32_16x16x32_bf16 v[20:23], a[4:7], a[36:39], v[20:23]\n"
    "buffer_load_dwordx4 v158, s[36:39], s58 offen lds\n"
    "v_mfma_f32_16x16x32_bf16 v[24:27], a[4:7], a[40:43], v[24:27]\n"
    "v_mfma_f32_16x16x32_bf16 v[28:31], a[4:7], a[44:47], v[28:31]\n"
    "v_mfma_f32_16x16x32_bf16 v[132:135], a[4:7], a[48:51], v[132:135]\n"
    "s_waitcnt lgkmcnt(2)\n"
    "ds_read_b64_tr_b16 a[16:17], v146 offset:32768\n"
    "v_mfma_f32_16x16x32_bf16 v[32:35], a[8:11], a[32:35], v[32:35]\n"
    "ds_read_b64_tr_b16 a[18:19], v146 offset:33792\n"
    "v_mfma_f32_16x16x32_bf16 v[36:39], a[8:11], a[36:39], v[36:39]\n"
    "buffer_load_dwordx4 v159, s[36:39], s58 offen offset:1024 lds\n"
    "v_mfma_f32_16x16x32_bf16 v[40:43], a[8:11], a[40:43], v[40:43]\n"
    "s_add_u32 m0, s66, 27648\n"
    "v_mfma_f32_16x16x32_bf16 v[44:47], a[8:11], a[44:47], v[44:47]\n"
    "ds_read_b64_tr_b16 a[20:21], v146 offset:32776\n"
    "ds_read_b64_tr_b16 a[22:23], v146 offset:33800\n"
    "v_mfma_f32_16x16x32_bf16 v[136:139], a[8:11], a[48:51], v[136:139]\n"
    "s_waitcnt lgkmcnt(4)\n"
    "v_mfma_f32_16x16x32_bf16 v[48:51], a[12:15], a[32:35], v[48:51]\n"
    "buffer_load_dword v160, s[36:39], s58 offen lds\n"
    "v_mfma_f32_16x16x32_bf16 v[52:55], a[12:15], a[36:39], v[52:55]\n"
    "s_add_u32 s58, s58, 64\n"
    "v_mfma_f32_16x16x32_bf16 v[56:59], a[12:15], a[40:43], v[56:59]\n"
    "s_cmp_eq_u32 s58, 1280\n"
    "s_cselect_b32 s58, 0, s58\n"
    "v_mfma_f32_16x16x32_bf16 v[60:63], a[12:15], a[44:47], v[60:63]\n"
    "s_lshl_b32 s57, s58, 12\n"
    "ds_read_b64_tr_b16 a[24:25], v147 offset:32768\n"
    "v_mfma_f32_16x16x32_bf16 v[140:143], a[12:15], a[48:51], v[140:143]\n"
    "s_waitcnt lgkmcnt(3)\n"
    "ds_read_b64_tr_b16 a[26:27], v147 offset:33792\n"
    "v_mfma_f32_16x16x32_bf16 v[64:67], a[16:19], a[32:35], v[64:67]\n"
    "s_waitcnt vmcnt(22)\n"
    "s_barrier\n"
    "v_mfma_f32_16x16x32_bf16 v[68:71], a[16:19], a[36:39], v[68:71]\n"
    "ds_read_b128 a[52:55], v152 offset:9216\n"
    "s_add_u32 m0, s61, 81920\n"
    "v_mfma_f32_16x16x32_bf16 v[72:75], a[16:19], a[40:43], v[72:75]\n"
    "ds_read_b64_tr_b16 a[28:29], v147 offset:32776\n"
    "ds_read_b64_tr_b16 a[30:31], v147 offset:33800\n"
    "v_mfma_f32_16x16x32_bf16 v[76:79], a[16:19], a[44:47], v[76:79]\n"
    "s_waitcnt lgkmcnt(5)\n"
    "ds_read_b128 a[56:59], v152 offset:10240\n"
    "v_mfma_f32_16x16x32_bf16 v[80:83], a[20:23], a[32:35], v[80:83]\n"
    "buffer_load_dwordx4 v154, s[40:43], s57 offen lds\n"
    "v_mfma_f32_16x16x32_bf16 v[84:87], a[20:23], a[36:39], v[84:87]\n"
    "ds_read_b128 a[60:63], v152 offset:11264\n"
    "ds_read_b128 a[64:67], v152 offset:12288\n"
    "v_mfma_f32_16x16x32_bf16 v[88:91], a[20:23], a[40:43], v[88:91]\n"
    "ds_read_b128 a[68:71], v153 offset:9216\n"
    "ds_read_b64_tr_b16 a[0:1], v148 offset:32768\n"
    "v_mfma_f32_16x16x32_bf16 v[92:95], a[20:23], a[44:47], v[92:95]\n"
    "s_waitcnt lgkmcnt(8)\n"
    "ds_read_b64_tr_b16 a[2:3], v148 offset:33792\n"
    "v_mfma_f32_16x16x32_bf16 v[96:99], a[24:27], a[32:35], v[96:99]\n"
    "buffer_load_dwordx4 v155, s[40:43], s57 offen offset:1024 lds\n"
    "v_mfma_f32_16x16x32_bf16 v[100:103], a[24:27], a[36:39], v[100:103]\n"
    "v_mfma_f32_16x16x32_bf16 v[104:107], a[24:27], a[40:43], v[104:107]\n"
    "ds_read_b64_tr_b16 a[4:5], v148 offset:32776\n"
    "ds_read_b64_tr_b16 a[6:7], v148 offset:33800\n"
    "v_mfma_f32_16x16x32_bf16 v[108:111], a[24:27], a[44:47], v[108:111]\n"
    "s_waitcnt lgkmcnt(8)\n"
    "v_mfma_f32_16x16x32_bf16 v[112:115], a[28:31], a[32:35], v[112:115]\n"
    "buffer_load_dwordx4 v156, s[40:43], s57 offen offset:2048 lds\n"
    "v_mfma_f32_16x16x32_bf16 v[116:119], a[28:31], a[36:39], v[116:119]\n"
    "v_mfma_f32_16x16x32_bf16 v[120:123], a[28:31], a[40:43], v[120:123]\n"
    "v_mfma_f32_16x16x32_bf16 v[124:127], a[28:31], a[44:47], v[124:127]\n"
    "s_waitcnt lgkmcnt(2)\n"
    "ds_read_b64_tr_b16 a[8:9], v149 offset:32768\n"
    "; ---- H11\n"
    "v_mfma_f32_16x16x32_bf16 v[0:3], a[0:3], a[52:55], v[0:3]\n"
    "ds_read_b64_tr_b16 a[10:11], v149 offset:33792\n"
    "v_mfma_f32_16x16x32_bf16 v[4:7], a[0:3], a[56:59], v[4:7]\n"
    "buffer_load_dwordx4 v157, s[40:43], s57 offen offset:3072 lds\n"
    "v_mfma_f32_16x16x32_bf16 v[8:11], a[0:3], a[60:63], v[8:11]\n"
    "s_add_u32 m0, s62, 36864\n"
    "v_mfma_f32_16x16x32_bf16 v[12:15], a[0:3], a[64:67], v[12:15]\n"
    "v_mfma_f32_16x16x32_bf16 v[128:131], a[0:3], a[68:71], v[128:131]\n"
    "s_waitcnt lgkmcnt(2)\n"
    "ds_read_b64_tr_b16 a[12:13], v149 offset:32776\n"
    "v_mfma_f32_16x16x32_bf16 v[16:19], a[4:7], a[52:55], v[16:19]\n"
    "ds_read_b64_tr_b16 a[14:15], v149 offset:33800\n"
    "v_mfma_f32_16x16x32_bf16 v[20:23], a[4:7], a[56:59], v[20:23]\n"
    "buffer_load_dwordx4 v158, s[36:39], s58 offen lds\n"
    "v_mfma_f32_16x16x32_bf16 v[24:27], a[4:7], a[60:63], v[24:27]\n"
    "v_mfma_f32_16x16x32_bf16 v[28:31], a[4:7], a[64:67], v[28:31]\n"
    "v_mfma_f32_16x16x32_bf16 v[132:135], a[4:7], a[68:71], v[132:135]\n"
    "s_waitcnt lgkmcnt(2)\n"
    "ds_read_b64_tr_b16 a[16:17], v150 offset:32768\n"
    "v_mfma_f32_16x16x32_bf16 v[32:35], a[8:11], a[52:55], v[32:35]\n"
    "ds_read_b64_tr_b16 a[18:19], v150 offset:33792\n"
    "v_mfma_f32_16x16x32_bf16 v[36:39], a[8:11], a[56:59], v[36:39]\n"
    "buffer_load_dwordx4 v159, s[36:39], s58 offen offset:1024 lds\n"
    "v_mfma_f32_16x16x32_bf16 v[40:43], a[8:11], a[60:63], v[40:43]\n"
    "s_add_u32 m0, s66, 36864\n"
    "v_mfma_f32_16x16x32_bf16 v[44:47], a[8:11], a[64:67], v[44:47]\n"
    "ds_read_b64_tr_b16 a[20:21], v150 offset:32776\n"
    "ds_read_b64_tr_b16 a[22:23], v150 offset:33800\n"
    "v_mfma_f32_16x16x32_bf16 v[136:139], a[8:11], a[68:71], v[136:139]\n"
    "s_waitcnt lgkmcnt(4)\n"
    "v_mfma_f32_16x16x32_bf16 v[48:51], a[12:15], a[52:55], v[48:51]\n"
    "buffer_load_dword v160, s[36:39], s58 offen lds\n"
    "v_mfma_f32_16x16x32_bf16 v[52:55], a[12:15], a[56:59], v[52:55]\n"
    "s_add_u32 s58, s58, 64\n"
    "v_mfma_f32_16x16x32_bf16 v[56:59], a[12:15], a[60:63], v[56:59]\n"
    "s_cmp_eq_u32 s58, 1280\n"
    "s_cselect_b32 s58, 0, s58\n"
    "v_mfma_f32_16x16x32_bf16 v[60:63], a[12:15], a[64:67], v[60:63]\n"
    "s_lshl_b32 s57, s58, 12\n"
    "ds_read_b64_tr_b16 a[24:25], v151 offset:32768\n"
    "v_mfma_f32_16x16x32_bf16 v[140:143], a[12:15], a[68:71], v[140:143]\n"
    "s_waitcnt lgkmcnt(3)\n"
    "ds_read_b64_tr_b16 a[26:27], v151 offset:33792\n"
    "v_mfma_f32_16x16x32_bf16 v[64:67], a[16:19], a[52:55], v[64:67]\n"
    "s_waitcnt vmcnt(22)\n"
    "s_barrier\n"
    "v_mfma_f32_16x16x32_bf16 v[68:71], a[16:19], a[56:59], v[68:71]\n"
    "ds_read_b128 a[32:35], v152 offset:18432\n"
    "s_add_u32 m0, s61, 32768\n"
    "v_mfma_f32_16x16x32_bf16 v[72:75], a[16:19], a[60:63], v[72:75]\n"
    "ds_read_b64_tr_b16 a[28:29], v151 offset:32776\n"
    "ds_read_b64_tr_b16 a[30:31], v151 offset:33800\n"
    "v_mfma_f32_16x16x32_bf16 v[76:79], a[16:19], a[64:67], v[76:79]\n"
    "s_waitcnt lgkmcnt(5)\n"
    "ds_read_b128 a[36:39], v152 offset:19456\n"
    "v_mfma_f32_16x16x32_bf16 v[80:83], a[20:23], a[52:55], v[80:83]\n"
    "buffer_load_dwordx4 v154, s[40:43], s57 offen lds\n"
    "v_mfma_f32_16x16x32_bf16 v[84:87], a[20:23], a[56:59], v[84:87]\n"
    "ds_read_b128 a[40:43], v152 offset:20480\n"
    "ds_read_b128 a[44:47], v152 offset:21504\n"
    "v_mfma_f32_16x16x32_bf16 v[88:91], a[20:23], a[60:63], v[88:91]\n"
    "ds_read_b128 a[48:51], v153 offset:18432\n"
    "ds_read_b64_tr_b16 a[0:1], v144 offset:49152\n"
    "v_mfma_f32_16x16x32_bf16 v[92:95], a[20:23], a[64:67], v[92:95]\n"
    "s_waitcnt lgkmcnt(8)\n"
    "ds_read_b64_tr_b16 a[2:3], v144 offset:50176\n"
    "v_mfma_f32_16x16x32_bf16 v[96:99], a[24:27], a[52:55], v[96:99]\n"
    "buffer_load_dwordx4 v155, s[40:43], s57 offen offset:1024 lds\n"
    "v_mfma_f32_16x16x32_bf16 v[100:103], a[24:27], a[56:59], v[100:103]\n"
    "v_mfma_f32_16x16x32_bf16 v[104:107], a[24:27], a[60:63], v[104:107]\n"
    "ds_read_b64_tr_b16 a[4:5], v144 offset:49160\n"
    "ds_read_b64_tr_b16 a[6:7], v144 offset:50184\n"
    "v_mfma_f32_16x16x32_bf16 v[108:111], a[24:27], a[64:67], v[108:111]\n"
    "s_waitcnt lgkmcnt(8)\n"
    "v_mfma_f32_16x16x32_bf16 v[112:115], a[28:31], a[52:55], v[112:115]\n"
    "buffer_load_dwordx4 v156, s[40:43], s57 offen offset:2048 lds\n"
    "v_mfma_f32_16x16x32_bf16 v[116:119], a[28:31], a[56:59], v[116:119]\n"
    "v_mfma_f32_16x16x32_bf16 v[120:123], a[28:31], a[60:63], v[120:123]\n"
    "v_mfma_f32_16x16x32_bf16 v[124:127], a[28:31], a[64:67], v[124:127]\n"
    "s_waitcnt lgkmcnt(2)\n"
    "ds_read_b64_tr_b16 a[8:9], v145 offset:49152\n"
    "; ---- H12\n"
    "v_mfma_f32_16x16x32_bf16 v[0:3], a[0:3], a[32:35], v[0:3]\n"
    "ds_read_b64_tr_b16 a[10:11], v145 offset:50176\n"
    "v_mfma_f32_16x16x32_bf16 v[4:7], a[0:3], a[36:39], v[4:7]\n"
    "buffer_load_dwordx4 v157, s[40:43], s57 offen offset:3072 lds\n"
    "v_mfma_f32_16x16x32_bf16 v[8:11], a[0:3], a[40:43], v[8:11]\n"
    "s_add_u32 m0, s62, 0\n"
    "v_mfma_f32_16x16x32_bf16 v[12:15], a[0:3], a[44:47], v[12:15]\n"
    "v_mfma_f32_16x16x32_bf16 v[128:131], a[0:3], a[48:51], v[128:131]\n"
    "s_waitcnt lgkmcnt(2)\n"
    "ds_read_b64_tr_b16 a[12:13], v145 offset:49160\n"
    "v_mfma_f32_16x16x32_bf16 v[16:19], a[4:7], a[32:35], v[16:19]\n"
    "ds_read_b64_tr_b16 a[14:15], v145 offset:50184\n"
    "v_mfma_f32_16x16x32_bf16 v[20:23], a[4:7], a[36:39], v[20:23]\n"
    "buffer_load_dwordx4 v158, s[36:39], s58 offen lds\n"
    "v_mfma_f32_16x16x32_bf16 v[24:27], a[4:7], a[40:43], v[24:27]\n"
    "v_mfma_f32_16x16x32_bf16 v[28:31], a[4:7], a[44:47], v[28:31]\n"
    "v_mfma_f32_16x16x32_bf16 v[132:135], a[4:7], a[48:51], v[132:135]\n"
    "s_waitcnt lgkmcnt(2)\n"
    "ds_read_b64_tr_b16 a[16:17], v146 offset:49152\n"
    "v_mfma_f32_16x16x32_bf16 v[32:35], a[8:11], a[32:35], v[32:35]\n"
    "ds_read_b64_tr_b16 a[18:19], v146 offset:50176\n"
    "v_mfma_f32_16x16x32_bf16 v[36:39], a[8:11], a[36:39], v[36:39]\n"
    "buffer_load_dwordx4 v159, s[36:39], s58 offen offset:1024 lds\n"
    "v_mfma_f32_16x16x32_bf16 v[40:43], a[8:11], a[40:43], v[40:43]\n"
    "s_add_u32 m0, s66, 0\n"
    "v_mfma_f32_16x16x32_bf16 v[44:47], a[8:11], a[44:47], v[44:47]\n"
    "ds_read_b64_tr_b16 a[20:21], v146 offset:49160\n"
    "ds_read_b64_tr_b16 a[22:23], v146 offset:50184\n"
    "v_mfma_f32_16x16x32_bf16 v[136:139], a[8:11], a[48:51], v[136:139]\n"
    "s_waitcnt lgkmcnt(4)\n"
    "v_mfma_f32_16x16x32_bf16 v[48:51], a[12:15], a[32:35], v[48:51]\n"
    "buffer_load_dword v160, s[36:39], s58 offen lds\n"
    "v_mfma_f32_16x16x32_bf16 v[52:55], a[12:15], a[36:39], v[52:55]\n"
    "s_add_u32 s58, s58, 64\n"
    "v_mfma_f32_16x16x32_bf16 v[56:59], a[12:15], a[40:43], v[56:59]\n"
    "s_cmp_eq_u32 s58, 1280\n"
    "s_cselect_b32 s58, 0, s58\n"
    "v_mfma_f32_16x16x32_bf16 v[60:63], a[12:15], a[44:47], v[60:63]\n"
    "s_lshl_b32 s57, s58, 12\n"
    "ds_read_b64_tr_b16 a[24:25], v147 offset:49152\n"
    "v_mfma_f32_16x16x32_bf16 v[140:143], a[12:15], a[48:51], v[140:143]\n"
    "s_waitcnt lgkmcnt(3)\n"
    "ds_read_b64_tr_b16 a[26:27], v147 offset:50176\n"
    "v_mfma_f32_16x16x32_bf16 v[64:67], a[16:19], a[32:35], v[64:67]\n"
    "s_waitcnt vmcnt(14)\n"
    "s_barrier\n"
    "v_mfma_f32_16x16x32_bf16 v[68:71], a[16:19], a[36:39], v[68:71]\n"
    "ds_read_b128 a[52:55], v152 offset:27648\n"
    "s_add_u32 m0, s61, 98304\n"
    "v_mfma_f32_16x16x32_bf16 v[72:75], a[16:19], a[40:43], v[72:75]\n"
    "ds_read_b64_tr_b16 a[28:29], v147 offset:49160\n"
    "ds_read_b64_tr_b16 a[30:31], v147 offset:50184\n"
    "v_mfma_f32_16x16x32_bf16 v[76:79], a[16:19], a[44:47], v[76:79]\n"
    "s_waitcnt lgkmcnt(5)\n"
    "ds_read_b128 a[56:59], v152 offset:28672\n"
    "v_mfma_f32_16x16x32_bf16 v[80:83], a[20:23], a[32:35], v[80:83]\n"
    "buffer_load_dwordx4 v154, s[40:43], s57 offen lds\n"
    "v_mfma_f32_16x16x32_bf16 v[84:87], a[20:23], a[36:39], v[84:87]\n"
    "ds_read_b128 a[60:63], v152 offset:29696\n"
    "ds_read_b128 a[64:67], v152 offset:30720\n"
    "v_mfma_f32_16x16x32_bf16 v[88:91], a[20:23], a[40:43], v[88:91]\n"
    "ds_read_b128 a[68:71], v153 offset:27648\n"
    "ds_read_b64_tr_b16 a[0:1], v144 offset:16384\n"
    "v_mfma_f32_16x16x32_bf16 v[92:95], a[20:23], a[44:47], v[92:95]\n"
    "s_waitcnt lgkmcnt(8)\n"
    "ds_read_b64_tr_b16 a[2:3], v144 offset:17408\n"
    "v_mfma_f32_16x16x32_bf16 v[96:99], a[24:27], a[32:35], v[96:99]\n"
    "buffer_load_dwordx4 v155, s[40:43], s57 offen offset:1024 lds\n"
    "v_mfma_f32_16x16x32_bf16 v[100:103], a[24:27], a[36:39], v[100:103]\n"
    "v_mfma_f32_16x16x32_bf16 v[104:107], a[24:27], a[40:43], v[104:107]\n"
    "ds_read_b64_tr_b16 a[4:5], v144 offset:16392\n"
    "ds_read_b64_tr_b16 a[6:7], v144 offset:17416\n"
    "v_mfma_f32_16x16x32_bf16 v[108:111], a[24:27], a[44:47], v[108:111]\n"
    "s_waitcnt lgkmcnt(8)\n"
    "v_mfma_f32_16x16x32_bf16 v[112:115], a[28:31], a[32:35], v[112:115]\n"
    "buffer_load_dwordx4 v156, s[40:43], s57 offen offset:2048 lds\n"
    "v_mfma_f32_16x16x32_bf16 v[116:119], a[28:31], a[36:39], v[116:119]\n"
    "v_mfma_f32_16x16x32_bf16 v[120:123], a[28:31], a[40:43], v[120:123]\n"
    "v_mfma_f32_16x16x32_bf16 v[124:127], a[28:31], a[44:47], v[124:127]\n"
    "s_waitcnt lgkmcnt(2)\n"
    "ds_read_b64_tr_b16 a[8:9], v145 offset:16384\n"
    "; ---- H13\n"
    "v_mfma_f32_16x16x32_bf16 v[0:3], a[0:3], a[52:55], v[0:3]\n"
    "ds_read_b64_tr_b16 a[10:11], v145 offset:17408\n"
    "v_mfma_f32_16x16x32_bf16 v[4:7], a[0:3], a[56:59], v[4:7]\n"
    "buffer_load_dwordx4 v157, s[40:43], s57 offen offset:3072 lds\n"
    "v_mfma_f32_16x16x32_bf16 v[8:11], a[0:3], a[60:63], v[8:11]\n"
    "s_add_u32 m0, s62, 9216\n"
    "v_mfma_f32_16x16x32_bf16 v[12:15], a[0:3], a[64:67], v[12:15]\n"
    "v_mfma_f32_16x16x32_bf16 v[128:131], a[0:3], a[68:71], v[128:131]\n"
    "s_waitcnt lgkmcnt(2)\n"
    "ds_read_b64_tr_b16 a[12:13], v145 offset:16392\n"
    "v_mfma_f32_16x16x32_bf16 v[16:19], a[4:7], a[52:55], v[16:19]\n"
    "ds_read_b64_tr_b16 a[14:15], v145 offset:17416\n"
    "v_mfma_f32_16x16x32_bf16 v[20:23], a[4:7], a[56:59], v[20:23]\n"
    "buffer_load_dwordx4 v158, s[36:39], s58 offen lds\n"
    "v_mfma_f32_16x16x32_bf16 v[24:27], a[4:7], a[60:63], v[24:27]\n"
    "v_mfma_f32_16x16x32_bf16 v[28:31], a[4:7], a[64:67], v[28:31]\n"
    "v_mfma_f32_16x16x32_bf16 v[132:135], a[4:7], a[68:71], v[132:135]\n"
    "s_waitcnt lgkmcnt(2)\n"
    "ds_read_b64_tr_b16 a[16:17], v146 offset:16384\n"
    "v_mfma_f32_16x16x32_bf16 v[32:35], a[8:11], a[52:55], v[32:35]\n"
    "ds_read_b64_tr_b16 a[18:19], v146 offset:17408\n"
    "v_mfma_f32_16x16x32_bf16 v[36:39], a[8:11], a[56:59], v[36:39]\n"
    "buffer_load_dwordx4 v159, s[36:39], s58 offen offset:1024 lds\n"
    "v_mfma_f32_16x16x32_bf16 v[40:43], a[8:11], a[60:63], v[40:43]\n"
    "s_add_u32 m0, s66, 9216\n"
    "v_mfma_f32_16x16x32_bf16 v[44:47], a[8:11], a[64:67], v[44:47]\n"
    "ds_read_b64_tr_b16 a[20:21], v146 offset:16392\n"
    "ds_read_b64_tr_b16 a[22:23], v146 offset:17416\n"
    "v_mfma_f32_16x16x32_bf16 v[136:139], a[8:11], a[68:71], v[136:139]\n"
    "s_waitcnt lgkmcnt(4)\n"
    "v_mfma_f32_16x16x32_bf16 v[48:51], a[12:15], a[52:55], v[48:51]\n"
    "buffer_load_dword v160, s[36:39], s58 offen lds\n"
    "v_mfma_f32_16x16x32_bf16 v[52:55], a[12:15], a[56:59], v[52:55]\n"
    "s_add_u32 s58, s58, 64\n"
    "v_mfma_f32_16x16x32_bf16 v[56:59], a[12:15], a[60:63], v[56:59]\n"
    "s_cmp_eq_u32 s58, 1280\n"
    "s_cselect_b32 s58, 0, s58\n"
    "v_mfma_f32_16x16x32_bf16 v[60:63], a[12:15], a[64:67], v[60:63]\n"
    "s_lshl_b32 s57, s58, 12\n"
    "ds_read_b64_tr_b16 a[24:25], v147 offset:16384\n"
    "v_mfma_f32_16x16x32_bf16 v[140:143], a[12:15], a[68:71], v[140:143]\n"
    "s_waitcnt lgkmcnt(3)\n"
    "ds_read_b64_tr_b16 a[26:27], v147 offset:17408\n"
    "v_mfma_f32_16x16x32_bf16 v[64:67], a[16:19], a[52:55], v[64:67]\n"
    "s_waitcnt vmcnt(14)\n"
    "s_barrier\n"
    "v_mfma_f32_16x16x32_bf16 v[68:71], a[16:19], a[56:59], v[68:71]\n"
    "ds_read_b128 a[32:35], v152 offset:36864\n"
    "s_add_u32 m0, s61, 49152\n"
    "v_mfma_f32_16x16x32_bf16 v[72:75], a[16:19], a[60:63], v[72:75]\n"
    "ds_read_b64_tr_b16 a[28:29], v147 offset:16392\n"
    "ds_read_b64_tr_b16 a[30:31], v147 offset:17416\n"
    "v_mfma_f32_16x16x32_bf16 v[76:79], a[16:19], a[64:67], v[76:79]\n"
    "s_waitcnt lgkmcnt(5)\n"
    "ds_read_b128 a[36:39], v152 offset:37888\n"
    "v_mfma_f32_16x16x32_bf16 v[80:83], a[20:23], a[52:55], v[80:83]\n"
    "buffer_load_dwordx4 v154, s[40:43], s57 offen lds\n"
    "v_mfma_f32_16x16x32_bf16 v[84:87], a[20:23], a[56:59], v[84:87]\n"
    "ds_read_b128 a[40:43], v152 offset:38912\n"
    "ds_read_b128 a[44:47], v152 offset:39936\n"
    "v_mfma_f32_16x16x32_bf16 v[88:91], a[20:23], a[60:63], v[88:91]\n"
    "ds_read_b128 a[48:51], v153 offset:36864\n"
    "ds_read_b64_tr_b16 a[0:1], v148 offset:16384\n"
    "v_mfma_f32_16x16x32_bf16 v[92:95], a[20:23], a[64:67], v[92:95]\n"
    "s_waitcnt lgkmcnt(8)\n"
    "ds_read_b64_tr_b16 a[2:3], v148 offset:17408\n"
    "v_mfma_f32_16x16x32_bf16 v[96:99], a[24:27], a[52:55], v[96:99]\n"
    "buffer_load_dwordx4 v155, s[40:43], s57 offen offset:1024 lds\n"
    "v_mfma_f32_16x16x32_bf16 v[100:103], a[24:27], a[56:59], v[100:103]\n"
    "ds_read_b128 a[112:115], v246\n"
    "ds_read_b128 a[116:119], v246 offset:256\n"
    "v_mfma_f32_16x16x32_bf16 v[104:107], a[24:27], a[60:63], v[104:107]\n"
    "ds_read_b128 a[120:123], v247\n"
    "ds_read_b128 a[124:127], v247 offset:256\n"
    "v_mfma_f32_16x16x32_bf16 v[108:111], a[24:27], a[64:67], v[108:111]\n"
    "s_waitcnt lgkmcnt(10)\n"
    "ds_read_b128 a[128:131], v246 offset:4096\n"
    "v_mfma_f32_16x16x32_bf16 v[112:115], a[28:31], a[52:55], v[112:115]\n"
    "ds_read_b64_tr_b16 a[4:5], v148 offset:16392\n"
    "ds_read_b64_tr_b16 a[6:7], v148 offset:17416\n"
    "v_mfma_f32_16x16x32_bf16 v[116:119], a[28:31], a[56:59], v[116:119]\n"
    "buffer_load_dwordx4 v156, s[40:43], s57 offen offset:2048 lds\n"
    "v_mfma_f32_16x16x32_bf16 v[120:123], a[28:31], a[60:63], v[120:123]\n"
    "ds_read_b128 a[132:135], v246 offset:4352\n"
    "ds_read_b128 a[136:139], v247 offset:4096\n"
    "v_mfma_f32_16x16x32_bf16 v[124:127], a[28:31], a[64:67], v[124:127]\n"
    "s_waitcnt lgkmcnt(9)\n"
    "ds_read_b128 a[140:143], v247 offset:4352\n"
    "; ---- H14\n"
    "v_mfma_f32_16x16x32_bf16 v[0:3], a[0:3], a[32:35], v[0:3]\n"
    "ds_read_b64_tr_b16 a[176:177], v248\n"
    "ds_read_b64_tr_b16 a[178:179], v248 offset:4096\n"
    "v_mfma_f32_16x16x32_bf16 v[4:7], a[0:3], a[36:39], v[4:7]\n"
    "ds_read_b64_tr_b16 a[180:181], v249\n"
    "ds_read_b64_tr_b16 a[8:9], v149 offset:16384\n"
    "v_mfma_f32_16x16x32_bf16 v[8:11], a[0:3], a[40:43], v[8:11]\n"
    "ds_read_b64_tr_b16 a[10:11], v149 offset:17408\n"
    "ds_read_b64_tr_b16 a[182:183], v249 offset:4096\n"
    "v_mfma_f32_16x16x32_bf16 v[12:15], a[0:3], a[44:47], v[12:15]\n"
    "buffer_load_dwordx4 v157, s[40:43], s57 offen offset:3072 lds\n"
    "v_mfma_f32_16x16x32_bf16 v[128:131], a[0:3], a[48:51], v[128:131]\n"
    "s_waitcnt lgkmcnt(9)\n"
    "s_add_u32 m0, s62, 18432\n"
    "v_mfma_f32_16x16x32_bf16 v[16:19], a[4:7], a[32:35], v[16:19]\n"
    "ds_read_b64_tr_b16 a[184:185], v248 offset:256\n"
    "ds_read_b64_tr_b16 a[186:187], v248 offset:4352\n"
    "v_mfma_f32_16x16x32_bf16 v[20:23], a[4:7], a[36:39], v[20:23]\n"
    "ds_read_b64_tr_b16 a[188:189], v249 offset:256\n"
    "ds_read_b64_tr_b16 a[190:191], v249 offset:4352\n"
    "v_mfma_f32_16x16x32_bf16 v[24:27], a[4:7], a[40:43], v[24:27]\n"
    "ds_read_b64_tr_b16 a[12:13], v149 offset:16392\n"
    "ds_read_b64_tr_b16 a[14:15], v149 offset:17416\n"
    "v_mfma_f32_16x16x32_bf16 v[28:31], a[4:7], a[44:47], v[28:31]\n"
    "buffer_load_dwordx4 v158, s[36:39], s58 offen lds\n"
    "v_mfma_f32_16x16x32_bf16 v[132:135], a[4:7], a[48:51], v[132:135]\n"
    "s_waitcnt lgkmcnt(7)\n"
    "ds_read_b64_tr_b16 a[192:193], v248 offset:512\n"
    "v_mfma_f32_16x16x32_bf16 v[32:35], a[8:11], a[32:35], v[32:35]\n"
    "ds_read_b64_tr_b16 a[194:195], v248 offset:4608\n"
    "ds_read_b64_tr_b16 a[196:197], v249 offset:512\n"
    "v_mfma_f32_16x16x32_bf16 v[36:39], a[8:11], a[36:39], v[36:39]\n"
    "ds_read_b64_tr_b16 a[198:199], v249 offset:4608\n"
    "ds_read_b64_tr_b16 a[200:201], v248 offset:768\n"
    "v_mfma_f32_16x16x32_bf16 v[40:43], a[8:11], a[40:43], v[40:43]\n"
    "ds_read_b64_tr_b16 a[202:203], v248 offset:4864\n"
    "ds_read_b64_tr_b16 a[16:17], v150 offset:16384\n"
    "v_mfma_f32_16x16x32_bf16 v[44:47], a[8:11], a[44:47], v[44:47]\n"
    "buffer_load_dwordx4 v159, s[36:39], s58 offen offset:1024 lds\n"
    "v_mfma_f32_16x16x32_bf16 v[136:139], a[8:11], a[48:51], v[136:139]\n"
    "s_waitcnt lgkmcnt(7)\n"
    "ds_read_b64_tr_b16 a[204:205], v249 offset:768\n"
    "ds_read_b64_tr_b16 a[206:207], v249 offset:4864\n"
    "v_mfma_f32_16x16x32_bf16 v[48:51], a[12:15], a[32:35], v[48:51]\n"
    "ds_read_b64_tr_b16 a[18:19], v150 offset:17408\n"
    "s_add_u32 m0, s66, 18432\n"
    "v_mfma_f32_16x16x32_bf16 v[52:55], a[12:15], a[36:39], v[52:55]\n"
    "ds_read_b64_tr_b16 a[20:21], v150 offset:16392\n"
    "ds_read_b64_tr_b16 a[22:23], v150 offset:17416\n"
    "v_mfma_f32_16x16x32_bf16 v[56:59], a[12:15], a[40:43], v[56:59]\n"
    "buffer_load_dword v160, s[36:39], s58 offen lds\n"
    "v_mfma_f32_16x16x32_bf16 v[60:63], a[12:15], a[44:47], v[60:63]\n"
    "s_add_u32 s58, s58, 64\n"
    "ds_read_b64_tr_b16 a[24:25], v151 offset:16384\n"
    "v_mfma_f32_16x16x32_bf16 v[140:143], a[12:15], a[48:51], v[140:143]\n"
    "s_waitcnt lgkmcnt(3)\n"
    "s_cmp_eq_u32 s58, 1280\n"
    "s_cselect_b32 s58, 0, s58\n"
    "s_lshl_b32 s57, s58, 12\n"
    "v_mfma_f32_16x16x32_bf16 v[64:67], a[16:19], a[32:35], v[64:67]\n"
    "s_waitcnt vmcnt(14)\n"
    "s_barrier\n"
    "v_mfma_f32_16x16x32_bf16 v[68:71], a[16:19], a[36:39], v[68:71]\n"
    "ds_read_b64_tr_b16 a[26:27], v151 offset:17408\n"
    "ds_read_b128 a[52:55], v152\n"
    "v_mfma_f32_16x16x32_bf16 v[72:75], a[16:19], a[40:43], v[72:75]\n"
    "s_add_u32 m0, s61, 16384\n"
    "ds_read_b64_tr_b16 a[28:29], v151 offset:16392\n"
    "v_mfma_f32_16x16x32_bf16 v[76:79], a[16:19], a[44:47], v[76:79]\n"
    "s_waitcnt lgkmcnt(4)\n"
    "ds_read_b64_tr_b16 a[30:31], v151 offset:17416\n"
    "v_mfma_f32_16x16x32_bf16 v[80:83], a[20:23], a[32:35], v[80:83]\n"
    "ds_read_b128 a[56:59], v152 offset:1024\n"
    "ds_read_b128 a[60:63], v152 offset:2048\n"
    "v_mfma_f32_16x16x32_bf16 v[84:87], a[20:23], a[36:39], v[84:87]\n"
    "buffer_load_dwordx4 v154, s[40:43], s57 offen lds\n"
    "v_mfma_f32_16x16x32_bf16 v[88:91], a[20:23], a[40:43], v[88:91]\n"
    "ds_read_b128 a[64:67], v152 offset:3072\n"
    "ds_read_b128 a[68:71], v153\n"
    "v_mfma_f32_16x16x32_bf16 v[92:95], a[20:23], a[44:47], v[92:95]\n"
    "s_waitcnt lgkmcnt(7)\n"
    "buffer_load_dwordx4 v155, s[40:43], s57 offen offset:1024 lds\n"
    "v_mfma_f32_16x16x32_bf16 v[96:99], a[24:27], a[32:35], v[96:99]\n"
    "ds_read_b64_tr_b16 a[0:1], v144 offset:32768\n"
    "ds_read_b64_tr_b16 a[2:3], v144 offset:33792\n"
    "v_mfma_f32_16x16x32_bf16 v[100:103], a[24:27], a[36:39], v[100:103]\n"
    "buffer_load_dwordx4 v156, s[40:43], s57 offen offset:2048 lds\n"
    "v_mfma_f32_16x16x32_bf16 v[104:107], a[24:27], a[40:43], v[104:107]\n"
    "buffer_load_dwordx4 v157, s[40:43], s57 offen offset:3072 lds\n"
    "v_mfma_f32_16x16x32_bf16 v[108:111], a[24:27], a[44:47], v[108:111]\n"
    "s_waitcnt lgkmcnt(6)\n"
    "s_add_u32 m0, s62, 27648\n"
    "v_mfma_f32_16x16x32_bf16 v[112:115], a[28:31], a[32:35], v[112:115]\n"
    "buffer_load_dwordx4 v158, s[36:39], s58 offen lds\n"
    "v_mfma_f32_16x16x32_bf16 v[116:119], a[28:31], a[36:39], v[116:119]\n"
    "ds_read_b64_tr_b16 a[4:5], v144 offset:32776\n"
    "ds_read_b64_tr_b16 a[6:7], v144 offset:33800\n"
    "v_mfma_f32_16x16x32_bf16 v[120:123], a[28:31], a[40:43], v[120:123]\n"
    "buffer_load_dwordx4 v159, s[36:39], s58 offen offset:1024 lds\n"
    "v_mfma_f32_16x16x32_bf16 v[124:127], a[28:31], a[44:47], v[124:127]\n"
    "s_waitcnt lgkmcnt(2)\n"
    "s_add_u32 m0, s66, 27648\n"
    "; ---- H15\n"
    "v_mfma_f32_16x16x32_bf16 v[0:3], a[0:3], a[52:55], v[0:3]\n"
    "buffer_load_dword v160, s[36:39], s58 offen lds\n"
    "v_mfma_f32_16x16x32_bf16 v[4:7], a[0:3], a[56:59], v[4:7]\n"
    "s_add_u32 s58, s58, 64\n"
    "ds_read_b64_tr_b16 a[8:9], v145 offset:32768\n"
    "v_mfma_f32_16x16x32_bf16 v[8:11], a[0:3], a[60:63], v[8:11]\n"
    "s_cmp_eq_u32 s58, 1280\n"
    "s_cselect_b32 s58, 0, s58\n"
    "s_lshl_b32 s57, s58, 12\n"
    "v_mfma_f32_16x16x32_bf16 v[12:15], a[0:3], a[64:67], v[12:15]\n"
    "global_load_dword v245, v244, s[80:81] offset:4 sc1\n"
    "v_mfma_f32_16x16x32_bf16 v[128:131], a[0:3], a[68:71], v[128:131]\n"
    "s_waitcnt lgkmcnt(1)\n"
    "ds_read_b64_tr_b16 a[10:11], v145 offset:33792\n"
    "v_mfma_f32_16x16x32_bf16 v[16:19], a[4:7], a[52:55], v[16:19]\n"
    "s_add_u32 s59, s67, 32768\n"
    "s_and_b32 s59, s59, 0x3ffff\n"
    "s_lshl_b32 s60, s77, 12\n"
    "s_add_u32 s59, s59, s60\n"
    "s_add_u32 m0, s61, 0\n"
    "v_mfma_f32_16x16x32_bf16 v[20:23], a[4:7], a[56:59], v[20:23]\n"
    "buffer_load_dwordx4 v250, s[44:47], s59 offen nt lds\n"
    "v_mfma_f32_16x16x32_bf16 v[24:27], a[4:7], a[60:63], v[24:27]\n"
    "ds_read_b64_tr_b16 a[12:13], v145 offset:32776\n"
    "ds_read_b64_tr_b16 a[14:15], v145 offset:33800\n"
    "v_mfma_f32_16x16x32_bf16 v[28:31], a[4:7], a[64:67], v[28:31]\n"
    "buffer_load_dwordx4 v251, s[44:47], s59 offen offset:1024 nt lds\n"
    "v_mfma_f32_16x16x32_bf16 v[132:135], a[4:7], a[68:71], v[132:135]\n"
    "s_waitcnt lgkmcnt(2)\n"
    "ds_read_b64_tr_b16 a[16:17], v146 offset:32768\n"
    "v_mfma_f32_16x16x32_bf16 v[32:35], a[8:11], a[52:55], v[32:35]\n"
    "buffer_load_dwordx4 v252, s[44:47], s59 offen offset:2048 nt lds\n"
    "v_mfma_f32_16x16x32_bf16 v[36:39], a[8:11], a[56:59], v[36:39]\n"
    "buffer_load_dwordx4 v253, s[44:47], s59 offen offset:3072 nt lds\n"
    "v_mfma_f32_16x16x32_bf16 v[40:43], a[8:11], a[60:63], v[40:43]\n"
    "s_add_u32 s59, s67, 32768\n"
    "s_and_b32 s59, s59, 0x3ffff\n"
    "v_mfma_f32_16x16x32_bf16 v[44:47], a[8:11], a[64:67], v[44:47]\n"
    "s_lshl_b32 s60, s77, 12\n"
    "s_add_u32 s59, s59, s60\n"
    "s_add_u32 m0, s61, 65536\n"
    "v_mfma_f32_16x16x32_bf16 v[136:139], a[8:11], a[68:71], v[136:139]\n"
    "s_waitcnt lgkmcnt(1)\n"
    "buffer_load_dwordx4 v250, s[48:51], s59 offen nt lds\n"
    "v_mfma_f32_16x16x32_bf16 v[48:51], a[12:15], a[52:55], v[48:51]\n"
    "ds_read_b64_tr_b16 a[18:19], v146 offset:33792\n"
    "ds_read_b64_tr_b16 a[20:21], v146 offset:32776\n"
    "v_mfma_f32_16x16x32_bf16 v[52:55], a[12:15], a[56:59], v[52:55]\n"
    "buffer_load_dwordx4 v252, s[48:51], s59 offen offset:1024 nt lds\n"
    "v_mfma_f32_16x16x32_bf16 v[56:59], a[12:15], a[60:63], v[56:59]\n"
    "ds_read_b64_tr_b16 a[22:23], v146 offset:33800\n"
    "ds_read_b64_tr_b16 a[24:25], v147 offset:32768\n"
    "v_mfma_f32_16x16x32_bf16 v[60:63], a[12:15], a[64:67], v[60:63]\n"
    "buffer_load_dwordx4 v250, s[48:51], s59 offen offset:2048 nt lds\n"
    "v_mfma_f32_16x16x32_bf16 v[140:143], a[12:15], a[68:71], v[140:143]\n"
    "s_waitcnt lgkmcnt(3)\n"
    "buffer_load_dwordx4 v252, s[48:51], s59 offen offset:3072 nt lds\n"
    "v_mfma_f32_16x16x32_bf16 v[64:67], a[16:19], a[52:55], v[64:67]\n"
    "s_waitcnt vmcnt(23)\n"
    "s_barrier\n"
    "v_mfma_f32_16x16x32_bf16 v[68:71], a[16:19], a[56:59], v[68:71]\n"
    "ds_read_b64_tr_b16 a[26:27], v147 offset:33792\n"
    "ds_read_b128 a[32:35], v152 offset:9216\n"
    "v_mfma_f32_16x16x32_bf16 v[72:75], a[16:19], a[60:63], v[72:75]\n"
    "s_add_u32 m0, s61, 81920\n"
    "ds_read_b64_tr_b16 a[28:29], v147 offset:32776\n"
    "v_mfma_f32_16x16x32_bf16 v[76:79], a[16:19], a[64:67], v[76:79]\n"
    "s_waitcnt lgkmcnt(4)\n"
    "ds_read_b64_tr_b16 a[30:31], v147 offset:33800\n"
    "v_mfma_f32_16x16x32_bf16 v[80:83], a[20:23], a[52:55], v[80:83]\n"
    "ds_read_b128 a[36:39], v152 offset:10240\n"
    "ds_read_b128 a[40:43], v152 offset:11264\n"
    "v_mfma_f32_16x16x32_bf16 v[84:87], a[20:23], a[56:59], v[84:87]\n"
    "buffer_load_dwordx4 v154, s[40:43], s57 offen lds\n"
    "v_mfma_f32_16x16x32_bf16 v[88:91], a[20:23], a[60:63], v[88:91]\n"
    "ds_read_b128 a[44:47], v152 offset:12288\n"
    "ds_read_b128 a[48:51], v153 offset:9216\n"
    "v_mfma_f32_16x16x32_bf16 v[92:95], a[20:23], a[64:67], v[92:95]\n"
    "s_waitcnt lgkmcnt(7)\n"
    "ds_read_b64_tr_b16 a[0:1], v148 offset:32768\n"
    "v_mfma_f32_16x16x32_bf16 v[96:99], a[24:27], a[52:55], v[96:99]\n"
    "ds_read_b64_tr_b16 a[2:3], v148 offset:33792\n"
    "v_mfma_f32_16x16x32_bf16 v[100:103], a[24:27], a[56:59], v[100:103]\n"
    "buffer_load_dwordx4 v155, s[40:43], s57 offen offset:1024 lds\n"
    "v_mfma_f32_16x16x32_bf16 v[104:107], a[24:27], a[60:63], v[104:107]\n"
    "ds_read_b64_tr_b16 a[4:5], v148 offset:32776\n"
    "ds_read_b64_tr_b16 a[6:7], v148 offset:33800\n"
    "v_mfma_f32_16x16x32_bf16 v[108:111], a[24:27], a[64:67], v[108:111]\n"
    "s_waitcnt lgkmcnt(8)\n"
    "v_mfma_f32_16x16x32_bf16 v[112:115], a[28:31], a[52:55], v[112:115]\n"
    "buffer_load_dwordx4 v156, s[40:43], s57 offen offset:2048 lds\n"
    "v_mfma_f32_16x16x32_bf16 v[116:119], a[28:31], a[56:59], v[116:119]\n"
    "v_mfma_f32_16x16x32_bf16 v[120:123], a[28:31], a[60:63], v[120:123]\n"
    "v_mfma_f32_16x16x32_bf16 v[124:127], a[28:31], a[64:67], v[124:127]\n"
    "s_waitcnt lgkmcnt(2)\n"
    "ds_read_b64_tr_b16 a[8:9], v149 offset:32768\n"
    "; ---- H16\n"
    "v_mfma_f32_16x16x32_bf16 v[0:3], a[0:3], a[32:35], v[0:3]\n"
    "ds_read_b64_tr_b16 a[10:11], v149 offset:33792\n"
    "v_mfma_f32_16x16x32_bf16 v[4:7], a[0:3], a[36:39], v[4:7]\n"
    "buffer_load_dwordx4 v157, s[40:43], s57 offen offset:3072 lds\n"
    "v_mfma_f32_16x16x32_bf16 v[8:11], a[0:3], a[40:43], v[8:11]\n"
    "s_add_u32 m0, s62, 36864\n"
    "v_mfma_f32_16x16x32_bf16 v[12:15], a[0:3], a[44:47], v[12:15]\n"
    "v_mfma_f32_16x16x32_bf16 v[128:131], a[0:3], a[48:51], v[128:131]\n"
    "s_waitcnt lgkmcnt(2)\n"
    "ds_read_b64_tr_b16 a[12:13], v149 offset:32776\n"
    "v_mfma_f32_16x16x32_bf16 v[16:19], a[4:7], a[32:35], v[16:19]\n"
    "ds_read_b64_tr_b16 a[14:15], v149 offset:33800\n"
    "v_mfma_f32_16x16x32_bf16 v[20:23], a[4:7], a[36:39], v[20:23]\n"
    "buffer_load_dwordx4 v158, s[36:39], s58 offen lds\n"
    "v_mfma_f32_16x16x32_bf16 v[24:27], a[4:7], a[40:43], v[24:27]\n"
    "v_mfma_f32_16x16x32_bf16 v[28:31], a[4:7], a[44:47], v[28:31]\n"
    "v_mfma_f32_16x16x32_bf16 v[132:135], a[4:7], a[48:51], v[132:135]\n"
    "s_waitcnt lgkmcnt(2)\n"
    "ds_read_b64_tr_b16 a[16:17], v150 offset:32768\n"
    "v_mfma_f32_16x16x32_bf16 v[32:35], a[8:11], a[32:35], v[32:35]\n"
    "ds_read_b64_tr_b16 a[18:19], v150 offset:33792\n"
    "v_mfma_f32_16x16x32_bf16 v[36:39], a[8:11], a[36:39], v[36:39]\n"
    "buffer_load_dwordx4 v159, s[36:39], s58 offen offset:1024 lds\n"
    "v_mfma_f32_16x16x32_bf16 v[40:43], a[8:11], a[40:43], v[40:43]\n"
    "s_add_u32 m0, s66, 36864\n"
    "v_mfma_f32_16x16x32_bf16 v[44:47], a[8:11], a[44:47], v[44:47]\n"
    "ds_read_b64_tr_b16 a[20:21], v150 offset:32776\n"
    "ds_read_b64_tr_b16 a[22:23], v150 offset:33800\n"
    "v_mfma_f32_16x16x32_bf16 v[136:139], a[8:11], a[48:51], v[136:139]\n"
    "s_waitcnt lgkmcnt(4)\n"
    "v_mfma_f32_16x16x32_bf16 v[48:51], a[12:15], a[32:35], v[48:51]\n"
    "buffer_load_dword v160, s[36:39], s58 offen lds\n"
    "v_mfma_f32_16x16x32_bf16 v[52:55], a[12:15], a[36:39], v[52:55]\n"
    "s_add_u32 s58, s58, 64\n"
    "v_mfma_f32_16x16x32_bf16 v[56:59], a[12:15], a[40:43], v[56:59]\n"
    "s_cmp_eq_u32 s58, 1280\n"
    "s_cselect_b32 s58, 0, s58\n"
    "v_mfma_f32_16x16x32_bf16 v[60:63], a[12:15], a[44:47], v[60:63]\n"
    "s_lshl_b32 s57, s58, 12\n"
    "ds_read_b64_tr_b16 a[24:25], v151 offset:32768\n"
    "v_mfma_f32_16x16x32_bf16 v[140:143], a[12:15], a[48:51], v[140:143]\n"
    "s_waitcnt lgkmcnt(3)\n"
    "ds_read_b64_tr_b16 a[26:27], v151 offset:33792\n"
    "v_mfma_f32_16x16x32_bf16 v[64:67], a[16:19], a[32:35], v[64:67]\n"
    "s_waitcnt vmcnt(23)\n"
    "s_barrier\n"
    "v_mfma_f32_16x16x32_bf16 v[68:71], a[16:19], a[36:39], v[68:71]\n"
    "ds_read_b128 a[52:55], v152 offset:18432\n"
    "s_add_u32 s59, s67, 16384\n"
    "v_mfma_f32_16x16x32_bf16 v[72:75], a[16:19], a[40:43], v[72:75]\n"
    "s_and_b32 s59, s59, 0x3ffff\n"
    "s_lshl_b32 s60, s77, 12\n"
    "v_mfma_f32_16x16x32_bf16 v[76:79], a[16:19], a[44:47], v[76:79]\n"
    "s_waitcnt lgkmcnt(3)\n"
    "s_add_u32 s59, s59, s60\n"
    "v_mfma_f32_16x16x32_bf16 v[80:83], a[20:23], a[32:35], v[80:83]\n"
    "s_add_u32 m0, s61, 32768\n"
    "ds_read_b64_tr_b16 a[28:29], v151 offset:32776\n"
    "v_mfma_f32_16x16x32_bf16 v[84:87], a[20:23], a[36:39], v[84:87]\n"
    "ds_read_b64_tr_b16 a[30:31], v151 offset:33800\n"
    "ds_read_b128 a[56:59], v152 offset:19456\n"
    "v_mfma_f32_16x16x32_bf16 v[88:91], a[20:23], a[40:43], v[88:91]\n"
    "ds_read_b128 a[60:63], v152 offset:20480\n"
    "ds_read_b128 a[64:67], v152 offset:21504\n"
    "v_mfma_f32_16x16x32_bf16 v[92:95], a[20:23], a[44:47], v[92:95]\n"
    "s_waitcnt lgkmcnt(6)\n"
    "buffer_load_dwordx4 v250, s[44:47], s59 offen nt lds\n"
    "v_mfma_f32_16x16x32_bf16 v[96:99], a[24:27], a[32:35], v[96:99]\n"
    "ds_read_b128 a[68:71], v153 offset:18432\n"
    "ds_read_b64_tr_b16 a[0:1], v144 offset:49152\n"
    "v_mfma_f32_16x16x32_bf16 v[100:103], a[24:27], a[36:39], v[100:103]\n"
    "ds_read_b64_tr_b16 a[2:3], v144 offset:50176\n"
    "v_mfma_f32_16x16x32_bf16 v[104:107], a[24:27], a[40:43], v[104:107]\n"
    "ds_read_b64_tr_b16 a[4:5], v144 offset:49160\n"
    "ds_read_b64_tr_b16 a[6:7], v144 offset:50184\n"
    "v_mfma_f32_16x16x32_bf16 v[108:111], a[24:27], a[44:47], v[108:111]\n"
    "s_waitcnt lgkmcnt(8)\n"
    "v_mfma_f32_16x16x32_bf16 v[112:115], a[28:31], a[32:35], v[112:115]\n"
    "buffer_load_dwordx4 v251, s[44:47], s59 offen offset:1024 nt lds\n"
    "v_mfma_f32_16x16x32_bf16 v[116:119], a[28:31], a[36:39], v[116:119]\n"
    "v_mfma_f32_16x16x32_bf16 v[120:123], a[28:31], a[40:43], v[120:123]\n"
    "v_mfma_f32_16x16x32_bf16 v[124:127], a[28:31], a[44:47], v[124:127]\n"
    "s_waitcnt lgkmcnt(2)\n"
    "ds_read_b64_tr_b16 a[8:9], v145 offset:49152\n"
    "; ---- H17\n"
    "v_mfma_f32_16x16x32_bf16 v[0:3], a[0:3], a[52:55], v[0:3]\n"
    "ds_read_b64_tr_b16 a[10:11], v145 offset:50176\n"
    "v_mfma_f32_16x16x32_bf16 v[4:7], a[0:3], a[56:59], v[4:7]\n"
    "v_mfma_f32_16x16x32_bf16 v[8:11], a[0:3], a[60:63], v[8:11]\n"
    "v_mfma_f32_16x16x32_bf16 v[12:15], a[0:3], a[64:67], v[12:15]\n"
    "buffer_load_dwordx4 v252, s[44:47], s59 offen offset:2048 nt lds\n"
    "v_mfma_f32_16x16x32_bf16 v[128:131], a[0:3], a[68:71], v[128:131]\n"
    "s_waitcnt lgkmcnt(2)\n"
    "ds_read_b64_tr_b16 a[12:13], v145 offset:49160\n"
    "v_mfma_f32_16x16x32_bf16 v[16:19], a[4:7], a[52:55], v[16:19]\n"
    "ds_read_b64_tr_b16 a[14:15], v145 offset:50184\n"
    "v_mfma_f32_16x16x32_bf16 v[20:23], a[4:7], a[56:59], v[20:23]\n"
    "v_mfma_f32_16x16x32_bf16 v[24:27], a[4:7], a[60:63], v[24:27]\n"
    "v_mfma_f32_16x16x32_bf16 v[28:31], a[4:7], a[64:67], v[28:31]\n"
    "v_mfma_f32_16x16x32_bf16 v[132:135], a[4:7], a[68:71], v[132:135]\n"
    "s_waitcnt lgkmcnt(2)\n"
    "ds_read_b64_tr_b16 a[16:17], v146 offset:49152\n"
    "v_mfma_f32_16x16x32_bf16 v[32:35], a[8:11], a[52:55], v[32:35]\n"
    "ds_read_b64_tr_b16 a[18:19], v146 offset:50176\n"
    "v_mfma_f32_16x16x32_bf16 v[36:39], a[8:11], a[56:59], v[36:39]\n"
    "buffer_load_dwordx4 v253, s[44:47], s59 offen offset:3072 nt lds\n"
    "v_mfma_f32_16x16x32_bf16 v[40:43], a[8:11], a[60:63], v[40:43]\n"
    "v_mfma_f32_16x16x32_bf16 v[44:47], a[8:11], a[64:67], v[44:47]\n"
    "ds_read_b64_tr_b16 a[20:21], v146 offset:49160\n"
    "ds_read_b64_tr_b16 a[22:23], v146 offset:50184\n"
    "v_mfma_f32_16x16x32_bf16 v[136:139], a[8:11], a[68:71], v[136:139]\n"
    "s_waitcnt lgkmcnt(4)\n"
    "v_mfma_f32_16x16x32_bf16 v[48:51], a[12:15], a[52:55], v[48:51]\n"
    "v_mfma_f32_16x16x32_bf16 v[52:55], a[12:15], a[56:59], v[52:55]\n"
    "v_mfma_f32_16x16x32_bf16 v[56:59], a[12:15], a[60:63], v[56:59]\n"
    "ds_read_b64_tr_b16 a[24:25], v147 offset:49152\n"
    "ds_read_b64_tr_b16 a[26:27], v147 offset:50176\n"
    "v_mfma_f32_16x16x32_bf16 v[60:63], a[12:15], a[64:67], v[60:63]\n"
    "v_mfma_f32_16x16x32_bf16 v[140:143], a[12:15], a[68:71], v[140:143]\n"
    "s_waitcnt lgkmcnt(4)\n"
    "v_mfma_f32_16x16x32_bf16 v[64:67], a[16:19], a[52:55], v[64:67]\n"
    "s_waitcnt vmcnt(20)\n"
    "s_barrier\n"
    "v_mfma_f32_16x16x32_bf16 v[68:71], a[16:19], a[56:59], v[68:71]\n"
    "ds_read_b128 a[32:35], v152 offset:27648\n"
    "s_add_u32 s59, s67, 16384\n"
    "v_mfma_f32_16x16x32_bf16 v[72:75], a[16:19], a[60:63], v[72:75]\n"
    "s_and_b32 s59, s59, 0x3ffff\n"
    "s_lshl_b32 s60, s77, 12\n"
    "v_mfma_f32_16x16x32_bf16 v[76:79], a[16:19], a[64:67], v[76:79]\n"
    "s_waitcnt lgkmcnt(3)\n"
    "s_add_u32 s59, s59, s60\n"
    "v_mfma_f32_16x16x32_bf16 v[80:83], a[20:23], a[52:55], v[80:83]\n"
    "s_add_u32 m0, s61, 98304\n"
    "ds_read_b64_tr_b16 a[28:29], v147 offset:49160\n"
    "v_mfma_f32_16x16x32_bf16 v[84:87], a[20:23], a[56:59], v[84:87]\n"
    "ds_read_b64_tr_b16 a[30:31], v147 offset:50184\n"
    "ds_read_b128 a[36:39], v152 offset:28672\n"
    "v_mfma_f32_16x16x32_bf16 v[88:91], a[20:23], a[60:63], v[88:91]\n"
    "ds_read_b128 a[40:43], v152 offset:29696\n"
    "ds_read_b128 a[44:47], v152 offset:30720\n"
    "v_mfma_f32_16x16x32_bf16 v[92:95], a[20:23], a[64:67], v[92:95]\n"
    "s_waitcnt lgkmcnt(6)\n"
    "buffer_load_dwordx4 v250, s[48:51], s59 offen nt lds\n"
    "v_mfma_f32_16x16x32_bf16 v[96:99], a[24:27], a[52:55], v[96:99]\n"
    "ds_read_b128 a[48:51], v153 offset:27648\n"
    "ds_read_b64_tr_b16 a[0:1], v144 offset:16384\n"
    "v_mfma_f32_16x16x32_bf16 v[100:103], a[24:27], a[56:59], v[100:103]\n"
    "ds_read_b64_tr_b16 a[2:3], v144 offset:17408\n"
    "v_mfma_f32_16x16x32_bf16 v[104:107], a[24:27], a[60:63], v[104:107]\n"
    "ds_read_b64_tr_b16 a[4:5], v144 offset:16392\n"
    "ds_read_b64_tr_b16 a[6:7], v144 offset:17416\n"
    "v_mfma_f32_16x16x32_bf16 v[108:111], a[24:27], a[64:67], v[108:111]\n"
    "s_waitcnt lgkmcnt(8)\n"
    "v_mfma_f32_16x16x32_bf16 v[112:115], a[28:31], a[52:55], v[112:115]\n"
    "buffer_load_dwordx4 v252, s[48:51], s59 offen offset:1024 nt lds\n"
    "v_mfma_f32_16x16x32_bf16 v[116:119], a[28:31], a[56:59], v[116:119]\n"
    "v_mfma_f32_16x16x32_bf16 v[120:123], a[28:31], a[60:63], v[120:123]\n"
    "v_mfma_f32_16x16x32_bf16 v[124:127], a[28:31], a[64:67], v[124:127]\n"
    "s_waitcnt lgkmcnt(2)\n"
    "ds_read_b64_tr_b16 a[8:9], v145 offset:16384\n"
    "; ---- H18\n"
    "v_mfma_f32_16x16x32_bf16 v[0:3], a[0:3], a[32:35], v[0:3]\n"
    "ds_read_b64_tr_b16 a[10:11], v145 offset:17408\n"
    "v_mfma_f32_16x16x32_bf16 v[4:7], a[0:3], a[36:39], v[4:7]\n"
    "v_mfma_f32_16x16x32_bf16 v[8:11], a[0:3], a[40:43], v[8:11]\n"
    "v_mfma_f32_16x16x32_bf16 v[12:15], a[0:3], a[44:47], v[12:15]\n"
    "buffer_load_dwordx4 v250, s[48:51], s59 offen offset:2048 nt lds\n"
    "v_mfma_f32_16x16x32_bf16 v[128:131], a[0:3], a[48:51], v[128:131]\n"
    "s_waitcnt lgkmcnt(2)\n"
    "ds_read_b64_tr_b16 a[12:13], v145 offset:16392\n"
    "v_mfma_f32_16x16x32_bf16 v[16:19], a[4:7], a[32:35], v[16:19]\n"
    "ds_read_b64_tr_b16 a[14:15], v145 offset:17416\n"
    "v_mfma_f32_16x16x32_bf16 v[20:23], a[4:7], a[36:39], v[20:23]\n"
    "v_mfma_f32_16x16x32_bf16 v[24:27], a[4:7], a[40:43], v[24:27]\n"
    "v_mfma_f32_16x16x32_bf16 v[28:31], a[4:7], a[44:47], v[28:31]\n"
    "v_mfma_f32_16x16x32_bf16 v[132:135], a[4:7], a[48:51], v[132:135]\n"
    "s_waitcnt lgkmcnt(2)\n"
    "ds_read_b64_tr_b16 a[16:17], v146 offset:16384\n"
    "v_mfma_f32_16x16x32_bf16 v[32:35], a[8:11], a[32:35], v[32:35]\n"
    "ds_read_b64_tr_b16 a[18:19], v146 offset:17408\n"
    "v_mfma_f32_16x16x32_bf16 v[36:39], a[8:11], a[36:39], v[36:39]\n"
    "buffer_load_dwordx4 v252, s[48:51], s59 offen offset:3072 nt lds\n"
    "v_mfma_f32_16x16x32_bf16 v[40:43], a[8:11], a[40:43], v[40:43]\n"
    "v_mfma_f32_16x16x32_bf16 v[44:47], a[8:11], a[44:47], v[44:47]\n"
    "ds_read_b64_tr_b16 a[20:21], v146 offset:16392\n"
    "ds_read_b64_tr_b16 a[22:23], v146 offset:17416\n"
    "v_mfma_f32_16x16x32_bf16 v[136:139], a[8:11], a[48:51], v[136:139]\n"
    "s_waitcnt lgkmcnt(4)\n"
    "v_mfma_f32_16x16x32_bf16 v[48:51], a[12:15], a[32:35], v[48:51]\n"
    "v_mfma_f32_16x16x32_bf16 v[52:55], a[12:15], a[36:39], v[52:55]\n"
    "v_mfma_f32_16x16x32_bf16 v[56:59], a[12:15], a[40:43], v[56:59]\n"
    "ds_read_b64_tr_b16 a[24:25], v147 offset:16384\n"
    "ds_read_b64_tr_b16 a[26:27], v147 offset:17408\n"
    "v_mfma_f32_16x16x32_bf16 v[60:63], a[12:15], a[44:47], v[60:63]\n"
    "v_mfma_f32_16x16x32_bf16 v[140:143], a[12:15], a[48:51], v[140:143]\n"
    "s_waitcnt lgkmcnt(4)\n"
    "v_mfma_f32_16x16x32_bf16 v[64:67], a[16:19], a[32:35], v[64:67]\n"
    "s_waitcnt vmcnt(8)\n"
    "s_barrier\n"
    "v_mfma_f32_16x16x32_bf16 v[68:71], a[16:19], a[36:39], v[68:71]\n"
    "ds_read_b128 a[52:55], v152 offset:36864\n"
    "s_add_u32 s59, s67, 24576\n"
    "v_mfma_f32_16x16x32_bf16 v[72:75], a[16:19], a[40:43], v[72:75]\n"
    "s_and_b32 s59, s59, 0x3ffff\n"
    "s_lshl_b32 s60, s77, 12\n"
    "v_mfma_f32_16x16x32_bf16 v[76:79], a[16:19], a[44:47], v[76:79]\n"
    "s_waitcnt lgkmcnt(3)\n"
    "s_add_u32 s59, s59, s60\n"
    "v_mfma_f32_16x16x32_bf16 v[80:83], a[20:23], a[32:35], v[80:83]\n"
    "s_add_u32 m0, s61, 49152\n"
    "ds_read_b64_tr_b16 a[28:29], v147 offset:16392\n"
    "v_mfma_f32_16x16x32_bf16 v[84:87], a[20:23], a[36:39], v[84:87]\n"
    "ds_read_b64_tr_b16 a[30:31], v147 offset:17416\n"
    "ds_read_b128 a[56:59], v152 offset:37888\n"
    "v_mfma_f32_16x16x32_bf16 v[88:91], a[20:23], a[40:43], v[88:91]\n"
    "ds_read_b128 a[60:63], v152 offset:38912\n"
    "ds_read_b128 a[64:67], v152 offset:39936\n"
    "v_mfma_f32_16x16x32_bf16 v[92:95], a[20:23], a[44:47], v[92:95]\n"
    "s_waitcnt lgkmcnt(6)\n"
    "buffer_load_dwordx4 v250, s[44:47], s59 offen nt lds\n"
    "v_mfma_f32_16x16x32_bf16 v[96:99], a[24:27], a[32:35], v[96:99]\n"
    "ds_read_b128 a[68:71], v153 offset:36864\n"
    "ds_read_b64_tr_b16 a[0:1], v148 offset:16384\n"
    "v_mfma_f32_16x16x32_bf16 v[100:103], a[24:27], a[36:39], v[100:103]\n"
    "ds_read_b64_tr_b16 a[2:3], v148 offset:17408\n"
    "v_mfma_f32_16x16x32_bf16 v[104:107], a[24:27], a[40:43], v[104:107]\n"
    "ds_read_b64_tr_b16 a[4:5], v148 offset:16392\n"
    "ds_read_b64_tr_b16 a[6:7], v148 offset:17416\n"
    "v_mfma_f32_16x16x32_bf16 v[108:111], a[24:27], a[44:47], v[108:111]\n"
    "s_waitcnt lgkmcnt(8)\n"
    "v_mfma_f32_16x16x32_bf16 v[112:115], a[28:31], a[32:35], v[112:115]\n"
    "v_mfma_f32_16x16x32_bf16 v[116:119], a[28:31], a[36:39], v[116:119]\n"
    "v_mfma_f32_16x16x32_bf16 v[120:123], a[28:31], a[40:43], v[120:123]\n"
    "v_mfma_f32_16x16x32_bf16 v[124:127], a[28:31], a[44:47], v[124:127]\n"
    "s_waitcnt lgkmcnt(2)\n"
    "ds_read_b64_tr_b16 a[8:9], v149 offset:16384\n"
    "; ---- H19\n"
    "v_mfma_f32_16x16x32_bf16 v[0:3], a[0:3], a[52:55], v[0:3]\n"
    "ds_read_b64_tr_b16 a[10:11], v149 offset:17408\n"
    "v_mfma_f32_16x16x32_bf16 v[4:7], a[0:3], a[56:59], v[4:7]\n"
    "buffer_load_dwordx4 v251, s[44:47], s59 offen offset:1024 nt lds\n"
    "v_mfma_f32_16x16x32_bf16 v[8:11], a[0:3], a[60:63], v[8:11]\n"
    "v_mfma_f32_16x16x32_bf16 v[12:15], a[0:3], a[64:67], v[12:15]\n"
    "v_mfma_f32_16x16x32_bf16 v[128:131], a[0:3], a[68:71], v[128:131]\n"
    "s_waitcnt lgkmcnt(2)\n"
    "ds_read_b64_tr_b16 a[12:13], v149 offset:16392\n"
    "v_mfma_f32_16x16x32_bf16 v[16:19], a[4:7], a[52:55], v[16:19]\n"
    "ds_read_b64_tr_b16 a[14:15], v149 offset:17416\n"
    "v_mfma_f32_16x16x32_bf16 v[20:23], a[4:7], a[56:59], v[20:23]\n"
    "v_mfma_f32_16x16x32_bf16 v[24:27], a[4:7], a[60:63], v[24:27]\n"
    "v_mfma_f32_16x16x32_bf16 v[28:31], a[4:7], a[64:67], v[28:31]\n"
    "v_mfma_f32_16x16x32_bf16 v[132:135], a[4:7], a[68:71], v[132:135]\n"
    "s_waitcnt lgkmcnt(2)\n"
    "ds_read_b64_tr_b16 a[16:17], v150 offset:16384\n"
    "v_mfma_f32_16x16x32_bf16 v[32:35], a[8:11], a[52:55], v[32:35]\n"
    "ds_read_b64_tr_b16 a[18:19], v150 offset:17408\n"
    "v_mfma_f32_16x16x32_bf16 v[36:39], a[8:11], a[56:59], v[36:39]\n"
    "buffer_load_dwordx4 v252, s[44:47], s59 offen offset:2048 nt lds\n"
    "v_mfma_f32_16x16x32_bf16 v[40:43], a[8:11], a[60:63], v[40:43]\n"
    "v_mfma_f32_16x16x32_bf16 v[44:47], a[8:11], a[64:67], v[44:47]\n"
    "ds_read_b64_tr_b16 a[20:21], v150 offset:16392\n"
    "ds_read_b64_tr_b16 a[22:23], v150 offset:17416\n"
    "v_mfma_f32_16x16x32_bf16 v[136:139], a[8:11], a[68:71], v[136:139]\n"
    "s_waitcnt lgkmcnt(4)\n"
    "v_mfma_f32_16x16x32_bf16 v[48:51], a[12:15], a[52:55], v[48:51]\n"
    "v_mfma_f32_16x16x32_bf16 v[52:55], a[12:15], a[56:59], v[52:55]\n"
    "v_mfma_f32_16x16x32_bf16 v[56:59], a[12:15], a[60:63], v[56:59]\n"
    "ds_read_b64_tr_b16 a[24:25], v151 offset:16384\n"
    "ds_read_b64_tr_b16 a[26:27], v151 offset:17408\n"
    "v_mfma_f32_16x16x32_bf16 v[60:63], a[12:15], a[64:67], v[60:63]\n"
    "v_mfma_f32_16x16x32_bf16 v[140:143], a[12:15], a[68:71], v[140:143]\n"
    "s_waitcnt lgkmcnt(4)\n"
    "v_mfma_f32_16x16x32_bf16 v[64:67], a[16:19], a[52:55], v[64:67]\n"
    "v_mfma_f32_16x16x32_bf16 v[68:71], a[16:19], a[56:59], v[68:71]\n"
    "ds_read_b64_tr_b16 a[28:29], v151 offset:16392\n"
    "ds_read_b64_tr_b16 a[30:31], v151 offset:17416\n"
    "v_mfma_f32_16x16x32_bf16 v[72:75], a[16:19], a[60:63], v[72:75]\n"
    "v_mfma_f32_16x16x32_bf16 v[76:79], a[16:19], a[64:67], v[76:79]\n"
    "s_waitcnt lgkmcnt(4)\n"
    "v_mfma_f32_16x16x32_bf16 v[80:83], a[20:23], a[52:55], v[80:83]\n"
    "buffer_load_dwordx4 v253, s[44:47], s59 offen offset:3072 nt lds\n"
    "v_mfma_f32_16x16x32_bf16 v[84:87], a[20:23], a[56:59], v[84:87]\n"
    "v_mfma_f32_16x16x32_bf16 v[88:91], a[20:23], a[60:63], v[88:91]\n"
    "v_mfma_f32_16x16x32_bf16 v[92:95], a[20:23], a[64:67], v[92:95]\n"
    "s_waitcnt lgkmcnt(2)\n"
    "v_mfma_f32_16x16x32_bf16 v[96:99], a[24:27], a[52:55], v[96:99]\n"
    "v_mfma_f32_16x16x32_bf16 v[100:103], a[24:27], a[56:59], v[100:103]\n"
    "v_mfma_f32_16x16x32_bf16 v[104:107], a[24:27], a[60:63], v[104:107]\n"
    "v_mfma_f32_16x16x32_bf16 v[108:111], a[24:27], a[64:67], v[108:111]\n"
    "s_waitcnt lgkmcnt(0)\n"
    "v_mfma_f32_16x16x32_bf16 v[112:115], a[28:31], a[52:55], v[112:115]\n"
    "v_mfma_f32_16x16x32_bf16 v[116:119], a[28:31], a[56:59], v[116:119]\n"
    "v_mfma_f32_16x16x32_bf16 v[120:123], a[28:31], a[60:63], v[120:123]\n"
    "v_mfma_f32_16x16x32_bf16 v[124:127], a[28:31], a[64:67], v[124:127]\n"
    "; ---- HEND\n"
    "s_barrier\n"
    "v_mbcnt_lo_u32_b32 v180, -1, 0\n"
    "v_mbcnt_hi_u32_b32 v180, -1, v180\n"
    "v_lshlrev_b32 v180, 4, v180\n"
    "s_lshl_b32 s74, s56, 11\n"
    "s_add_u32 s74, s74, 131072\n"
    "v_add_u32 v181, s74, v180\n"
    "v_xor_b32 v235, 2048, v181\n"
    "v_pk_mul_f32 v[128:129], v[128:129], s[72:73]\n"
    "v_pk_mul_f32 v[130:131], v[130:131], s[72:73]\n"
    "v_pk_mul_f32 v[132:133], v[132:133], s[72:73]\n"
    "v_pk_mul_f32 v[134:135], v[134:135], s[72:73]\n"
    "v_cvt_pk_bf16_f32 v164, v128, v129\n"
    "v_cvt_pk_bf16_f32 v165, v130, v131\n"
    "v_cvt_pk_bf16_f32 v166, v132, v133\n"
    "v_cvt_pk_bf16_f32 v167, v134, v135\n"
    "ds_write_b128 v181, v[164:167]\n"
    "v_pk_mul_f32 v[136:137], v[136:137], s[72:73]\n"
    "v_pk_mul_f32 v[138:139], v[138:139], s[72:73]\n"
    "v_pk_mul_f32 v[140:141], v[140:141], s[72:73]\n"
    "v_pk_mul_f32 v[142:143], v[142:143], s[72:73]\n"
    "v_cvt_pk_bf16_f32 v168, v136, v137\n"
    "v_cvt_pk_bf16_f32 v169, v138, v139\n"
    "v_cvt_pk_bf16_f32 v170, v140, v141\n"
    "v_cvt_pk_bf16_f32 v171, v142, v143\n"
    "ds_write_b128 v181, v[168:171] offset:1024\n"
    "v_accvgpr_write_b32 a64, v164\n"
    "v_accvgpr_write_b32 a65, v165\n"
    "v_accvgpr_write_b32 a66, v166\n"
    "v_accvgpr_write_b32 a67, v167\n"
    "v_accvgpr_write_b32 a68, v168\n"
    "v_accvgpr_write_b32 a69, v169\n"
    "v_accvgpr_write_b32 a70, v170\n"
    "v_accvgpr_write_b32 a71, v171\n"
    "v_pk_mul_f32 v[0:1], v[0:1], s[72:73]\n"
    "v_pk_mul_f32 v[2:3], v[2:3], s[72:73]\n"
    "v_pk_mul_f32 v[16:17], v[16:17], s[72:73]\n"
    "v_pk_mul_f32 v[18:19], v[18:19], s[72:73]\n"
    "v_cvt_pk_bf16_f32 v172, v0, v1\n"
    "v_cvt_pk_bf16_f32 v173, v2, v3\n"
    "v_cvt_pk_bf16_f32 v174, v16, v17\n"
    "v_cvt_pk_bf16_f32 v175, v18, v19\n"
    "v_accvgpr_write_b32 a0, v172\n"
    "v_accvgpr_write_b32 a1, v173\n"
    "v_accvgpr_write_b32 a2, v174\n"
    "v_accvgpr_write_b32 a3, v175\n"
    "v_pk_mul_f32 v[4:5], v[4:5], s[72:73]\n"
    "v_pk_mul_f32 v[6:7], v[6:7], s[72:73]\n"
    "v_pk_mul_f32 v[20:21], v[20:21], s[72:73]\n"
    "v_pk_mul_f32 v[22:23], v[22:23], s[72:73]\n"
    "v_cvt_pk_bf16_f32 v176, v4, v5\n"
    "v_cvt_pk_bf16_f32 v177, v6, v7\n"
    "v_cvt_pk_bf16_f32 v178, v20, v21\n"
    "v_cvt_pk_bf16_f32 v179, v22, v23\n"
    "v_accvgpr_write_b32 a16, v176\n"
    "v_accvgpr_write_b32 a17, v177\n"
    "v_accvgpr_write_b32 a18, v178\n"
    "v_accvgpr_write_b32 a19, v179\n"
    "v_pk_mul_f32 v[8:9], v[8:9], s[72:73]\n"
    "v_pk_mul_f32 v[10:11], v[10:11], s[72:73]\n"
    "v_pk_mul_f32 v[24:25], v[24:25], s[72:73]\n"
    "v_pk_mul_f32 v[26:27], v[26:27], s[72:73]\n"
    "v_cvt_pk_bf16_f32 v172, v8, v9\n"
    "v_cvt_pk_bf16_f32 v173, v10, v11\n"
    "v_cvt_pk_bf16_f32 v174, v24, v25\n"
    "v_cvt_pk_bf16_f32 v175, v26, v27\n"
    "v_accvgpr_write_b32 a32, v172\n"
    "v_accvgpr_write_b32 a33, v173\n"
    "v_accvgpr_write_b32 a34, v174\n"
    "v_accvgpr_write_b32 a35, v175\n"
    "v_pk_mul_f32 v[12:13], v[12:13], s[72:73]\n"
    "v_pk_mul_f32 v[14:15], v[14:15], s[72:73]\n"
    "v_pk_mul_f32 v[28:29], v[28:29], s[72:73]\n"
    "v_pk_mul_f32 v[30:31], v[30:31], s[72:73]\n"
    "v_cvt_pk_bf16_f32 v176, v12, v13\n"
    "v_cvt_pk_bf16_f32 v177, v14, v15\n"
    "v_cvt_pk_bf16_f32 v178, v28, v29\n"
    "v_cvt_pk_bf16_f32 v179, v30, v31\n"
    "v_accvgpr_write_b32 a48, v176\n"
    "v_accvgpr_write_b32 a49, v177\n"
    "v_accvgpr_write_b32 a50, v178\n"
    "v_accvgpr_write_b32 a51, v179\n"
    "v_pk_mul_f32 v[32:33], v[32:33], s[72:73]\n"
    "v_pk_mul_f32 v[34:35], v[34:35], s[72:73]\n"
    "v_pk_mul_f32 v[48:49], v[48:49], s[72:73]\n"
    "v_pk_mul_f32 v[50:51], v[50:51], s[72:73]\n"
    "v_cvt_pk_bf16_f32 v172, v32, v33\n"
    "v_cvt_pk_bf16_f32 v173, v34, v35\n"
    "v_cvt_pk_bf16_f32 v174, v48, v49\n"
    "v_cvt_pk_bf16_f32 v175, v50, v51\n"
    "v_accvgpr_write_b32 a4, v172\n"
    "v_accvgpr_write_b32 a5, v173\n"
    "v_accvgpr_write_b32 a6, v174\n"
    "v_accvgpr_write_b32 a7, v175\n"
    "v_pk_mul_f32 v[36:37], v[36:37], s[72:73]\n"
    "v_pk_mul_f32 v[38:39], v[38:39], s[72:73]\n"
    "v_pk_mul_f32 v[52:53], v[52:53], s[72:73]\n"
    "v_pk_mul_f32 v[54:55], v[54:55], s[72:73]\n"
    "v_cvt_pk_bf16_f32 v176, v36, v37\n"
    "v_cvt_pk_bf16_f32 v177, v38, v39\n"
    "v_cvt_pk_bf16_f32 v178, v52, v53\n"
    "v_cvt_pk_bf16_f32 v179, v54, v55\n"
    "v_accvgpr_write_b32 a20, v176\n"
    "v_accvgpr_write_b32 a21, v177\n"
    "v_accvgpr_write_b32 a22, v178\n"
    "v_accvgpr_write_b32 a23, v179\n"
    "v_pk_mul_f32 v[40:41], v[40:41], s[72:73]\n"
    "v_pk_mul_f32 v[42:43], v[42:43], s[72:73]\n"
    "v_pk_mul_f32 v[56:57], v[56:57], s[72:73]\n"
    "v_pk_mul_f32 v[58:59], v[58:59], s[72:73]\n"
    "v_cvt_pk_bf16_f32 v172, v40, v41\n"
    "v_cvt_pk_bf16_f32 v173, v42, v43\n"
    "v_cvt_pk_bf16_f32 v174, v56, v57\n"
    "v_cvt_pk_bf16_f32 v175, v58, v59\n"
    "v_accvgpr_write_b32 a36, v172\n"
    "v_accvgpr_write_b32 a37, v173\n"
    "v_accvgpr_write_b32 a38, v174\n"
    "v_accvgpr_write_b32 a39, v175\n"
    "v_pk_mul_f32 v[44:45], v[44:45], s[72:73]\n"
    "v_pk_mul_f32 v[46:47], v[46:47], s[72:73]\n"
    "v_pk_mul_f32 v[60:61], v[60:61], s[72:73]\n"
    "v_pk_mul_f32 v[62:63], v[62:63], s[72:73]\n"
    "v_cvt_pk_bf16_f32 v176, v44, v45\n"
    "v_cvt_pk_bf16_f32 v177, v46, v47\n"
    "v_cvt_pk_bf16_f32 v178, v60, v61\n"
    "v_cvt_pk_bf16_f32 v179, v62, v63\n"
    "v_accvgpr_write_b32 a52, v176\n"
    "v_accvgpr_write_b32 a53, v177\n"
    "v_accvgpr_write_b32 a54, v178\n"
    "v_accvgpr_write_b32 a55, v179\n"
    "v_pk_mul_f32 v[64:65], v[64:65], s[72:73]\n"
    "v_pk_mul_f32 v[66:67], v[66:67], s[72:73]\n"
    "v_pk_mul_f32 v[80:81], v[80:81], s[72:73]\n"
    "v_pk_mul_f32 v[82:83], v[82:83], s[72:73]\n"
    "v_cvt_pk_bf16_f32 v172, v64, v65\n"
    "v_cvt_pk_bf16_f32 v173, v66, v67\n"
    "v_cvt_pk_bf16_f32 v174, v80, v81\n"
    "v_cvt_pk_bf16_f32 v175, v82, v83\n"
    "v_accvgpr_write_b32 a8, v172\n"
    "v_accvgpr_write_b32 a9, v173\n"
    "v_accvgpr_write_b32 a10, v174\n"
    "v_accvgpr_write_b32 a11, v175\n"
    "v_pk_mul_f32 v[68:69], v[68:69], s[72:73]\n"
    "v_pk_mul_f32 v[70:71], v[70:71], s[72:73]\n"
    "v_pk_mul_f32 v[84:85], v[84:85], s[72:73]\n"
    "v_pk_mul_f32 v[86:87], v[86:87], s[72:73]\n"
    "v_cvt_pk_bf16_f32 v176, v68, v69\n"
    "v_cvt_pk_bf16_f32 v177, v70, v71\n"
    "v_cvt_pk_bf16_f32 v178, v84, v85\n"
    "v_cvt_pk_bf16_f32 v179, v86, v87\n"
    "v_accvgpr_write_b32 a24, v176\n"
    "v_accvgpr_write_b32 a25, v177\n"
    "v_accvgpr_write_b32 a26, v178\n"
    "v_accvgpr_write_b32 a27, v179\n"
    "v_pk_mul_f32 v[72:73], v[72:73], s[72:73]\n"
    "v_pk_mul_f32 v[74:75], v[74:75], s[72:73]\n"
    "v_pk_mul_f32 v[88:89], v[88:89], s[72:73]\n"
    "v_pk_mul_f32 v[90:91], v[90:91], s[72:73]\n"
    "v_cvt_pk_bf16_f32 v172, v72, v73\n"
    "v_cvt_pk_bf16_f32 v173, v74, v75\n"
    "v_cvt_pk_bf16_f32 v174, v88, v89\n"
    "v_cvt_pk_bf16_f32 v175, v90, v91\n"
    "v_accvgpr_write_b32 a40, v172\n"
    "v_accvgpr_write_b32 a41, v173\n"
    "v_accvgpr_write_b32 a42, v174\n"
    "v_accvgpr_write_b32 a43, v175\n"
    "v_pk_mul_f32 v[76:77], v[76:77], s[72:73]\n"
    "v_pk_mul_f32 v[78:79], v[78:79], s[72:73]\n"
    "v_pk_mul_f32 v[92:93], v[92:93], s[72:73]\n"
    "v_pk_mul_f32 v[94:95], v[94:95], s[72:73]\n"
    "v_cvt_pk_bf16_f32 v176, v76, v77\n"
    "v_cvt_pk_bf16_f32 v177, v78, v79\n"
    "v_cvt_pk_bf16_f32 v178, v92, v93\n"
    "v_cvt_pk_bf16_f32 v179, v94, v95\n"
    "v_accvgpr_write_b32 a56, v176\n"
    "v_accvgpr_write_b32 a57, v177\n"
    "v_accvgpr_write_b32 a58, v178\n"
    "v_accvgpr_write_b32 a59, v179\n"
    "v_pk_mul_f32 v[96:97], v[96:97], s[72:73]\n"
    "v_pk_mul_f32 v[98:99], v[98:99], s[72:73]\n"
    "v_pk_mul_f32 v[112:113], v[112:113], s[72:73]\n"
    "v_pk_mul_f32 v[114:115], v[114:115], s[72:73]\n"
    "v_cvt_pk_bf16_f32 v172, v96, v97\n"
    "v_cvt_pk_bf16_f32 v173, v98, v99\n"
    "v_cvt_pk_bf16_f32 v174, v112, v113\n"
    "v_cvt_pk_bf16_f32 v175, v114, v115\n"
    "v_accvgpr_write_b32 a12, v172\n"
    "v_accvgpr_write_b32 a13, v173\n"
    "v_accvgpr_write_b32 a14, v174\n"
    "v_accvgpr_write_b32 a15, v175\n"
    "v_pk_mul_f32 v[100:101], v[100:101], s[72:73]\n"
    "v_pk_mul_f32 v[102:103], v[102:103], s[72:73]\n"
    "v_pk_mul_f32 v[116:117], v[116:117], s[72:73]\n"
    "v_pk_mul_f32 v[118:119], v[118:119], s[72:73]\n"
    "v_cvt_pk_bf16_f32 v176, v100, v101\n"
    "v_cvt_pk_bf16_f32 v177, v102, v103\n"
    "v_cvt_pk_bf16_f32 v178, v116, v117\n"
    "v_cvt_pk_bf16_f32 v179, v118, v119\n"
    "v_accvgpr_write_b32 a28, v176\n"
    "v_accvgpr_write_b32 a29, v177\n"
    "v_accvgpr_write_b32 a30, v178\n"
    "v_accvgpr_write_b32 a31, v179\n"
    "v_pk_mul_f32 v[104:105], v[104:105], s[72:73]\n"
    "v_pk_mul_f32 v[106:107], v[106:107], s[72:73]\n"
    "v_pk_mul_f32 v[120:121], v[120:121], s[72:73]\n"
    "v_pk_mul_f32 v[122:123], v[122:123], s[72:73]\n"
    "v_cvt_pk_bf16_f32 v172, v104, v105\n"
    "v_cvt_pk_bf16_f32 v173, v106, v107\n"
    "v_cvt_pk_bf16_f32 v174, v120, v121\n"
    "v_cvt_pk_bf16_f32 v175, v122, v123\n"
    "v_accvgpr_write_b32 a44, v172\n"
    "v_accvgpr_write_b32 a45, v173\n"
    "v_accvgpr_write_b32 a46, v174\n"
    "v_accvgpr_write_b32 a47, v175\n"
    "v_pk_mul_f32 v[108:109], v[108:109], s[72:73]\n"
    "v_pk_mul_f32 v[110:111], v[110:111], s[72:73]\n"
    "v_pk_mul_f32 v[124:125], v[124:125], s[72:73]\n"
    "v_pk_mul_f32 v[126:127], v[126:127], s[72:73]\n"
    "v_cvt_pk_bf16_f32 v176, v108, v109\n"
    "v_cvt_pk_bf16_f32 v177, v110, v111\n"
    "v_cvt_pk_bf16_f32 v178, v124, v125\n"
    "v_cvt_pk_bf16_f32 v179, v126, v127\n"
    "v_accvgpr_write_b32 a60, v176\n"
    "v_accvgpr_write_b32 a61, v177\n"
    "v_accvgpr_write_b32 a62, v178\n"
    "v_accvgpr_write_b32 a63, v179\n"
    "s_waitcnt lgkmcnt(0)\n"
    "s_barrier\n"
    "s_cmp_lg_u32 s77, 0\n"
    "s_cbranch_scc1 Llight\n"
    "; ---- EPRE\n"
    "ds_read_b128 a[72:75], v235\n"
    "ds_read_b128 a[76:79], v235 offset:1024\n"
    "v_mov_b32 v224, 0x3f803f80\n"
    "v_accvgpr_write_b32 a240, v224\n"
    "v_accvgpr_write_b32 a241, v224\n"
    "v_accvgpr_write_b32 a242, v224\n"
    "v_accvgpr_write_b32 a243, v224\n"
    "s_mov_b32 s70, 0x45800000\n"
    "s_mov_b64 s[36:37], s[16:17]\n"
    "s_and_b32 s37, s37, 0xffff\n"
    "s_mov_b32 s38, 1313856\n"
    "s_mov_b32 s39, 0x20000\n"
    "v_add_u32 v245, 1, v245\n"
    "s_mov_b32 exec_lo, s93\n"
    "s_mov_b32 exec_hi, 0\n"
    "global_store_dword v243, v245, s[80:81] sc1\n"
    "s_mov_b64 exec, -1\n"
    "v_mfma_f32_16x16x32_bf16 v[128:131], a[80:83], a[0:3], 0\n"
    "v_mfma_f32_16x16x32_bf16 v[128:131], a[84:87], a[4:7], v[128:131]\n"
    "v_mfma_f32_16x16x32_bf16 v[128:131], a[88:91], a[8:11], v[128:131]\n"
    "v_mfma_f32_16x16x32_bf16 v[128:131], a[92:95], a[12:15], v[128:131]\n"
    "v_mfma_f32_16x16x32_bf16 v[132:135], a[96:99], a[0:3], 0\n"
    "v_mfma_f32_16x16x32_bf16 v[132:135], a[100:103], a[4:7], v[132:135]\n"
    "v_mfma_f32_16x16x32_bf16 v[132:135], a[104:107], a[8:11], v[132:135]\n"
    "v_mfma_f32_16x16x32_bf16 v[132:135], a[108:111], a[12:15], v[132:135]\n"
    "; ---- ES0\n"
    "v_mfma_f32_16x16x32_bf16 v[136:139], a[80:83], a[16:19], 0\n"
    "s_nop 2\n"
    "v_max3_f32 v224, v128, v129, v130\n"
    "s_nop 2\n"
    "v_max3_f32 v225, v131, v132, v133\n"
    "v_max3_f32 v224, v224, v134, v135\n"
    "v_mfma_f32_16x16x32_bf16 v[136:139], a[84:87], a[20:23], v[136:139]\n"
    "v_max_f32 v224, v224, v225\n"
    "v_mov_b32 v225, v224\n"
    "v_mfma_f32_16x16x32_bf16 v[136:139], a[88:91], a[24:27], v[136:139]\n"
    "s_nop 0\n"
    "v_permlane16_swap_b32 v224, v225\n"
    "v_max_f32 v224, v224, v225\n"
    "v_mov_b32 v225, v224\n"
    "v_mfma_f32_16x16x32_bf16 v[136:139], a[92:95], a[28:31], v[136:139]\n"
    "s_nop 0\n"
    "v_permlane32_swap_b32 v224, v225\n"
    "v_max_f32 v224, v224, v225\n"
    "v_mfma_f32_16x16x32_bf16 v[140:143], a[96:99], a[16:19], 0\n"
    "v_sub_f32 v128, v128, v224\n"
    "v_sub_f32 v129, v129, v224\n"
    "v_sub_f32 v130, v130, v224\n"
    "v_mfma_f32_16x16x32_bf16 v[140:143], a[100:103], a[20:23], v[140:143]\n"
    "v_sub_f32 v131, v131, v224\n"
    "v_sub_f32 v132, v132, v224\n"
    "v_mfma_f32_16x16x32_bf16 v[140:143], a[104:107], a[24:27], v[140:143]\n"
    "v_sub_f32 v133, v133, v224\n"
    "v_sub_f32 v134, v134, v224\n"
    "v_mfma_f32_16x16x32_bf16 v[140:143], a[108:111], a[28:31], v[140:143]\n"
    "v_sub_f32 v135, v135, v224\n"
    "v_sub_f32 v188, 0, v224\n"
    "v_sub_f32 v189, 0, v224\n"
    "v_sub_f32 v190, 0, v224\n"
    "v_sub_f32 v191, 0, v224\n"
    "v_exp_f32 v218, v128\n"
    "v_exp_f32 v219, v129\n"
    "v_exp_f32 v220, v130\n"
    "v_exp_f32 v221, v131\n"
    "v_cvt_pk_bf16_f32 v168, v218, v219\n"
    "v_exp_f32 v222, v132\n"
    "v_exp_f32 v223, v133\n"
    "v_cvt_pk_bf16_f32 v169, v220, v221\n"
    "v_exp_f32 v218, v134\n"
    "v_exp_f32 v219, v135\n"
    "v_cvt_pk_bf16_f32 v170, v222, v223\n"
    "v_cvt_pk_bf16_f32 v171, v218, v219\n"
    "s_nop 1\n"
    "v_mfma_f32_16x16x32_bf16 v[208:211], a[240:243], v[168:171], 0\n"
    "v_mfma_f32_16x16x32_bf16 v[144:147], a[80:83], a[32:35], 0\n"
    "v_max3_f32 v224, v136, v137, v138\n"
    "v_max3_f32 v225, v139, v140, v141\n"
    "v_max3_f32 v224, v224, v142, v143\n"
    "v_mfma_f32_16x16x32_bf16 v[144:147], a[84:87], a[36:39], v[144:147]\n"
    "v_max_f32 v224, v224, v225\n"
    "v_mov_b32 v225, v224\n"
    "v_mfma_f32_16x16x32_bf16 v[144:147], a[88:91], a[40:43], v[144:147]\n"
    "s_nop 0\n"
    "v_permlane16_swap_b32 v224, v225\n"
    "v_max_f32 v224, v224, v225\n"
    "v_mov_b32 v225, v224\n"
    "v_mfma_f32_16x16x32_bf16 v[144:147], a[92:95], a[44:47], v[144:147]\n"
    "s_nop 0\n"
    "v_permlane32_swap_b32 v224, v225\n"
    "v_max_f32 v224, v224, v225\n"
    "v_mfma_f32_16x16x32_bf16 v[148:151], a[96:99], a[32:35], 0\n"
    "v_sub_f32 v136, v136, v224\n"
    "v_sub_f32 v137, v137, v224\n"
    "v_sub_f32 v138, v138, v224\n"
    "v_mfma_f32_16x16x32_bf16 v[148:151], a[100:103], a[36:39], v[148:151]\n"
    "v_sub_f32 v139, v139, v224\n"
    "v_sub_f32 v140, v140, v224\n"
    "v_mfma_f32_16x16x32_bf16 v[148:151], a[104:107], a[40:43], v[148:151]\n"
    "v_mov_b32 v212, v208\n"
    "v_sub_f32 v141, v141, v224\n"
    "v_sub_f32 v142, v142, v224\n"
    "v_mfma_f32_16x16x32_bf16 v[148:151], a[108:111], a[44:47], v[148:151]\n"
    "v_sub_f32 v143, v143, v224\n"
    "v_sub_f32 v192, 0, v224\n"
    "v_sub_f32 v193, 0, v224\n"
    "v_mfma_f32_16x16x32_bf16 v[0:3], a[144:147], v[168:171], 0\n"
    "v_sub_f32 v194, 0, v224\n"
    "v_sub_f32 v195, 0, v224\n"
    "v_mfma_f32_16x16x32_bf16 v[4:7], a[148:151], v[168:171], 0\n"
    "v_exp_f32 v218, v136\n"
    "v_exp_f32 v219, v137\n"
    "v_exp_f32 v220, v138\n"
    "v_mfma_f32_16x16x32_bf16 v[8:11], a[152:155], v[168:171], 0\n"
    "v_exp_f32 v221, v139\n"
    "v_cvt_pk_bf16_f32 v172, v218, v219\n"
    "v_mfma_f32_16x16x32_bf16 v[12:15], a[156:159], v[168:171], 0\n"
    "v_exp_f32 v222, v140\n"
    "v_exp_f32 v223, v141\n"
    "v_cvt_pk_bf16_f32 v173, v220, v221\n"
    "v_mfma_f32_16x16x32_bf16 v[16:19], a[160:163], v[168:171], 0\n"
    "v_exp_f32 v218, v142\n"
    "v_exp_f32 v219, v143\n"
    "v_mfma_f32_16x16x32_bf16 v[20:23], a[164:167], v[168:171], 0\n"
    "v_cvt_pk_bf16_f32 v174, v222, v223\n"
    "v_cvt_pk_bf16_f32 v175, v218, v219\n"
    "v_mfma_f32_16x16x32_bf16 v[24:27], a[168:171], v[168:171], 0\n"
    "v_mfma_f32_16x16x32_bf16 v[28:31], a[172:175], v[168:171], 0\n"
    "v_mfma_f32_16x16x32_bf16 v[208:211], a[240:243], v[172:175], 0\n"
    "v_mfma_f32_16x16x32_bf16 v[152:155], a[80:83], a[48:51], 0\n"
    "v_max3_f32 v224, v144, v145, v146\n"
    "v_max3_f32 v225, v147, v148, v149\n"
    "v_max3_f32 v224, v224, v150, v151\n"
    "v_mfma_f32_16x16x32_bf16 v[152:155], a[84:87], a[52:55], v[152:155]\n"
    "v_max_f32 v224, v224, v225\n"
    "v_mov_b32 v225, v224\n"
    "v_mfma_f32_16x16x32_bf16 v[152:155], a[88:91], a[56:59], v[152:155]\n"
    "s_nop 0\n"
    "v_permlane16_swap_b32 v224, v225\n"
    "v_max_f32 v224, v224, v225\n"
    "v_mov_b32 v225, v224\n"
    "v_mfma_f32_16x16x32_bf16 v[152:155], a[92:95], a[60:63], v[152:155]\n"
    "s_nop 0\n"
    "v_permlane32_swap_b32 v224, v225\n"
    "v_max_f32 v224, v224, v225\n"
    "v_mfma_f32_16x16x32_bf16 v[156:159], a[96:99], a[48:51], 0\n"
    "v_sub_f32 v144, v144, v224\n"
    "v_sub_f32 v145, v145, v224\n"
    "v_sub_f32 v146, v146, v224\n"
    "v_mfma_f32_16x16x32_bf16 v[156:159], a[100:103], a[52:55], v[156:159]\n"
    "v_sub_f32 v147, v147, v224\n"
    "v_sub_f32 v148, v148, v224\n"
    "v_mfma_f32_16x16x32_bf16 v[156:159], a[104:107], a[56:59], v[156:159]\n"
    "v_mov_b32 v213, v208\n"
    "v_sub_f32 v149, v149, v224\n"
    "v_sub_f32 v150, v150, v224\n"
    "v_mfma_f32_16x16x32_bf16 v[156:159], a[108:111], a[60:63], v[156:159]\n"
    "v_sub_f32 v151, v151, v224\n"
    "v_sub_f32 v196, 0, v224\n"
    "v_sub_f32 v197, 0, v224\n"
    "v_mfma_f32_16x16x32_bf16 v[32:35], a[144:147], v[172:175], 0\n"
    "v_sub_f32 v198, 0, v224\n"
    "v_sub_f32 v199, 0, v224\n"
    "v_mfma_f32_16x16x32_bf16 v[36:39], a[148:151], v[172:175], 0\n"
    "v_exp_f32 v218, v144\n"
    "v_exp_f32 v219, v145\n"
    "v_exp_f32 v220, v146\n"
    "v_mfma_f32_16x16x32_bf16 v[40:43], a[152:155], v[172:175], 0\n"
    "v_exp_f32 v221, v147\n"
    "v_cvt_pk_bf16_f32 v176, v218, v219\n"
    "v_mfma_f32_16x16x32_bf16 v[44:47], a[156:159], v[172:175], 0\n"
    "v_exp_f32 v222, v148\n"
    "v_exp_f32 v223, v149\n"
    "v_cvt_pk_bf16_f32 v177, v220, v221\n"
    "v_mfma_f32_16x16x32_bf16 v[48:51], a[160:163], v[172:175], 0\n"
    "v_exp_f32 v218, v150\n"
    "v_exp_f32 v219, v151\n"
    "v_mfma_f32_16x16x32_bf16 v[52:55], a[164:167], v[172:175], 0\n"
    "v_cvt_pk_bf16_f32 v178, v222, v223\n"
    "v_cvt_pk_bf16_f32 v179, v218, v219\n"
    "v_mfma_f32_16x16x32_bf16 v[56:59], a[168:171], v[172:175], 0\n"
    "v_mfma_f32_16x16x32_bf16 v[60:63], a[172:175], v[172:175], 0\n"
    "v_mfma_f32_16x16x32_bf16 v[208:211], a[240:243], v[176:179], 0\n"
    "v_mfma_f32_16x16x32_bf16 v[160:163], a[80:83], a[64:67], 0\n"
    "v_max3_f32 v224, v152, v153, v154\n"
    "v_max3_f32 v225, v155, v156, v157\n"
    "v_max3_f32 v224, v224, v158, v159\n"
    "v_mfma_f32_16x16x32_bf16 v[160:163], a[84:87], a[68:71], v[160:163]\n"
    "v_max_f32 v224, v224, v225\n"
    "v_mov_b32 v225, v224\n"
    "s_waitcnt lgkmcnt(1)\n"
    "v_mfma_f32_16x16x32_bf16 v[160:163], a[88:91], a[72:75], v[160:163]\n"
    "v_permlane16_swap_b32 v224, v225\n"
    "v_max_f32 v224, v224, v225\n"
    "v_mov_b32 v225, v224\n"
    "s_waitcnt lgkmcnt(0)\n"
    "v_mfma_f32_16x16x32_bf16 v[160:163], a[92:95], a[76:79], v[160:163]\n"
    "v_permlane32_swap_b32 v224, v225\n"
    "v_max_f32 v224, v224, v225\n"
    "v_mfma_f32_16x16x32_bf16 v[164:167], a[96:99], a[64:67], 0\n"
    "v_sub_f32 v152, v152, v224\n"
    "v_sub_f32 v153, v153, v224\n"
    "v_sub_f32 v154, v154, v224\n"
    "v_mfma_f32_16x16x32_bf16 v[164:167], a[100:103], a[68:71], v[164:167]\n"
    "v_sub_f32 v155, v155, v224\n"
    "v_sub_f32 v156, v156, v224\n"
    "v_mfma_f32_16x16x32_bf16 v[164:167], a[104:107], a[72:75], v[164:167]\n"
    "v_mov_b32 v214, v208\n"
    "v_sub_f32 v157, v157, v224\n"
    "v_sub_f32 v158, v158, v224\n"
    "v_mfma_f32_16x16x32_bf16 v[164:167], a[108:111], a[76:79], v[164:167]\n"
    "v_sub_f32 v159, v159, v224\n"
    "v_sub_f32 v200, 0, v224\n"
    "v_sub_f32 v201, 0, v224\n"
    "v_mfma_f32_16x16x32_bf16 v[64:67], a[144:147], v[176:179], 0\n"
    "v_sub_f32 v202, 0, v224\n"
    "v_sub_f32 v203, 0, v224\n"
    "v_mfma_f32_16x16x32_bf16 v[68:71], a[148:151], v[176:179], 0\n"
    "v_exp_f32 v218, v152\n"
    "v_exp_f32 v219, v153\n"
    "v_exp_f32 v220, v154\n"
    "v_mfma_f32_16x16x32_bf16 v[72:75], a[152:155], v[176:179], 0\n"
    "v_exp_f32 v221, v155\n"
    "v_cvt_pk_bf16_f32 v180, v218, v219\n"
    "v_mfma_f32_16x16x32_bf16 v[76:79], a[156:159], v[176:179], 0\n"
    "v_exp_f32 v222, v156\n"
    "v_exp_f32 v223, v157\n"
    "v_cvt_pk_bf16_f32 v181, v220, v221\n"
    "v_mfma_f32_16x16x32_bf16 v[80:83], a[160:163], v[176:179], 0\n"
    "v_exp_f32 v218, v158\n"
    "v_exp_f32 v219, v159\n"
    "v_mfma_f32_16x16x32_bf16 v[84:87], a[164:167], v[176:179], 0\n"
    "v_cvt_pk_bf16_f32 v182, v222, v223\n"
    "v_cvt_pk_bf16_f32 v183, v218, v219\n"
    "v_mfma_f32_16x16x32_bf16 v[88:91], a[168:171], v[176:179], 0\n"
    "v_mfma_f32_16x16x32_bf16 v[92:95], a[172:175], v[176:179], 0\n"
    "v_mfma_f32_16x16x32_bf16 v[208:211], a[240:243], v[180:183], 0\n"
    "v_mfma_f32_16x16x32_bf16 v[128:131], a[112:115], a[0:3], v[188:191]\n"
    "v_max3_f32 v224, v160, v161, v162\n"
    "v_max3_f32 v225, v163, v164, v165\n"
    "v_max3_f32 v224, v224, v166, v167\n"
    "v_mfma_f32_16x16x32_bf16 v[128:131], a[116:119], a[4:7], v[128:131]\n"
    "v_max_f32 v224, v224, v225\n"
    "v_mov_b32 v225, v224\n"
    "v_mfma_f32_16x16x32_bf16 v[128:131], a[120:123], a[8:11], v[128:131]\n"
    "s_nop 0\n"
    "v_permlane16_swap_b32 v224, v225\n"
    "v_max_f32 v224, v224, v225\n"
    "v_mov_b32 v225, v224\n"
    "v_mfma_f32_16x16x32_bf16 v[128:131], a[124:127], a[12:15], v[128:131]\n"
    "s_nop 0\n"
    "v_permlane32_swap_b32 v224, v225\n"
    "v_max_f32 v224, v224, v225\n"
    "v_mfma_f32_16x16x32_bf16 v[132:135], a[128:131], a[0:3], v[188:191]\n"
    "v_sub_f32 v160, v160, v224\n"
    "v_sub_f32 v161, v161, v224\n"
    "v_sub_f32 v162, v162, v224\n"
    "v_mfma_f32_16x16x32_bf16 v[132:135], a[132:135], a[4:7], v[132:135]\n"
    "v_sub_f32 v163, v163, v224\n"
    "v_sub_f32 v164, v164, v224\n"
    "v_mfma_f32_16x16x32_bf16 v[132:135], a[136:139], a[8:11], v[132:135]\n"
    "v_mov_b32 v215, v208\n"
    "v_sub_f32 v165, v165, v224\n"
    "v_sub_f32 v166, v166, v224\n"
    "v_mfma_f32_16x16x32_bf16 v[132:135], a[140:143], a[12:15], v[132:135]\n"
    "v_sub_f32 v167, v167, v224\n"
    "v_sub_f32 v204, 0, v224\n"
    "v_sub_f32 v205, 0, v224\n"
    "v_mfma_f32_16x16x32_bf16 v[96:99], a[144:147], v[180:183], 0\n"
    "v_sub_f32 v206, 0, v224\n"
    "v_sub_f32 v207, 0, v224\n"
    "v_mfma_f32_16x16x32_bf16 v[100:103], a[148:151], v[180:183], 0\n"
    "v_exp_f32 v218, v160\n"
    "v_exp_f32 v219, v161\n"
    "v_exp_f32 v220, v162\n"
    "v_mfma_f32_16x16x32_bf16 v[104:107], a[152:155], v[180:183], 0\n"
    "v_exp_f32 v221, v163\n"
    "v_cvt_pk_bf16_f32 v184, v218, v219\n"
    "v_mfma_f32_16x16x32_bf16 v[108:111], a[156:159], v[180:183], 0\n"
    "v_exp_f32 v222, v164\n"
    "v_exp_f32 v223, v165\n"
    "v_cvt_pk_bf16_f32 v185, v220, v221\n"
    "v_mfma_f32_16x16x32_bf16 v[112:115], a[160:163], v[180:183], 0\n"
    "v_exp_f32 v218, v166\n"
    "v_exp_f32 v219, v167\n"
    "v_mfma_f32_16x16x32_bf16 v[116:119], a[164:167], v[180:183], 0\n"
    "v_cvt_pk_bf16_f32 v186, v222, v223\n"
    "v_cvt_pk_bf16_f32 v187, v218, v219\n"
    "v_mfma_f32_16x16x32_bf16 v[120:123], a[168:171], v[180:183], 0\n"
    "v_mfma_f32_16x16x32_bf16 v[124:127], a[172:175], v[180:183], 0\n"
    "v_mfma_f32_16x16x32_bf16 v[208:211], a[240:243], v[184:187], 0\n"
    "; ---- ES1\n"
    "v_mfma_f32_16x16x32_bf16 v[136:139], a[112:115], a[16:19], v[192:195]\n"
    "s_waitcnt vmcnt(0) lgkmcnt(0)\n"
    "s_barrier\n"
    "v_mfma_f32_16x16x32_bf16 v[136:139], a[116:119], a[20:23], v[136:139]\n"
    "v_exp_f32 v218, v128\n"
    "v_mfma_f32_16x16x32_bf16 v[136:139], a[120:123], a[24:27], v[136:139]\n"
    "v_exp_f32 v219, v129\n"
    "v_mfma_f32_16x16x32_bf16 v[136:139], a[124:127], a[28:31], v[136:139]\n"
    "ds_read_b128 a[80:83], v246 offset:32768\n"
    "ds_read_b128 a[84:87], v246 offset:33024\n"
    "v_mfma_f32_16x16x32_bf16 v[140:143], a[128:131], a[16:19], v[192:195]\n"
    "v_exp_f32 v220, v130\n"
    "v_mfma_f32_16x16x32_bf16 v[140:143], a[132:135], a[20:23], v[140:143]\n"
    "v_exp_f32 v221, v131\n"
    "v_mfma_f32_16x16x32_bf16 v[140:143], a[136:139], a[24:27], v[140:143]\n"
    "v_mov_b32 v216, v208\n"
    "v_cvt_pk_bf16_f32 v168, v218, v219\n"
    "v_mfma_f32_16x16x32_bf16 v[140:143], a[140:143], a[28:31], v[140:143]\n"
    "v_exp_f32 v222, v132\n"
    "v_mfma_f32_16x16x32_bf16 a[208:211], a[144:147], v[184:187], 0\n"
    "ds_read_b128 a[88:91], v247 offset:32768\n"
    "ds_read_b128 a[92:95], v247 offset:33024\n"
    "v_mfma_f32_16x16x32_bf16 a[212:215], a[148:151], v[184:187], 0\n"
    "v_exp_f32 v223, v133\n"
    "v_mfma_f32_16x16x32_bf16 a[216:219], a[152:155], v[184:187], 0\n"
    "v_cvt_pk_bf16_f32 v169, v220, v221\n"
    "ds_read_b128 a[96:99], v246 offset:36864\n"
    "v_mfma_f32_16x16x32_bf16 a[220:223], a[156:159], v[184:187], 0\n"
    "v_exp_f32 v218, v134\n"
    "v_mfma_f32_16x16x32_bf16 a[224:227], a[160:163], v[184:187], 0\n"
    "v_exp_f32 v219, v135\n"
    "v_mfma_f32_16x16x32_bf16 a[228:231], a[164:167], v[184:187], 0\n"
    "v_cvt_pk_bf16_f32 v170, v222, v223\n"
    "v_cvt_pk_bf16_f32 v171, v218, v219\n"
    "v_mfma_f32_16x16x32_bf16 a[232:235], a[168:171], v[184:187], 0\n"
    "ds_read_b128 a[100:103], v246 offset:37120\n"
    "v_mfma_f32_16x16x32_bf16 a[236:239], a[172:175], v[184:187], 0\n"
    "ds_read_b128 a[104:107], v247 offset:36864\n"
    "v_mfma_f32_16x16x32_bf16 v[208:211], a[240:243], v[168:171], 0\n"
    "v_mfma_f32_16x16x32_bf16 v[144:147], a[112:115], a[32:35], v[196:199]\n"
    "v_exp_f32 v218, v136\n"
    "v_mfma_f32_16x16x32_bf16 v[144:147], a[116:119], a[36:39], v[144:147]\n"
    "v_exp_f32 v219, v137\n"
    "v_mfma_f32_16x16x32_bf16 v[144:147], a[120:123], a[40:43], v[144:147]\n"
    "v_exp_f32 v220, v138\n"
    "v_mfma_f32_16x16x32_bf16 v[144:147], a[124:127], a[44:47], v[144:147]\n"
    "ds_read_b128 a[108:111], v247 offset:37120\n"
    "v_mfma_f32_16x16x32_bf16 v[148:151], a[128:131], a[32:35], v[196:199]\n"
    "v_cmp_nge_f32 vcc, s70, v208\n"
    "ds_read_b64_tr_b16 a[144:145], v248 offset:32768\n"
    "v_mfma_f32_16x16x32_bf16 v[148:151], a[132:135], a[36:39], v[148:151]\n"
    "v_exp_f32 v221, v139\n"
    "v_mfma_f32_16x16x32_bf16 v[148:151], a[136:139], a[40:43], v[148:151]\n"
    "s_cbranch_vccnz Lstub0_b_1\n"
    "Lback0_b_1:\n"
    "v_add_f32 v212, v212, v208\n"
    "v_mfma_f32_16x16x32_bf16 v[148:151], a[140:143], a[44:47], v[148:151]\n"
    "v_cvt_pk_bf16_f32 v172, v218, v219\n"
    "ds_read_b64_tr_b16 a[146:147], v248 offset:36864\n"
    "v_mfma_f32_16x16x32_bf16 v[0:3], a[176:179], v[168:171], v[0:3]\n"
    "v_exp_f32 v222, v140\n"
    "v_mfma_f32_16x16x32_bf16 v[4:7], a[180:183], v[168:171], v[4:7]\n"
    "v_exp_f32 v223, v141\n"
    "v_mfma_f32_16x16x32_bf16 v[8:11], a[184:187], v[168:171], v[8:11]\n"
    "v_cvt_pk_bf16_f32 v173, v220, v221\n"
    "ds_read_b64_tr_b16 a[148:149], v249 offset:32768\n"
    "v_mfma_f32_16x16x32_bf16 v[12:15], a[188:191], v[168:171], v[12:15]\n"
    "v_exp_f32 v218, v142\n"
    "v_mfma_f32_16x16x32_bf16 v[16:19], a[192:195], v[168:171], v[16:19]\n"
    "v_exp_f32 v219, v143\n"
    "v_mfma_f32_16x16x32_bf16 v[20:23], a[196:199], v[168:171], v[20:23]\n"
    "v_cvt_pk_bf16_f32 v174, v222, v223\n"
    "v_cvt_pk_bf16_f32 v175, v218, v219\n"
    "v_mfma_f32_16x16x32_bf16 v[24:27], a[200:203], v[168:171], v[24:27]\n"
    "ds_read_b64_tr_b16 a[150:151], v249 offset:36864\n"
    "v_mfma_f32_16x16x32_bf16 v[28:31], a[204:207], v[168:171], v[28:31]\n"
    "v_mfma_f32_16x16x32_bf16 v[208:211], a[240:243], v[172:175], 0\n"
    "v_mfma_f32_16x16x32_bf16 v[152:155], a[112:115], a[48:51], v[200:203]\n"
    "v_exp_f32 v218, v144\n"
    "v_mfma_f32_16x16x32_bf16 v[152:155], a[116:119], a[52:55], v[152:155]\n"
    "v_exp_f32 v219, v145\n"
    "v_mfma_f32_16x16x32_bf16 v[152:155], a[120:123], a[56:59], v[152:155]\n"
    "ds_read_b64_tr_b16 a[152:153], v248 offset:33024\n"
    "v_mfma_f32_16x16x32_bf16 v[152:155], a[124:127], a[60:63], v[152:155]\n"
    "v_exp_f32 v220, v146\n"
    "v_mfma_f32_16x16x32_bf16 v[156:159], a[128:131], a[48:51], v[200:203]\n"
    "v_cmp_nge_f32 vcc, s70, v208\n"
    "ds_read_b64_tr_b16 a[154:155], v248 offset:37120\n"
    "v_mfma_f32_16x16x32_bf16 v[156:159], a[132:135], a[52:55], v[156:159]\n"
    "v_exp_f32 v221, v147\n"
    "v_mfma_f32_16x16x32_bf16 v[156:159], a[136:139], a[56:59], v[156:159]\n"
    "s_cbranch_vccnz Lstub0_b_2\n"
    "Lback0_b_2:\n"
    "v_add_f32 v213, v213, v208\n"
    "v_mfma_f32_16x16x32_bf16 v[156:159], a[140:143], a[60:63], v[156:159]\n"
    "v_cvt_pk_bf16_f32 v176, v218, v219\n"
    "ds_read_b64_tr_b16 a[156:157], v249 offset:33024\n"
    "v_mfma_f32_16x16x32_bf16 v[32:35], a[176:179], v[172:175], v[32:35]\n"
    "v_exp_f32 v222, v148\n"
    "v_mfma_f32_16x16x32_bf16 v[36:39], a[180:183], v[172:175], v[36:39]\n"
    "v_exp_f32 v223, v149\n"
    "v_mfma_f32_16x16x32_bf16 v[40:43], a[184:187], v[172:175], v[40:43]\n"
    "v_cvt_pk_bf16_f32 v177, v220, v221\n"
    "ds_read_b64_tr_b16 a[158:159], v249 offset:37120\n"
    "v_mfma_f32_16x16x32_bf16 v[44:47], a[188:191], v[172:175], v[44:47]\n"
    "v_exp_f32 v218, v150\n"
    "v_mfma_f32_16x16x32_bf16 v[48:51], a[192:195], v[172:175], v[48:51]\n"
    "v_exp_f32 v219, v151\n"
    "v_mfma_f32_16x16x32_bf16 v[52:55], a[196:199], v[172:175], v[52:55]\n"
    "v_cvt_pk_bf16_f32 v178, v222, v223\n"
    "v_cvt_pk_bf16_f32 v179, v218, v219\n"
    "v_mfma_f32_16x16x32_bf16 v[56:59], a[200:203], v[172:175], v[56:59]\n"
    "ds_read_b64_tr_b16 a[160:161], v248 offset:33280\n"
    "v_mfma_f32_16x16x32_bf16 v[60:63], a[204:207], v[172:175], v[60:63]\n"
    "v_mfma_f32_16x16x32_bf16 v[208:211], a[240:243], v[176:179], 0\n"
    "ds_read_b64_tr_b16 a[162:163], v248 offset:37376\n"
    "v_mfma_f32_16x16x32_bf16 v[160:163], a[112:115], a[64:67], v[204:207]\n"
    "v_exp_f32 v218, v152\n"
    "v_mfma_f32_16x16x32_bf16 v[160:163], a[116:119], a[68:71], v[160:163]\n"
    "v_exp_f32 v219, v153\n"
    "v_mfma_f32_16x16x32_bf16 v[160:163], a[120:123], a[72:75], v[160:163]\n"
    "v_exp_f32 v220, v154\n"
    "v_mfma_f32_16x16x32_bf16 v[160:163], a[124:127], a[76:79], v[160:163]\n"
    "v_exp_f32 v221, v155\n"
    "v_mfma_f32_16x16x32_bf16 v[164:167], a[128:131], a[64:67], v[204:207]\n"
    "v_cmp_nge_f32 vcc, s70, v208\n"
    "v_cvt_pk_bf16_f32 v180, v218, v219\n"
    "v_mfma_f32_16x16x32_bf16 v[164:167], a[132:135], a[68:71], v[164:167]\n"
    "ds_read_b64_tr_b16 a[164:165], v249 offset:33280\n"
    "v_mfma_f32_16x16x32_bf16 v[164:167], a[136:139], a[72:75], v[164:167]\n"
    "s_cbranch_vccnz Lstub0_b_3\n"
    "Lback0_b_3:\n"
    "v_add_f32 v214, v214, v208\n"
    "v_mfma_f32_16x16x32_bf16 v[164:167], a[140:143], a[76:79], v[164:167]\n"
    "v_exp_f32 v222, v156\n"
    "v_mfma_f32_16x16x32_bf16 v[64:67], a[176:179], v[176:179], v[64:67]\n"
    "v_exp_f32 v223, v157\n"
    "v_mfma_f32_16x16x32_bf16 v[68:71], a[180:183], v[176:179], v[68:71]\n"
    "ds_read_b64_tr_b16 a[166:167], v249 offset:37376\n"
    "v_cvt_pk_bf16_f32 v181, v220, v221\n"
    "v_mfma_f32_16x16x32_bf16 v[72:75], a[184:187], v[176:179], v[72:75]\n"
    "v_exp_f32 v218, v158\n"
    "v_mfma_f32_16x16x32_bf16 v[76:79], a[188:191], v[176:179], v[76:79]\n"
    "v_exp_f32 v219, v159\n"
    "v_mfma_f32_16x16x32_bf16 v[80:83], a[192:195], v[176:179], v[80:83]\n"
    "ds_read_b64_tr_b16 a[168:169], v248 offset:33536\n"
    "v_cvt_pk_bf16_f32 v182, v222, v223\n"
    "v_mfma_f32_16x16x32_bf16 v[84:87], a[196:199], v[176:179], v[84:87]\n"
    "v_cvt_pk_bf16_f32 v183, v218, v219\n"
    "ds_read_b64_tr_b16 a[170:171], v248 offset:37632\n"
    "v_mfma_f32_16x16x32_bf16 v[88:91], a[200:203], v[176:179], v[88:91]\n"
    "v_mfma_f32_16x16x32_bf16 v[92:95], a[204:207], v[176:179], v[92:95]\n"
    "v_mfma_f32_16x16x32_bf16 v[208:211], a[240:243], v[180:183], 0\n"
    "s_waitcnt lgkmcnt(14)\n"
    "ds_read_b64_tr_b16 a[172:173], v249 offset:33536\n"
    "v_mfma_f32_16x16x32_bf16 v[128:131], a[80:83], a[0:3], v[188:191]\n"
    "v_exp_f32 v218, v160\n"
    "v_mfma_f32_16x16x32_bf16 v[128:131], a[84:87], a[4:7], v[128:131]\n"
    "v_exp_f32 v219, v161\n"
    "v_mfma_f32_16x16x32_bf16 v[128:131], a[88:91], a[8:11], v[128:131]\n"
    "v_exp_f32 v220, v162\n"
    "v_mfma_f32_16x16x32_bf16 v[128:131], a[92:95], a[12:15], v[128:131]\n"
    "v_exp_f32 v221, v163\n"
    "v_mfma_f32_16x16x32_bf16 v[132:135], a[96:99], a[0:3], v[188:191]\n"
    "v_cmp_nge_f32 vcc, s70, v208\n"
    "ds_read_b64_tr_b16 a[174:175], v249 offset:37632\n"
    "v_mfma_f32_16x16x32_bf16 v[132:135], a[100:103], a[4:7], v[132:135]\n"
    "v_cvt_pk_bf16_f32 v184, v218, v219\n"
    "v_mfma_f32_16x16x32_bf16 v[132:135], a[104:107], a[8:11], v[132:135]\n"
    "s_cbranch_vccnz Lstub0_b_4\n"
    "Lback0_b_4:\n"
    "v_add_f32 v215, v215, v208\n"
    "v_mfma_f32_16x16x32_bf16 v[132:135], a[108:111], a[12:15], v[132:135]\n"
    "v_exp_f32 v222, v164\n"
    "v_mfma_f32_16x16x32_bf16 v[96:99], a[176:179], v[180:183], v[96:99]\n"
    "v_exp_f32 v223, v165\n"
    "v_mfma_f32_16x16x32_bf16 v[100:103], a[180:183], v[180:183], v[100:103]\n"
    "v_cvt_pk_bf16_f32 v185, v220, v221\n"
    "v_mfma_f32_16x16x32_bf16 v[104:107], a[184:187], v[180:183], v[104:107]\n"
    "v_exp_f32 v218, v166\n"
    "v_mfma_f32_16x16x32_bf16 v[108:111], a[188:191], v[180:183], v[108:111]\n"
    "v_exp_f32 v219, v167\n"
    "v_mfma_f32_16x16x32_bf16 v[112:115], a[192:195], v[180:183], v[112:115]\n"
    "v_cvt_pk_bf16_f32 v186, v222, v223\n"
    "v_cvt_pk_bf16_f32 v187, v218, v219\n"
    "v_mfma_f32_16x16x32_bf16 v[116:119], a[196:199], v[180:183], v[116:119]\n"
    "v_mfma_f32_16x16x32_bf16 v[120:123], a[200:203], v[180:183], v[120:123]\n"
    "v_mfma_f32_16x16x32_bf16 v[124:127], a[204:207], v[180:183], v[124:127]\n"
    "v_mfma_f32_16x16x32_bf16 v[208:211], a[240:243], v[184:187], 0\n"
    "s_mov_b32 s63, 7\n"
    "Lloop0:\n"
    "; ---- ES6\n"
    "v_mfma_f32_16x16x32_bf16 v[136:139], a[80:83], a[16:19], v[192:195]\n"
    "s_waitcnt lgkmcnt(0)\n"
    "s_barrier\n"
    "v_mfma_f32_16x16x32_bf16 v[136:139], a[84:87], a[20:23], v[136:139]\n"
    "v_exp_f32 v218, v128\n"
    "v_mfma_f32_16x16x32_bf16 v[136:139], a[88:91], a[24:27], v[136:139]\n"
    "v_exp_f32 v219, v129\n"
    "v_mfma_f32_16x16x32_bf16 v[136:139], a[92:95], a[28:31], v[136:139]\n"
    "ds_read_b128 a[112:115], v246 offset:49152\n"
    "ds_read_b128 a[116:119], v246 offset:49408\n"
    "v_mfma_f32_16x16x32_bf16 v[140:143], a[96:99], a[16:19], v[192:195]\n"
    "v_cmp_nge_f32 vcc, s70, v208\n"
    "v_mfma_f32_16x16x32_bf16 v[140:143], a[100:103], a[20:23], v[140:143]\n"
    "v_exp_f32 v220, v130\n"
    "v_mfma_f32_16x16x32_bf16 v[140:143], a[104:107], a[24:27], v[140:143]\n"
    "s_cbranch_vccnz Lstub0_2_0\n"
    "Lback0_2_0:\n"
    "v_add_f32 v216, v216, v208\n"
    "v_mfma_f32_16x16x32_bf16 v[140:143], a[108:111], a[28:31], v[140:143]\n"
    "v_exp_f32 v221, v131\n"
    "v_mfma_f32_16x16x32_bf16 a[208:211], a[176:179], v[184:187], a[208:211]\n"
    "v_cvt_pk_bf16_f32 v168, v218, v219\n"
    "ds_read_b128 a[120:123], v247 offset:49152\n"
    "v_mfma_f32_16x16x32_bf16 a[212:215], a[180:183], v[184:187], a[212:215]\n"
    "v_exp_f32 v222, v132\n"
    "v_mfma_f32_16x16x32_bf16 a[216:219], a[184:187], v[184:187], a[216:219]\n"
    "v_exp_f32 v223, v133\n"
    "v_mfma_f32_16x16x32_bf16 a[220:223], a[188:191], v[184:187], a[220:223]\n"
    "v_cvt_pk_bf16_f32 v169, v220, v221\n"
    "ds_read_b128 a[124:127], v247 offset:49408\n"
    "v_mfma_f32_16x16x32_bf16 a[224:227], a[192:195], v[184:187], a[224:227]\n"
    "v_exp_f32 v218, v134\n"
    "v_mfma_f32_16x16x32_bf16 a[228:231], a[196:199], v[184:187], a[228:231]\n"
    "v_exp_f32 v219, v135\n"
    "v_mfma_f32_16x16x32_bf16 a[232:235], a[200:203], v[184:187], a[232:235]\n"
    "v_cvt_pk_bf16_f32 v170, v222, v223\n"
    "ds_read_b128 a[128:131], v246 offset:53248\n"
    "v_mfma_f32_16x16x32_bf16 a[236:239], a[204:207], v[184:187], a[236:239]\n"
    "v_cvt_pk_bf16_f32 v171, v218, v219\n"
    "ds_read_b128 a[132:135], v246 offset:53504\n"
    "s_nop 0\n"
    "v_mfma_f32_16x16x32_bf16 v[208:211], a[240:243], v[168:171], 0\n"
    "ds_read_b128 a[136:139], v247 offset:53248\n"
    "v_mfma_f32_16x16x32_bf16 v[144:147], a[80:83], a[32:35], v[196:199]\n"
    "v_exp_f32 v218, v136\n"
    "v_mfma_f32_16x16x32_bf16 v[144:147], a[84:87], a[36:39], v[144:147]\n"
    "v_exp_f32 v219, v137\n"
    "v_mfma_f32_16x16x32_bf16 v[144:147], a[88:91], a[40:43], v[144:147]\n"
    "v_exp_f32 v220, v138\n"
    "v_mfma_f32_16x16x32_bf16 v[144:147], a[92:95], a[44:47], v[144:147]\n"
    "ds_read_b128 a[140:143], v247 offset:53504\n"
    "v_mfma_f32_16x16x32_bf16 v[148:151], a[96:99], a[32:35], v[196:199]\n"
    "v_cmp_nge_f32 vcc, s70, v208\n"
    "ds_read_b64_tr_b16 a[176:177], v248 offset:49152\n"
    "v_mfma_f32_16x16x32_bf16 v[148:151], a[100:103], a[36:39], v[148:151]\n"
    "v_exp_f32 v221, v139\n"
    "v_mfma_f32_16x16x32_bf16 v[148:151], a[104:107], a[40:43], v[148:151]\n"
    "s_cbranch_vccnz Lstub0_2_1\n"
    "Lback0_2_1:\n"
    "v_add_f32 v212, v212, v208\n"
    "v_mfma_f32_16x16x32_bf16 v[148:151], a[108:111], a[44:47], v[148:151]\n"
    "v_cvt_pk_bf16_f32 v172, v218, v219\n"
    "ds_read_b64_tr_b16 a[178:179], v248 offset:53248\n"
    "v_mfma_f32_16x16x32_bf16 v[0:3], a[144:147], v[168:171], v[0:3]\n"
    "v_exp_f32 v222, v140\n"
    "v_mfma_f32_16x16x32_bf16 v[4:7], a[148:151], v[168:171], v[4:7]\n"
    "v_exp_f32 v223, v141\n"
    "v_mfma_f32_16x16x32_bf16 v[8:11], a[152:155], v[168:171], v[8:11]\n"
    "v_cvt_pk_bf16_f32 v173, v220, v221\n"
    "ds_read_b64_tr_b16 a[180:181], v249 offset:49152\n"
    "v_mfma_f32_16x16x32_bf16 v[12:15], a[156:159], v[168:171], v[12:15]\n"
    "v_exp_f32 v218, v142\n"
    "v_mfma_f32_16x16x32_bf16 v[16:19], a[160:163], v[168:171], v[16:19]\n"
    "v_exp_f32 v219, v143\n"
    "v_mfma_f32_16x16x32_bf16 v[20:23], a[164:167], v[168:171], v[20:23]\n"
    "v_cvt_pk_bf16_f32 v174, v222, v223\n"
    "v_cvt_pk_bf16_f32 v175, v218, v219\n"
    "v_mfma_f32_16x16x32_bf16 v[24:27], a[168:171], v[168:171], v[24:27]\n"
    "ds_read_b64_tr_b16 a[182:183], v249 offset:53248\n"
    "v_mfma_f32_16x16x32_bf16 v[28:31], a[172:175], v[168:171], v[28:31]\n"
    "v_mfma_f32_16x16x32_bf16 v[208:211], a[240:243], v[172:175], 0\n"
    "v_mfma_f32_16x16x32_bf16 v[152:155], a[80:83], a[48:51], v[200:203]\n"
    "v_exp_f32 v218, v144\n"
    "v_mfma_f32_16x16x32_bf16 v[152:155], a[84:87], a[52:55], v[152:155]\n"
    "v_exp_f32 v219, v145\n"
    "v_mfma_f32_16x16x32_bf16 v[152:155], a[88:91], a[56:59], v[152:155]\n"
    "ds_read_b64_tr_b16 a[184:185], v248 offset:49408\n"
    "v_mfma_f32_16x16x32_bf16 v[152:155], a[92:95], a[60:63], v[152:155]\n"
    "v_exp_f32 v220, v146\n"
    "v_mfma_f32_16x16x32_bf16 v[156:159], a[96:99], a[48:51], v[200:203]\n"
    "v_cmp_nge_f32 vcc, s70, v208\n"
    "ds_read_b64_tr_b16 a[186:187], v248 offset:53504\n"
    "v_mfma_f32_16x16x32_bf16 v[156:159], a[100:103], a[52:55], v[156:159]\n"
    "v_exp_f32 v221, v147\n"
    "v_mfma_f32_16x16x32_bf16 v[156:159], a[104:107], a[56:59], v[156:159]\n"
    "s_cbranch_vccnz Lstub0_2_2\n"
    "Lback0_2_2:\n"
    "v_add_f32 v213, v213, v208\n"
    "v_mfma_f32_16x16x32_bf16 v[156:159], a[108:111], a[60:63], v[156:159]\n"
    "v_cvt_pk_bf16_f32 v176, v218, v219\n"
    "ds_read_b64_tr_b16 a[188:189], v249 offset:49408\n"
    "v_mfma_f32_16x16x32_bf16 v[32:35], a[144:147], v[172:175], v[32:35]\n"
    "v_exp_f32 v222, v148\n"
    "v_mfma_f32_16x16x32_bf16 v[36:39], a[148:151], v[172:175], v[36:39]\n"
    "v_exp_f32 v223, v149\n"
    "v_mfma_f32_16x16x32_bf16 v[40:43], a[152:155], v[172:175], v[40:43]\n"
    "v_cvt_pk_bf16_f32 v177, v220, v221\n"
    "ds_read_b64_tr_b16 a[190:191], v249 offset:53504\n"
    "v_mfma_f32_16x16x32_bf16 v[44:47], a[156:159], v[172:175], v[44:47]\n"
    "v_exp_f32 v218, v150\n"
    "v_mfma_f32_16x16x32_bf16 v[48:51], a[160:163], v[172:175], v[48:51]\n"
    "v_exp_f32 v219, v151\n"
    "v_mfma_f32_16x16x32_bf16 v[52:55], a[164:167], v[172:175], v[52:55]\n"
    "v_cvt_pk_bf16_f32 v178, v222, v223\n"
    "v_cvt_pk_bf16_f32 v179, v218, v219\n"
    "v_mfma_f32_16x16x32_bf16 v[56:59], a[168:171], v[172:175], v[56:59]\n"
    "ds_read_b64_tr_b16 a[192:193], v248 offset:49664\n"
    "v_mfma_f32_16x16x32_bf16 v[60:63], a[172:175], v[172:175], v[60:63]\n"
    "v_mfma_f32_16x16x32_bf16 v[208:211], a[240:243], v[176:179], 0\n"
    "ds_read_b64_tr_b16 a[194:195], v248 offset:53760\n"
    "v_mfma_f32_16x16x32_bf16 v[160:163], a[80:83], a[64:67], v[204:207]\n"
    "v_exp_f32 v218, v152\n"
    "v_mfma_f32_16x16x32_bf16 v[160:163], a[84:87], a[68:71], v[160:163]\n"
    "v_exp_f32 v219, v153\n"
    "v_mfma_f32_16x16x32_bf16 v[160:163], a[88:91], a[72:75], v[160:163]\n"
    "v_exp_f32 v220, v154\n"
    "v_mfma_f32_16x16x32_bf16 v[160:163], a[92:95], a[76:79], v[160:163]\n"
    "v_exp_f32 v221, v155\n"
    "v_mfma_f32_16x16x32_bf16 v[164:167], a[96:99], a[64:67], v[204:207]\n"
    "v_cmp_nge_f32 vcc, s70, v208\n"
    "v_cvt_pk_bf16_f32 v180, v218, v219\n"
    "v_mfma_f32_16x16x32_bf16 v[164:167], a[100:103], a[68:71], v[164:167]\n"
    "ds_read_b64_tr_b16 a[196:197], v249 offset:49664\n"
    "v_mfma_f32_16x16x32_bf16 v[164:167], a[104:107], a[72:75], v[164:167]\n"
    "s_cbranch_vccnz Lstub0_2_3\n"
    "Lback0_2_3:\n"
    "v_add_f32 v214, v214, v208\n"
    "v_mfma_f32_16x16x32_bf16 v[164:167], a[108:111], a[76:79], v[164:167]\n"
    "v_exp_f32 v222, v156\n"
    "v_mfma_f32_16x16x32_bf16 v[64:67], a[144:147], v[176:179], v[64:67]\n"
    "v_exp_f32 v223, v157\n"
    "v_mfma_f32_16x16x32_bf16 v[68:71], a[148:151], v[176:179], v[68:71]\n"
    "ds_read_b64_tr_b16 a[198:199], v249 offset:53760\n"
    "v_cvt_pk_bf16_f32 v181, v220, v221\n"
    "v_mfma_f32_16x16x32_bf16 v[72:75], a[152:155], v[176:179], v[72:75]\n"
    "v_exp_f32 v218, v158\n"
    "v_mfma_f32_16x16x32_bf16 v[76:79], a[156:159], v[176:179], v[76:79]\n"
    "v_exp_f32 v219, v159\n"
    "v_mfma_f32_16x16x32_bf16 v[80:83], a[160:163], v[176:179], v[80:83]\n"
    "ds_read_b64_tr_b16 a[200:201], v248 offset:49920\n"
    "v_cvt_pk_bf16_f32 v182, v222, v223\n"
    "v_mfma_f32_16x16x32_bf16 v[84:87], a[164:167], v[176:179], v[84:87]\n"
    "v_cvt_pk_bf16_f32 v183, v218, v219\n"
    "ds_read_b64_tr_b16 a[202:203], v248 offset:54016\n"
    "v_mfma_f32_16x16x32_bf16 v[88:91], a[168:171], v[176:179], v[88:91]\n"
    "v_mfma_f32_16x16x32_bf16 v[92:95], a[172:175], v[176:179], v[92:95]\n"
    "v_mfma_f32_16x16x32_bf16 v[208:211], a[240:243], v[180:183], 0\n"
    "s_waitcnt lgkmcnt(14)\n"
    "ds_read_b64_tr_b16 a[204:205], v249 offset:49920\n"
    "v_mfma_f32_16x16x32_bf16 v[128:131], a[112:115], a[0:3], v[188:191]\n"
    "v_exp_f32 v218, v160\n"
    "v_mfma_f32_16x16x32_bf16 v[128:131], a[116:119], a[4:7], v[128:131]\n"
    "v_exp_f32 v219, v161\n"
    "v_mfma_f32_16x16x32_bf16 v[128:131], a[120:123], a[8:11], v[128:131]\n"
    "v_exp_f32 v220, v162\n"
    "v_mfma_f32_16x16x32_bf16 v[128:131], a[124:127], a[12:15], v[128:131]\n"
    "v_exp_f32 v221, v163\n"
    "v_mfma_f32_16x16x32_bf16 v[132:135], a[128:131], a[0:3], v[188:191]\n"
    "v_cmp_nge_f32 vcc, s70, v208\n"
    "ds_read_b64_tr_b16 a[206:207], v249 offset:54016\n"
    "v_mfma_f32_16x16x32_bf16 v[132:135], a[132:135], a[4:7], v[132:135]\n"
    "v_cvt_pk_bf16_f32 v184, v218, v219\n"
    "v_mfma_f32_16x16x32_bf16 v[132:135], a[136:139], a[8:11], v[132:135]\n"
    "s_cbranch_vccnz Lstub0_2_4\n"
    "Lback0_2_4:\n"
    "v_add_f32 v215, v215, v208\n"
    "v_mfma_f32_16x16x32_bf16 v[132:135], a[140:143], a[12:15], v[132:135]\n"
    "v_exp_f32 v222, v164\n"
    "v_mfma_f32_16x16x32_bf16 v[96:99], a[144:147], v[180:183], v[96:99]\n"
    "v_exp_f32 v223, v165\n"
    "v_mfma_f32_16x16x32_bf16 v[100:103], a[148:151], v[180:183], v[100:103]\n"
    "v_cvt_pk_bf16_f32 v185, v220, v221\n"
    "v_mfma_f32_16x16x32_bf16 v[104:107], a[152:155], v[180:183], v[104:107]\n"
    "v_exp_f32 v218, v166\n"
    "v_mfma_f32_16x16x32_bf16 v[108:111], a[156:159], v[180:183], v[108:111]\n"
    "v_exp_f32 v219, v167\n"
    "v_mfma_f32_16x16x32_bf16 v[112:115], a[160:163], v[180:183], v[112:115]\n"
    "v_cvt_pk_bf16_f32 v186, v222, v223\n"
    "v_cvt_pk_bf16_f32 v187, v218, v219\n"
    "v_mfma_f32_16x16x32_bf16 v[116:119], a[164:167], v[180:183], v[116:119]\n"
    "v_mfma_f32_16x16x32_bf16 v[120:123], a[168:171], v[180:183], v[120:123]\n"
    "v_mfma_f32_16x16x32_bf16 v[124:127], a[172:175], v[180:183], v[124:127]\n"
    "v_mfma_f32_16x16x32_bf16 v[208:211], a[240:243], v[184:187], 0\n"
    "; ---- ES7\n"
    "v_mfma_f32_16x16x32_bf16 v[136:139], a[112:115], a[16:19], v[192:195]\n"
    "s_waitcnt lgkmcnt(0)\n"
    "s_barrier\n"
    "v_mfma_f32_16x16x32_bf16 v[136:139], a[116:119], a[20:23], v[136:139]\n"
    "v_exp_f32 v218, v128\n"
    "v_mfma_f32_16x16x32_bf16 v[136:139], a[120:123], a[24:27], v[136:139]\n"
    "v_exp_f32 v219, v129\n"
    "v_mfma_f32_16x16x32_bf16 v[136:139], a[124:127], a[28:31], v[136:139]\n"
    "ds_read_b128 a[80:83], v246\n"
    "ds_read_b128 a[84:87], v246 offset:256\n"
    "v_mfma_f32_16x16x32_bf16 v[140:143], a[128:131], a[16:19], v[192:195]\n"
    "v_cmp_nge_f32 vcc, s70, v208\n"
    "v_mfma_f32_16x16x32_bf16 v[140:143], a[132:135], a[20:23], v[140:143]\n"
    "v_exp_f32 v220, v130\n"
    "v_mfma_f32_16x16x32_bf16 v[140:143], a[136:139], a[24:27], v[140:143]\n"
    "s_cbranch_vccnz Lstub0_3_0\n"
    "Lback0_3_0:\n"
    "v_add_f32 v216, v216, v208\n"
    "v_mfma_f32_16x16x32_bf16 v[140:143], a[140:143], a[28:31], v[140:143]\n"
    "v_exp_f32 v221, v131\n"
    "v_mfma_f32_16x16x32_bf16 a[208:211], a[144:147], v[184:187], a[208:211]\n"
    "v_cvt_pk_bf16_f32 v168, v218, v219\n"
    "ds_read_b128 a[88:91], v247\n"
    "v_mfma_f32_16x16x32_bf16 a[212:215], a[148:151], v[184:187], a[212:215]\n"
    "v_exp_f32 v222, v132\n"
    "v_mfma_f32_16x16x32_bf16 a[216:219], a[152:155], v[184:187], a[216:219]\n"
    "v_exp_f32 v223, v133\n"
    "v_mfma_f32_16x16x32_bf16 a[220:223], a[156:159], v[184:187], a[220:223]\n"
    "v_cvt_pk_bf16_f32 v169, v220, v221\n"
    "ds_read_b128 a[92:95], v247 offset:256\n"
    "v_mfma_f32_16x16x32_bf16 a[224:227], a[160:163], v[184:187], a[224:227]\n"
    "v_exp_f32 v218, v134\n"
    "v_mfma_f32_16x16x32_bf16 a[228:231], a[164:167], v[184:187], a[228:231]\n"
    "v_exp_f32 v219, v135\n"
    "v_mfma_f32_16x16x32_bf16 a[232:235], a[168:171], v[184:187], a[232:235]\n"
    "v_cvt_pk_bf16_f32 v170, v222, v223\n"
    "ds_read_b128 a[96:99], v246 offset:4096\n"
    "v_mfma_f32_16x16x32_bf16 a[236:239], a[172:175], v[184:187], a[236:239]\n"
    "v_cvt_pk_bf16_f32 v171, v218, v219\n"
    "ds_read_b128 a[100:103], v246 offset:4352\n"
    "s_nop 0\n"
    "v_mfma_f32_16x16x32_bf16 v[208:211], a[240:243], v[168:171], 0\n"
    "ds_read_b128 a[104:107], v247 offset:4096\n"
    "v_mfma_f32_16x16x32_bf16 v[144:147], a[112:115], a[32:35], v[196:199]\n"
    "v_exp_f32 v218, v136\n"
    "v_mfma_f32_16x16x32_bf16 v[144:147], a[116:119], a[36:39], v[144:147]\n"
    "v_exp_f32 v219, v137\n"
    "v_mfma_f32_16x16x32_bf16 v[144:147], a[120:123], a[40:43], v[144:147]\n"
    "v_exp_f32 v220, v138\n"
    "v_mfma_f32_16x16x32_bf16 v[144:147], a[124:127], a[44:47], v[144:147]\n"
    "ds_read_b128 a[108:111], v247 offset:4352\n"
    "v_mfma_f32_16x16x32_bf16 v[148:151], a[128:131], a[32:35], v[196:199]\n"
    "v_cmp_nge_f32 vcc, s70, v208\n"
    "ds_read_b64_tr_b16 a[144:145], v248\n"
    "v_mfma_f32_16x16x32_bf16 v[148:151], a[132:135], a[36:39], v[148:151]\n"
    "v_exp_f32 v221, v139\n"
    "v_mfma_f32_16x16x32_bf16 v[148:151], a[136:139], a[40:43], v[148:151]\n"
    "s_cbranch_vccnz Lstub0_3_1\n"
    "Lback0_3_1:\n"
    "v_add_f32 v212, v212, v208\n"
    "v_mfma_f32_16x16x32_bf16 v[148:151], a[140:143], a[44:47], v[148:151]\n"
    "v_cvt_pk_bf16_f32 v172, v218, v219\n"
    "ds_read_b64_tr_b16 a[146:147], v248 offset:4096\n"
    "v_mfma_f32_16x16x32_bf16 v[0:3], a[176:179], v[168:171], v[0:3]\n"
    "v_exp_f32 v222, v140\n"
    "v_mfma_f32_16x16x32_bf16 v[4:7], a[180:183], v[168:171], v[4:7]\n"
    "v_exp_f32 v223, v141\n"
    "v_mfma_f32_16x16x32_bf16 v[8:11], a[184:187], v[168:171], v[8:11]\n"
    "v_cvt_pk_bf16_f32 v173, v220, v221\n"
    "ds_read_b64_tr_b16 a[148:149], v249\n"
    "v_mfma_f32_16x16x32_bf16 v[12:15], a[188:191], v[168:171], v[12:15]\n"
    "v_exp_f32 v218, v142\n"
    "v_mfma_f32_16x16x32_bf16 v[16:19], a[192:195], v[168:171], v[16:19]\n"
    "v_exp_f32 v219, v143\n"
    "v_mfma_f32_16x16x32_bf16 v[20:23], a[196:199], v[168:171], v[20:23]\n"
    "v_cvt_pk_bf16_f32 v174, v222, v223\n"
    "v_cvt_pk_bf16_f32 v175, v218, v219\n"
    "v_mfma_f32_16x16x32_bf16 v[24:27], a[200:203], v[168:171], v[24:27]\n"
    "ds_read_b64_tr_b16 a[150:151], v249 offset:4096\n"
    "v_mfma_f32_16x16x32_bf16 v[28:31], a[204:207], v[168:171], v[28:31]\n"
    "v_mfma_f32_16x16x32_bf16 v[208:211], a[240:243], v[172:175], 0\n"
    "v_mfma_f32_16x16x32_bf16 v[152:155], a[112:115], a[48:51], v[200:203]\n"
    "v_exp_f32 v218, v144\n"
    "v_mfma_f32_16x16x32_bf16 v[152:155], a[116:119], a[52:55], v[152:155]\n"
    "v_exp_f32 v219, v145\n"
    "v_mfma_f32_16x16x32_bf16 v[152:155], a[120:123], a[56:59], v[152:155]\n"
    "ds_read_b64_tr_b16 a[152:153], v248 offset:256\n"
    "v_mfma_f32_16x16x32_bf16 v[152:155], a[124:127], a[60:63], v[152:155]\n"
    "v_exp_f32 v220, v146\n"
    "v_mfma_f32_16x16x32_bf16 v[156:159], a[128:131], a[48:51], v[200:203]\n"
    "v_cmp_nge_f32 vcc, s70, v208\n"
    "ds_read_b64_tr_b16 a[154:155], v248 offset:4352\n"
    "v_mfma_f32_16x16x32_bf16 v[156:159], a[132:135], a[52:55], v[156:159]\n"
    "v_exp_f32 v221, v147\n"
    "v_mfma_f32_16x16x32_bf16 v[156:159], a[136:139], a[56:59], v[156:159]\n"
    "s_cbranch_vccnz Lstub0_3_2\n"
    "Lback0_3_2:\n"
    "v_add_f32 v213, v213, v208\n"
    "v_mfma_f32_16x16x32_bf16 v[156:159], a[140:143], a[60:63], v[156:159]\n"
    "v_cvt_pk_bf16_f32 v176, v218, v219\n"
    "ds_read_b64_tr_b16 a[156:157], v249 offset:256\n"
    "v_mfma_f32_16x16x32_bf16 v[32:35], a[176:179], v[172:175], v[32:35]\n"
    "v_exp_f32 v222, v148\n"
    "v_mfma_f32_16x16x32_bf16 v[36:39], a[180:183], v[172:175], v[36:39]\n"
    "v_exp_f32 v223, v149\n"
    "v_mfma_f32_16x16x32_bf16 v[40:43], a[184:187], v[172:175], v[40:43]\n"
    "v_cvt_pk_bf16_f32 v177, v220, v221\n"
    "ds_read_b64_tr_b16 a[158:159], v249 offset:4352\n"
    "v_mfma_f32_16x16x32_bf16 v[44:47], a[188:191], v[172:175], v[44:47]\n"
    "v_exp_f32 v218, v150\n"
    "v_mfma_f32_16x16x32_bf16 v[48:51], a[192:195], v[172:175], v[48:51]\n"
    "v_exp_f32 v219, v151\n"
    "v_mfma_f32_16x16x32_bf16 v[52:55], a[196:199], v[172:175], v[52:55]\n"
    "v_cvt_pk_bf16_f32 v178, v222, v223\n"
    "v_cvt_pk_bf16_f32 v179, v218, v219\n"
    "v_mfma_f32_16x16x32_bf16 v[56:59], a[200:203], v[172:175], v[56:59]\n"
    "ds_read_b64_tr_b16 a[160:161], v248 offset:512\n"
    "v_mfma_f32_16x16x32_bf16 v[60:63], a[204:207], v[172:175], v[60:63]\n"
    "v_mfma_f32_16x16x32_bf16 v[208:211], a[240:243], v[176:179], 0\n"
    "ds_read_b64_tr_b16 a[162:163], v248 offset:4608\n"
    "v_mfma_f32_16x16x32_bf16 v[160:163], a[112:115], a[64:67], v[204:207]\n"
    "v_exp_f32 v218, v152\n"
    "v_mfma_f32_16x16x32_bf16 v[160:163], a[116:119], a[68:71], v[160:163]\n"
    "v_exp_f32 v219, v153\n"
    "v_mfma_f32_16x16x32_bf16 v[160:163], a[120:123], a[72:75], v[160:163]\n"
    "v_exp_f32 v220, v154\n"
    "v_mfma_f32_16x16x32_bf16 v[160:163], a[124:127], a[76:79], v[160:163]\n"
    "v_exp_f32 v221, v155\n"
    "v_mfma_f32_16x16x32_bf16 v[164:167], a[128:131], a[64:67], v[204:207]\n"
    "v_cmp_nge_f32 vcc, s70, v208\n"
    "v_cvt_pk_bf16_f32 v180, v218, v219\n"
    "v_mfma_f32_16x16x32_bf16 v[164:167], a[132:135], a[68:71], v[164:167]\n"
    "ds_read_b64_tr_b16 a[164:165], v249 offset:512\n"
    "v_mfma_f32_16x16x32_bf16 v[164:167], a[136:139], a[72:75], v[164:167]\n"
    "s_cbranch_vccnz Lstub0_3_3\n"
    "Lback0_3_3:\n"
    "v_add_f32 v214, v214, v208\n"
    "v_mfma_f32_16x16x32_bf16 v[164:167], a[140:143], a[76:79], v[164:167]\n"
    "v_exp_f32 v222, v156\n"
    "v_mfma_f32_16x16x32_bf16 v[64:67], a[176:179], v[176:179], v[64:67]\n"
    "v_exp_f32 v223, v157\n"
    "v_mfma_f32_16x16x32_bf16 v[68:71], a[180:183], v[176:179], v[68:71]\n"
    "ds_read_b64_tr_b16 a[166:167], v249 offset:4608\n"
    "v_cvt_pk_bf16_f32 v181, v220, v221\n"
    "v_mfma_f32_16x16x32_bf16 v[72:75], a[184:187], v[176:179], v[72:75]\n"
    "v_exp_f32 v218, v158\n"
    "v_mfma_f32_16x16x32_bf16 v[76:79], a[188:191], v[176:179], v[76:79]\n"
    "v_exp_f32 v219, v159\n"
    "v_mfma_f32_16x16x32_bf16 v[80:83], a[192:195], v[176:179], v[80:83]\n"
    "ds_read_b64_tr_b16 a[168:169], v248 offset:768\n"
    "v_cvt_pk_bf16_f32 v182, v222, v223\n"
    "v_mfma_f32_16x16x32_bf16 v[84:87], a[196:199], v[176:179], v[84:87]\n"
    "v_cvt_pk_bf16_f32 v183, v218, v219\n"
    "ds_read_b64_tr_b16 a[170:171], v248 offset:4864\n"
    "v_mfma_f32_16x16x32_bf16 v[88:91], a[200:203], v[176:179], v[88:91]\n"
    "v_mfma_f32_16x16x32_bf16 v[92:95], a[204:207], v[176:179], v[92:95]\n"
    "v_mfma_f32_16x16x32_bf16 v[208:211], a[240:243], v[180:183], 0\n"
    "s_waitcnt lgkmcnt(14)\n"
    "ds_read_b64_tr_b16 a[172:173], v249 offset:768\n"
    "v_mfma_f32_16x16x32_bf16 v[128:131], a[80:83], a[0:3], v[188:191]\n"
    "v_exp_f32 v218, v160\n"
    "v_mfma_f32_16x16x32_bf16 v[128:131], a[84:87], a[4:7], v[128:131]\n"
    "v_exp_f32 v219, v161\n"
    "v_mfma_f32_16x16x32_bf16 v[128:131], a[88:91], a[8:11], v[128:131]\n"
    "v_exp_f32 v220, v162\n"
    "v_mfma_f32_16x16x32_bf16 v[128:131], a[92:95], a[12:15], v[128:131]\n"
    "v_exp_f32 v221, v163\n"
    "v_mfma_f32_16x16x32_bf16 v[132:135], a[96:99], a[0:3], v[188:191]\n"
    "v_cmp_nge_f32 vcc, s70, v208\n"
    "ds_read_b64_tr_b16 a[174:175], v249 offset:4864\n"
    "v_mfma_f32_16x16x32_bf16 v[132:135], a[100:103], a[4:7], v[132:135]\n"
    "v_cvt_pk_bf16_f32 v184, v218, v219\n"
    "v_mfma_f32_16x16x32_bf16 v[132:135], a[104:107], a[8:11], v[132:135]\n"
    "s_cbranch_vccnz Lstub0_3_4\n"
    "Lback0_3_4:\n"
    "v_add_f32 v215, v215, v208\n"
    "v_mfma_f32_16x16x32_bf16 v[132:135], a[108:111], a[12:15], v[132:135]\n"
    "v_exp_f32 v222, v164\n"
    "v_mfma_f32_16x16x32_bf16 v[96:99], a[176:179], v[180:183], v[96:99]\n"
    "v_exp_f32 v223, v165\n"
    "v_mfma_f32_16x16x32_bf16 v[100:103], a[180:183], v[180:183], v[100:103]\n"
    "v_cvt_pk_bf16_f32 v185, v220, v221\n"
    "v_mfma_f32_16x16x32_bf16 v[104:107], a[184:187], v[180:183], v[104:107]\n"
    "v_exp_f32 v218, v166\n"
    "v_mfma_f32_16x16x32_bf16 v[108:111], a[188:191], v[180:183], v[108:111]\n"
    "v_exp_f32 v219, v167\n"
    "v_mfma_f32_16x16x32_bf16 v[112:115], a[192:195], v[180:183], v[112:115]\n"
    "v_cvt_pk_bf16_f32 v186, v222, v223\n"
    "v_cvt_pk_bf16_f32 v187, v218, v219\n"
    "v_mfma_f32_16x16x32_bf16 v[116:119], a[196:199], v[180:183], v[116:119]\n"
    "v_mfma_f32_16x16x32_bf16 v[120:123], a[200:203], v[180:183], v[120:123]\n"
    "v_mfma_f32_16x16x32_bf16 v[124:127], a[204:207], v[180:183], v[124:127]\n"
    "v_mfma_f32_16x16x32_bf16 v[208:211], a[240:243], v[184:187], 0\n"
    "; ---- ES8\n"
    "v_mfma_f32_16x16x32_bf16 v[136:139], a[80:83], a[16:19], v[192:195]\n"
    "s_waitcnt lgkmcnt(0)\n"
    "s_barrier\n"
    "v_mfma_f32_16x16x32_bf16 v[136:139], a[84:87], a[20:23], v[136:139]\n"
    "v_exp_f32 v218, v128\n"
    "v_mfma_f32_16x16x32_bf16 v[136:139], a[88:91], a[24:27], v[136:139]\n"
    "v_exp_f32 v219, v129\n"
    "v_mfma_f32_16x16x32_bf16 v[136:139], a[92:95], a[28:31], v[136:139]\n"
    "ds_read_b128 a[112:115], v246 offset:16384\n"
    "ds_read_b128 a[116:119], v246 offset:16640\n"
    "v_mfma_f32_16x16x32_bf16 v[140:143], a[96:99], a[16:19], v[192:195]\n"
    "v_cmp_nge_f32 vcc, s70, v208\n"
    "v_mfma_f32_16x16x32_bf16 v[140:143], a[100:103], a[20:23], v[140:143]\n"
    "v_exp_f32 v220, v130\n"
    "v_mfma_f32_16x16x32_bf16 v[140:143], a[104:107], a[24:27], v[140:143]\n"
    "s_cbranch_vccnz Lstub0_0_0\n"
    "Lback0_0_0:\n"
    "v_add_f32 v216, v216, v208\n"
    "v_mfma_f32_16x16x32_bf16 v[140:143], a[108:111], a[28:31], v[140:143]\n"
    "v_exp_f32 v221, v131\n"
    "v_mfma_f32_16x16x32_bf16 a[208:211], a[176:179], v[184:187], a[208:211]\n"
    "v_cvt_pk_bf16_f32 v168, v218, v219\n"
    "ds_read_b128 a[120:123], v247 offset:16384\n"
    "v_mfma_f32_16x16x32_bf16 a[212:215], a[180:183], v[184:187], a[212:215]\n"
    "v_exp_f32 v222, v132\n"
    "v_mfma_f32_16x16x32_bf16 a[216:219], a[184:187], v[184:187], a[216:219]\n"
    "v_exp_f32 v223, v133\n"
    "v_mfma_f32_16x16x32_bf16 a[220:223], a[188:191], v[184:187], a[220:223]\n"
    "v_cvt_pk_bf16_f32 v169, v220, v221\n"
    "ds_read_b128 a[124:127], v247 offset:16640\n"
    "v_mfma_f32_16x16x32_bf16 a[224:227], a[192:195], v[184:187], a[224:227]\n"
    "v_exp_f32 v218, v134\n"
    "v_mfma_f32_16x16x32_bf16 a[228:231], a[196:199], v[184:187], a[228:231]\n"
    "v_exp_f32 v219, v135\n"
    "v_mfma_f32_16x16x32_bf16 a[232:235], a[200:203], v[184:187], a[232:235]\n"
    "v_cvt_pk_bf16_f32 v170, v222, v223\n"
    "ds_read_b128 a[128:131], v246 offset:20480\n"
    "v_mfma_f32_16x16x32_bf16 a[236:239], a[204:207], v[184:187], a[236:239]\n"
    "v_cvt_pk_bf16_f32 v171, v218, v219\n"
    "ds_read_b128 a[132:135], v246 offset:20736\n"
    "s_nop 0\n"
    "v_mfma_f32_16x16x32_bf16 v[208:211], a[240:243], v[168:171], 0\n"
    "ds_read_b128 a[136:139], v247 offset:20480\n"
    "v_mfma_f32_16x16x32_bf16 v[144:147], a[80:83], a[32:35], v[196:199]\n"
    "v_exp_f32 v218, v136\n"
    "v_mfma_f32_16x16x32_bf16 v[144:147], a[84:87], a[36:39], v[144:147]\n"
    "v_exp_f32 v219, v137\n"
    "v_mfma_f32_16x16x32_bf16 v[144:147], a[88:91], a[40:43], v[144:147]\n"
    "v_exp_f32 v220, v138\n"
    "v_mfma_f32_16x16x32_bf16 v[144:147], a[92:95], a[44:47], v[144:147]\n"
    "ds_read_b128 a[140:143], v247 offset:20736\n"
    "v_mfma_f32_16x16x32_bf16 v[148:151], a[96:99], a[32:35], v[196:199]\n"
    "v_cmp_nge_f32 vcc, s70, v208\n"
    "ds_read_b64_tr_b16 a[176:177], v248 offset:16384\n"
    "v_mfma_f32_16x16x32_bf16 v[148:151], a[100:103], a[36:39], v[148:151]\n"
    "v_exp_f32 v221, v139\n"
    "v_mfma_f32_16x16x32_bf16 v[148:151], a[104:107], a[40:43], v[148:151]\n"
    "s_cbranch_vccnz Lstub0_0_1\n"
    "Lback0_0_1:\n"
    "v_add_f32 v212, v212, v208\n"
    "v_mfma_f32_16x16x32_bf16 v[148:151], a[108:111], a[44:47], v[148:151]\n"
    "v_cvt_pk_bf16_f32 v172, v218, v219\n"
    "ds_read_b64_tr_b16 a[178:179], v248 offset:20480\n"
    "v_mfma_f32_16x16x32_bf16 v[0:3], a[144:147], v[168:171], v[0:3]\n"
    "v_exp_f32 v222, v140\n"
    "v_mfma_f32_16x16x32_bf16 v[4:7], a[148:151], v[168:171], v[4:7]\n"
    "v_exp_f32 v223, v141\n"
    "v_mfma_f32_16x16x32_bf16 v[8:11], a[152:155], v[168:171], v[8:11]\n"
    "v_cvt_pk_bf16_f32 v173, v220, v221\n"
    "ds_read_b64_tr_b16 a[180:181], v249 offset:16384\n"
    "v_mfma_f32_16x16x32_bf16 v[12:15], a[156:159], v[168:171], v[12:15]\n"
    "v_exp_f32 v218, v142\n"
    "v_mfma_f32_16x16x32_bf16 v[16:19], a[160:163], v[168:171], v[16:19]\n"
    "v_exp_f32 v219, v143\n"
    "v_mfma_f32_16x16x32_bf16 v[20:23], a[164:167], v[168:171], v[20:23]\n"
    "v_cvt_pk_bf16_f32 v174, v222, v223\n"
    "v_cvt_pk_bf16_f32 v175, v218, v219\n"
    "v_mfma_f32_16x16x32_bf16 v[24:27], a[168:171], v[168:171], v[24:27]\n"
    "ds_read_b64_tr_b16 a[182:183], v249 offset:20480\n"
    "v_mfma_f32_16x16x32_bf16 v[28:31], a[172:175], v[168:171], v[28:31]\n"
    "v_mfma_f32_16x16x32_bf16 v[208:211], a[240:243], v[172:175], 0\n"
    "v_mfma_f32_16x16x32_bf16 v[152:155], a[80:83], a[48:51], v[200:203]\n"
    "v_exp_f32 v218, v144\n"
    "v_mfma_f32_16x16x32_bf16 v[152:155], a[84:87], a[52:55], v[152:155]\n"
    "v_exp_f32 v219, v145\n"
    "v_mfma_f32_16x16x32_bf16 v[152:155], a[88:91], a[56:59], v[152:155]\n"
    "ds_read_b64_tr_b16 a[184:185], v248 offset:16640\n"
    "v_mfma_f32_16x16x32_bf16 v[152:155], a[92:95], a[60:63], v[152:155]\n"
    "v_exp_f32 v220, v146\n"
    "v_mfma_f32_16x16x32_bf16 v[156:159], a[96:99], a[48:51], v[200:203]\n"
    "v_cmp_nge_f32 vcc, s70, v208\n"
    "ds_read_b64_tr_b16 a[186:187], v248 offset:20736\n"
    "v_mfma_f32_16x16x32_bf16 v[156:159], a[100:103], a[52:55], v[156:159]\n"
    "v_exp_f32 v221, v147\n"
    "v_mfma_f32_16x16x32_bf16 v[156:159], a[104:107], a[56:59], v[156:159]\n"
    "s_cbranch_vccnz Lstub0_0_2\n"
    "Lback0_0_2:\n"
    "v_add_f32 v213, v213, v208\n"
    "v_mfma_f32_16x16x32_bf16 v[156:159], a[108:111], a[60:63], v[156:159]\n"
    "v_cvt_pk_bf16_f32 v176, v218, v219\n"
    "ds_read_b64_tr_b16 a[188:189], v249 offset:16640\n"
    "v_mfma_f32_16x16x32_bf16 v[32:35], a[144:147], v[172:175], v[32:35]\n"
    "v_exp_f32 v222, v148\n"
    "v_mfma_f32_16x16x32_bf16 v[36:39], a[148:151], v[172:175], v[36:39]\n"
    "v_exp_f32 v223, v149\n"
    "v_mfma_f32_16x16x32_bf16 v[40:43], a[152:155], v[172:175], v[40:43]\n"
    "v_cvt_pk_bf16_f32 v177, v220, v221\n"
    "ds_read_b64_tr_b16 a[190:191], v249 offset:20736\n"
    "v_mfma_f32_16x16x32_bf16 v[44:47], a[156:159], v[172:175], v[44:47]\n"
    "v_exp_f32 v218, v150\n"
    "v_mfma_f32_16x16x32_bf16 v[48:51], a[160:163], v[172:175], v[48:51]\n"
    "v_exp_f32 v219, v151\n"
    "v_mfma_f32_16x16x32_bf16 v[52:55], a[164:167], v[172:175], v[52:55]\n"
    "v_cvt_pk_bf16_f32 v178, v222, v223\n"
    "v_cvt_pk_bf16_f32 v179, v218, v219\n"
    "v_mfma_f32_16x16x32_bf16 v[56:59], a[168:171], v[172:175], v[56:59]\n"
    "ds_read_b64_tr_b16 a[192:193], v248 offset:16896\n"
    "v_mfma_f32_16x16x32_bf16 v[60:63], a[172:175], v[172:175], v[60:63]\n"
    "v_mfma_f32_16x16x32_bf16 v[208:211], a[240:243], v[176:179], 0\n"
    "ds_read_b64_tr_b16 a[194:195], v248 offset:20992\n"
    "v_mfma_f32_16x16x32_bf16 v[160:163], a[80:83], a[64:67], v[204:207]\n"
    "v_exp_f32 v218, v152\n"
    "v_mfma_f32_16x16x32_bf16 v[160:163], a[84:87], a[68:71], v[160:163]\n"
    "v_exp_f32 v219, v153\n"
    "v_mfma_f32_16x16x32_bf16 v[160:163], a[88:91], a[72:75], v[160:163]\n"
    "v_exp_f32 v220, v154\n"
    "v_mfma_f32_16x16x32_bf16 v[160:163], a[92:95], a[76:79], v[160:163]\n"
    "v_exp_f32 v221, v155\n"
    "v_mfma_f32_16x16x32_bf16 v[164:167], a[96:99], a[64:67], v[204:207]\n"
    "v_cmp_nge_f32 vcc, s70, v208\n"
    "v_cvt_pk_bf16_f32 v180, v218, v219\n"
    "v_mfma_f32_16x16x32_bf16 v[164:167], a[100:103], a[68:71], v[164:167]\n"
    "ds_read_b64_tr_b16 a[196:197], v249 offset:16896\n"
    "v_mfma_f32_16x16x32_bf16 v[164:167], a[104:107], a[72:75], v[164:167]\n"
    "s_cbranch_vccnz Lstub0_0_3\n"
    "Lback0_0_3:\n"
    "v_add_f32 v214, v214, v208\n"
    "v_mfma_f32_16x16x32_bf16 v[164:167], a[108:111], a[76:79], v[164:167]\n"
    "v_exp_f32 v222, v156\n"
    "v_mfma_f32_16x16x32_bf16 v[64:67], a[144:147], v[176:179], v[64:67]\n"
    "v_exp_f32 v223, v157\n"
    "v_mfma_f32_16x16x32_bf16 v[68:71], a[148:151], v[176:179], v[68:71]\n"
    "ds_read_b64_tr_b16 a[198:199], v249 offset:20992\n"
    "v_cvt_pk_bf16_f32 v181, v220, v221\n"
    "v_mfma_f32_16x16x32_bf16 v[72:75], a[152:155], v[176:179], v[72:75]\n"
    "v_exp_f32 v218, v158\n"
    "v_mfma_f32_16x16x32_bf16 v[76:79], a[156:159], v[176:179], v[76:79]\n"
    "v_exp_f32 v219, v159\n"
    "v_mfma_f32_16x16x32_bf16 v[80:83], a[160:163], v[176:179], v[80:83]\n"
    "ds_read_b64_tr_b16 a[200:201], v248 offset:17152\n"
    "v_cvt_pk_bf16_f32 v182, v222, v223\n"
    "v_mfma_f32_16x16x32_bf16 v[84:87], a[164:167], v[176:179], v[84:87]\n"
    "v_cvt_pk_bf16_f32 v183, v218, v219\n"
    "ds_read_b64_tr_b16 a[202:203], v248 offset:21248\n"
    "v_mfma_f32_16x16x32_bf16 v[88:91], a[168:171], v[176:179], v[88:91]\n"
    "v_mfma_f32_16x16x32_bf16 v[92:95], a[172:175], v[176:179], v[92:95]\n"
    "v_mfma_f32_16x16x32_bf16 v[208:211], a[240:243], v[180:183], 0\n"
    "s_waitcnt lgkmcnt(14)\n"
    "ds_read_b64_tr_b16 a[204:205], v249 offset:17152\n"
    "v_mfma_f32_16x16x32_bf16 v[128:131], a[112:115], a[0:3], v[188:191]\n"
    "v_exp_f32 v218, v160\n"
    "v_mfma_f32_16x16x32_bf16 v[128:131], a[116:119], a[4:7], v[128:131]\n"
    "v_exp_f32 v219, v161\n"
    "v_mfma_f32_16x16x32_bf16 v[128:131], a[120:123], a[8:11], v[128:131]\n"
    "v_exp_f32 v220, v162\n"
    "v_mfma_f32_16x16x32_bf16 v[128:131], a[124:127], a[12:15], v[128:131]\n"
    "v_exp_f32 v221, v163\n"
    "v_mfma_f32_16x16x32_bf16 v[132:135], a[128:131], a[0:3], v[188:191]\n"
    "v_cmp_nge_f32 vcc, s70, v208\n"
    "ds_read_b64_tr_b16 a[206:207], v249 offset:21248\n"
    "v_mfma_f32_16x16x32_bf16 v[132:135], a[132:135], a[4:7], v[132:135]\n"
    "v_cvt_pk_bf16_f32 v184, v218, v219\n"
    "v_mfma_f32_16x16x32_bf16 v[132:135], a[136:139], a[8:11], v[132:135]\n"
    "s_cbranch_vccnz Lstub0_0_4\n"
    "Lback0_0_4:\n"
    "v_add_f32 v215, v215, v208\n"
    "v_mfma_f32_16x16x32_bf16 v[132:135], a[140:143], a[12:15], v[132:135]\n"
    "v_exp_f32 v222, v164\n"
    "v_mfma_f32_16x16x32_bf16 v[96:99], a[144:147], v[180:183], v[96:99]\n"
    "v_exp_f32 v223, v165\n"
    "v_mfma_f32_16x16x32_bf16 v[100:103], a[148:151], v[180:183], v[100:103]\n"
    "v_cvt_pk_bf16_f32 v185, v220, v221\n"
    "v_mfma_f32_16x16x32_bf16 v[104:107], a[152:155], v[180:183], v[104:107]\n"
    "v_exp_f32 v218, v166\n"
    "v_mfma_f32_16x16x32_bf16 v[108:111], a[156:159], v[180:183], v[108:111]\n"
    "v_exp_f32 v219, v167\n"
    "v_mfma_f32_16x16x32_bf16 v[112:115], a[160:163], v[180:183], v[112:115]\n"
    "v_cvt_pk_bf16_f32 v186, v222, v223\n"
    "v_cvt_pk_bf16_f32 v187, v218, v219\n"
    "v_mfma_f32_16x16x32_bf16 v[116:119], a[164:167], v[180:183], v[116:119]\n"
    "v_mfma_f32_16x16x32_bf16 v[120:123], a[168:171], v[180:183], v[120:123]\n"
    "v_mfma_f32_16x16x32_bf16 v[124:127], a[172:175], v[180:183], v[124:127]\n"
    "v_mfma_f32_16x16x32_bf16 v[208:211], a[240:243], v[184:187], 0\n"
    "; ---- ES9\n"
    "v_mfma_f32_16x16x32_bf16 v[136:139], a[112:115], a[16:19], v[192:195]\n"
    "s_waitcnt lgkmcnt(0)\n"
    "s_barrier\n"
    "v_mfma_f32_16x16x32_bf16 v[136:139], a[116:119], a[20:23], v[136:139]\n"
    "v_exp_f32 v218, v128\n"
    "v_mfma_f32_16x16x32_bf16 v[136:139], a[120:123], a[24:27], v[136:139]\n"
    "v_exp_f32 v219, v129\n"
    "v_mfma_f32_16x16x32_bf16 v[136:139], a[124:127], a[28:31], v[136:139]\n"
    "ds_read_b128 a[80:83], v246 offset:32768\n"
    "ds_read_b128 a[84:87], v246 offset:33024\n"
    "v_mfma_f32_16x16x32_bf16 v[140:143], a[128:131], a[16:19], v[192:195]\n"
    "v_cmp_nge_f32 vcc, s70, v208\n"
    "v_mfma_f32_16x16x32_bf16 v[140:143], a[132:135], a[20:23], v[140:143]\n"
    "v_exp_f32 v220, v130\n"
    "v_mfma_f32_16x16x32_bf16 v[140:143], a[136:139], a[24:27], v[140:143]\n"
    "s_cbranch_vccnz Lstub0_1_0\n"
    "Lback0_1_0:\n"
    "v_add_f32 v216, v216, v208\n"
    "v_mfma_f32_16x16x32_bf16 v[140:143], a[140:143], a[28:31], v[140:143]\n"
    "v_exp_f32 v221, v131\n"
    "v_mfma_f32_16x16x32_bf16 a[208:211], a[144:147], v[184:187], a[208:211]\n"
    "v_cvt_pk_bf16_f32 v168, v218, v219\n"
    "ds_read_b128 a[88:91], v247 offset:32768\n"
    "v_mfma_f32_16x16x32_bf16 a[212:215], a[148:151], v[184:187], a[212:215]\n"
    "v_exp_f32 v222, v132\n"
    "v_mfma_f32_16x16x32_bf16 a[216:219], a[152:155], v[184:187], a[216:219]\n"
    "v_exp_f32 v223, v133\n"
    "v_mfma_f32_16x16x32_bf16 a[220:223], a[156:159], v[184:187], a[220:223]\n"
    "v_cvt_pk_bf16_f32 v169, v220, v221\n"
    "ds_read_b128 a[92:95], v247 offset:33024\n"
    "v_mfma_f32_16x16x32_bf16 a[224:227], a[160:163], v[184:187], a[224:227]\n"
    "v_exp_f32 v218, v134\n"
    "v_mfma_f32_16x16x32_bf16 a[228:231], a[164:167], v[184:187], a[228:231]\n"
    "v_exp_f32 v219, v135\n"
    "v_mfma_f32_16x16x32_bf16 a[232:235], a[168:171], v[184:187], a[232:235]\n"
    "v_cvt_pk_bf16_f32 v170, v222, v223\n"
    "ds_read_b128 a[96:99], v246 offset:36864\n"
    "v_mfma_f32_16x16x32_bf16 a[236:239], a[172:175], v[184:187], a[236:239]\n"
    "v_cvt_pk_bf16_f32 v171, v218, v219\n"
    "ds_read_b128 a[100:103], v246 offset:37120\n"
    "s_nop 0\n"
    "v_mfma_f32_16x16x32_bf16 v[208:211], a[240:243], v[168:171], 0\n"
    "ds_read_b128 a[104:107], v247 offset:36864\n"
    "v_mfma_f32_16x16x32_bf16 v[144:147], a[112:115], a[32:35], v[196:199]\n"
    "v_exp_f32 v218, v136\n"
    "v_mfma_f32_16x16x32_bf16 v[144:147], a[116:119], a[36:39], v[144:147]\n"
    "v_exp_f32 v219, v137\n"
    "v_mfma_f32_16x16x32_bf16 v[144:147], a[120:123], a[40:43], v[144:147]\n"
    "v_exp_f32 v220, v138\n"
    "v_mfma_f32_16x16x32_bf16 v[144:147], a[124:127], a[44:47], v[144:147]\n"
    "ds_read_b128 a[108:111], v247 offset:37120\n"
    "v_mfma_f32_16x16x32_bf16 v[148:151], a[128:131], a[32:35], v[196:199]\n"
    "v_cmp_nge_f32 vcc, s70, v208\n"
    "ds_read_b64_tr_b16 a[144:145], v248 offset:32768\n"
    "v_mfma_f32_16x16x32_bf16 v[148:151], a[132:135], a[36:39], v[148:151]\n"
    "v_exp_f32 v221, v139\n"
    "v_mfma_f32_16x16x32_bf16 v[148:151], a[136:139], a[40:43], v[148:151]\n"
    "s_cbranch_vccnz Lstub0_1_1\n"
    "Lback0_1_1:\n"
    "v_add_f32 v212, v212, v208\n"
    "v_mfma_f32_16x16x32_bf16 v[148:151], a[140:143], a[44:47], v[148:151]\n"
    "v_cvt_pk_bf16_f32 v172, v218, v219\n"
    "ds_read_b64_tr_b16 a[146:147], v248 offset:36864\n"
    "v_mfma_f32_16x16x32_bf16 v[0:3], a[176:179], v[168:171], v[0:3]\n"
    "v_exp_f32 v222, v140\n"
    "v_mfma_f32_16x16x32_bf16 v[4:7], a[180:183], v[168:171], v[4:7]\n"
    "v_exp_f32 v223, v141\n"
    "v_mfma_f32_16x16x32_bf16 v[8:11], a[184:187], v[168:171], v[8:11]\n"
    "v_cvt_pk_bf16_f32 v173, v220, v221\n"
    "ds_read_b64_tr_b16 a[148:149], v249 offset:32768\n"
    "v_mfma_f32_16x16x32_bf16 v[12:15], a[188:191], v[168:171], v[12:15]\n"
    "v_exp_f32 v218, v142\n"
    "v_mfma_f32_16x16x32_bf16 v[16:19], a[192:195], v[168:171], v[16:19]\n"
    "v_exp_f32 v219, v143\n"
    "v_mfma_f32_16x16x32_bf16 v[20:23], a[196:199], v[168:171], v[20:23]\n"
    "v_cvt_pk_bf16_f32 v174, v222, v223\n"
    "v_cvt_pk_bf16_f32 v175, v218, v219\n"
    "v_mfma_f32_16x16x32_bf16 v[24:27], a[200:203], v[168:171], v[24:27]\n"
    "ds_read_b64_tr_b16 a[150:151], v249 offset:36864\n"
    "v_mfma_f32_16x16x32_bf16 v[28:31], a[204:207], v[168:171], v[28:31]\n"
    "v_mfma_f32_16x16x32_bf16 v[208:211], a[240:243], v[172:175], 0\n"
    "v_mfma_f32_16x16x32_bf16 v[152:155], a[112:115], a[48:51], v[200:203]\n"
    "v_exp_f32 v218, v144\n"
    "v_mfma_f32_16x16x32_bf16 v[152:155], a[116:119], a[52:55], v[152:155]\n"
    "v_exp_f32 v219, v145\n"
    "v_mfma_f32_16x16x32_bf16 v[152:155], a[120:123], a[56:59], v[152:155]\n"
    "ds_read_b64_tr_b16 a[152:153], v248 offset:33024\n"
    "v_mfma_f32_16x16x32_bf16 v[152:155], a[124:127], a[60:63], v[152:155]\n"
    "v_exp_f32 v220, v146\n"
    "v_mfma_f32_16x16x32_bf16 v[156:159], a[128:131], a[48:51], v[200:203]\n"
    "v_cmp_nge_f32 vcc, s70, v208\n"
    "ds_read_b64_tr_b16 a[154:155], v248 offset:37120\n"
    "v_mfma_f32_16x16x32_bf16 v[156:159], a[132:135], a[52:55], v[156:159]\n"
    "v_exp_f32 v221, v147\n"
    "v_mfma_f32_16x16x32_bf16 v[156:159], a[136:139], a[56:59], v[156:159]\n"
    "s_cbranch_vccnz Lstub0_1_2\n"
    "Lback0_1_2:\n"
    "v_add_f32 v213, v213, v208\n"
    "v_mfma_f32_16x16x32_bf16 v[156:159], a[140:143], a[60:63], v[156:159]\n"
    "v_cvt_pk_bf16_f32 v176, v218, v219\n"
    "ds_read_b64_tr_b16 a[156:157], v249 offset:33024\n"
    "v_mfma_f32_16x16x32_bf16 v[32:35], a[176:179], v[172:175], v[32:35]\n"
    "v_exp_f32 v222, v148\n"
    "v_mfma_f32_16x16x32_bf16 v[36:39], a[180:183], v[172:175], v[36:39]\n"
    "v_exp_f32 v223, v149\n"
    "v_mfma_f32_16x16x32_bf16 v[40:43], a[184:187], v[172:175], v[40:43]\n"
    "v_cvt_pk_bf16_f32 v177, v220, v221\n"
    "ds_read_b64_tr_b16 a[158:159], v249 offset:37120\n"
    "v_mfma_f32_16x16x32_bf16 v[44:47], a[188:191], v[172:175], v[44:47]\n"
    "v_exp_f32 v218, v150\n"
    "v_mfma_f32_16x16x32_bf16 v[48:51], a[192:195], v[172:175], v[48:51]\n"
    "v_exp_f32 v219, v151\n"
    "v_mfma_f32_16x16x32_bf16 v[52:55], a[196:199], v[172:175], v[52:55]\n"
    "v_cvt_pk_bf16_f32 v178, v222, v223\n"
    "v_cvt_pk_bf16_f32 v179, v218, v219\n"
    "v_mfma_f32_16x16x32_bf16 v[56:59], a[200:203], v[172:175], v[56:59]\n"
    "ds_read_b64_tr_b16 a[160:161], v248 offset:33280\n"
    "v_mfma_f32_16x16x32_bf16 v[60:63], a[204:207], v[172:175], v[60:63]\n"
    "v_mfma_f32_16x16x32_bf16 v[208:211], a[240:243], v[176:179], 0\n"
    "ds_read_b64_tr_b16 a[162:163], v248 offset:37376\n"
    "v_mfma_f32_16x16x32_bf16 v[160:163], a[112:115], a[64:67], v[204:207]\n"
    "v_exp_f32 v218, v152\n"
    "v_mfma_f32_16x16x32_bf16 v[160:163], a[116:119], a[68:71], v[160:163]\n"
    "v_exp_f32 v219, v153\n"
    "v_mfma_f32_16x16x32_bf16 v[160:163], a[120:123], a[72:75], v[160:163]\n"
    "v_exp_f32 v220, v154\n"
    "v_mfma_f32_16x16x32_bf16 v[160:163], a[124:127], a[76:79], v[160:163]\n"
    "v_exp_f32 v221, v155\n"
    "v_mfma_f32_16x16x32_bf16 v[164:167], a[128:131], a[64:67], v[204:207]\n"
    "v_cmp_nge_f32 vcc, s70, v208\n"
    "v_cvt_pk_bf16_f32 v180, v218, v219\n"
    "v_mfma_f32_16x16x32_bf16 v[164:167], a[132:135], a[68:71], v[164:167]\n"
    "ds_read_b64_tr_b16 a[164:165], v249 offset:33280\n"
    "v_mfma_f32_16x16x32_bf16 v[164:167], a[136:139], a[72:75], v[164:167]\n"
    "s_cbranch_vccnz Lstub0_1_3\n"
    "Lback0_1_3:\n"
    "v_add_f32 v214, v214, v208\n"
    "v_mfma_f32_16x16x32_bf16 v[164:167], a[140:143], a[76:79], v[164:167]\n"
    "v_exp_f32 v222, v156\n"
    "v_mfma_f32_16x16x32_bf16 v[64:67], a[176:179], v[176:179], v[64:67]\n"
    "v_exp_f32 v223, v157\n"
    "v_mfma_f32_16x16x32_bf16 v[68:71], a[180:183], v[176:179], v[68:71]\n"
    "ds_read_b64_tr_b16 a[166:167], v249 offset:37376\n"
    "v_cvt_pk_bf16_f32 v181, v220, v221\n"
    "v_mfma_f32_16x16x32_bf16 v[72:75], a[184:187], v[176:179], v[72:75]\n"
    "v_exp_f32 v218, v158\n"
    "v_mfma_f32_16x16x32_bf16 v[76:79], a[188:191], v[176:179], v[76:79]\n"
    "v_exp_f32 v219, v159\n"
    "v_mfma_f32_16x16x32_bf16 v[80:83], a[192:195], v[176:179], v[80:83]\n"
    "ds_read_b64_tr_b16 a[168:169], v248 offset:33536\n"
    "v_cvt_pk_bf16_f32 v182, v222, v223\n"
    "v_mfma_f32_16x16x32_bf16 v[84:87], a[196:199], v[176:179], v[84:87]\n"
    "v_cvt_pk_bf16_f32 v183, v218, v219\n"
    "ds_read_b64_tr_b16 a[170:171], v248 offset:37632\n"
    "v_mfma_f32_16x16x32_bf16 v[88:91], a[200:203], v[176:179], v[88:91]\n"
    "v_mfma_f32_16x16x32_bf16 v[92:95], a[204:207], v[176:179], v[92:95]\n"
    "v_mfma_f32_16x16x32_bf16 v[208:211], a[240:243], v[180:183], 0\n"
    "s_waitcnt lgkmcnt(14)\n"
    "ds_read_b64_tr_b16 a[172:173], v249 offset:33536\n"
    "v_mfma_f32_16x16x32_bf16 v[128:131], a[80:83], a[0:3], v[188:191]\n"
    "v_exp_f32 v218, v160\n"
    "v_mfma_f32_16x16x32_bf16 v[128:131], a[84:87], a[4:7], v[128:131]\n"
    "v_exp_f32 v219, v161\n"
    "v_mfma_f32_16x16x32_bf16 v[128:131], a[88:91], a[8:11], v[128:131]\n"
    "v_exp_f32 v220, v162\n"
    "v_mfma_f32_16x16x32_bf16 v[128:131], a[92:95], a[12:15], v[128:131]\n"
    "v_exp_f32 v221, v163\n"
    "v_mfma_f32_16x16x32_bf16 v[132:135], a[96:99], a[0:3], v[188:191]\n"
    "v_cmp_nge_f32 vcc, s70, v208\n"
    "ds_read_b64_tr_b16 a[174:175], v249 offset:37632\n"
    "v_mfma_f32_16x16x32_bf16 v[132:135], a[100:103], a[4:7], v[132:135]\n"
    "v_cvt_pk_bf16_f32 v184, v218, v219\n"
    "v_mfma_f32_16x16x32_bf16 v[132:135], a[104:107], a[8:11], v[132:135]\n"
    "s_cbranch_vccnz Lstub0_1_4\n"
    "Lback0_1_4:\n"
    "v_add_f32 v215, v215, v208\n"
    "v_mfma_f32_16x16x32_bf16 v[132:135], a[108:111], a[12:15], v[132:135]\n"
    "v_exp_f32 v222, v164\n"
    "v_mfma_f32_16x16x32_bf16 v[96:99], a[176:179], v[180:183], v[96:99]\n"
    "v_exp_f32 v223, v165\n"
    "v_mfma_f32_16x16x32_bf16 v[100:103], a[180:183], v[180:183], v[100:103]\n"
    "v_cvt_pk_bf16_f32 v185, v220, v221\n"
    "v_mfma_f32_16x16x32_bf16 v[104:107], a[184:187], v[180:183], v[104:107]\n"
    "v_exp_f32 v218, v166\n"
    "v_mfma_f32_16x16x32_bf16 v[108:111], a[188:191], v[180:183], v[108:111]\n"
    "v_exp_f32 v219, v167\n"
    "v_mfma_f32_16x16x32_bf16 v[112:115], a[192:195], v[180:183], v[112:115]\n"
    "v_cvt_pk_bf16_f32 v186, v222, v223\n"
    "v_cvt_pk_bf16_f32 v187, v218, v219\n"
    "v_mfma_f32_16x16x32_bf16 v[116:119], a[196:199], v[180:183], v[116:119]\n"
    "v_mfma_f32_16x16x32_bf16 v[120:123], a[200:203], v[180:183], v[120:123]\n"
    "v_mfma_f32_16x16x32_bf16 v[124:127], a[204:207], v[180:183], v[124:127]\n"
    "v_mfma_f32_16x16x32_bf16 v[208:211], a[240:243], v[184:187], 0\n"
    "s_sub_u32 s63, s63, 1\n"
    "s_cmp_lg_u32 s63, 0\n"
    "s_cbranch_scc1 Lloop0\n"
    "; ---- ES30\n"
    "v_mfma_f32_16x16x32_bf16 v[136:139], a[80:83], a[16:19], v[192:195]\n"
    "s_waitcnt lgkmcnt(0)\n"
    "s_barrier\n"
    "v_mfma_f32_16x16x32_bf16 v[136:139], a[84:87], a[20:23], v[136:139]\n"
    "v_exp_f32 v218, v128\n"
    "v_mfma_f32_16x16x32_bf16 v[136:139], a[88:91], a[24:27], v[136:139]\n"
    "v_exp_f32 v219, v129\n"
    "v_mfma_f32_16x16x32_bf16 v[136:139], a[92:95], a[28:31], v[136:139]\n"
    "ds_read_b128 a[112:115], v246 offset:49152\n"
    "v_mbcnt_lo_u32_b32 v234, -1, 0\n"
    "v_mfma_f32_16x16x32_bf16 v[140:143], a[96:99], a[16:19], v[192:195]\n"
    "v_cmp_nge_f32 vcc, s70, v208\n"
    "ds_read_b128 a[116:119], v246 offset:49408\n"
    "v_mfma_f32_16x16x32_bf16 v[140:143], a[100:103], a[20:23], v[140:143]\n"
    "v_exp_f32 v220, v130\n"
    "v_mfma_f32_16x16x32_bf16 v[140:143], a[104:107], a[24:27], v[140:143]\n"
    "s_cbranch_vccnz Lstub0_c_0\n"
    "Lback0_c_0:\n"
    "v_add_f32 v216, v216, v208\n"
    "v_mfma_f32_16x16x32_bf16 v[140:143], a[108:111], a[28:31], v[140:143]\n"
    "v_exp_f32 v221, v131\n"
    "v_mfma_f32_16x16x32_bf16 a[208:211], a[176:179], v[184:187], a[208:211]\n"
    "v_cvt_pk_bf16_f32 v168, v218, v219\n"
    "ds_read_b128 a[120:123], v247 offset:49152\n"
    "v_mfma_f32_16x16x32_bf16 a[212:215], a[180:183], v[184:187], a[212:215]\n"
    "v_exp_f32 v222, v132\n"
    "v_mfma_f32_16x16x32_bf16 a[216:219], a[184:187], v[184:187], a[216:219]\n"
    "v_exp_f32 v223, v133\n"
    "v_mfma_f32_16x16x32_bf16 a[220:223], a[188:191], v[184:187], a[220:223]\n"
    "v_cvt_pk_bf16_f32 v169, v220, v221\n"
    "ds_read_b128 a[124:127], v247 offset:49408\n"
    "v_mfma_f32_16x16x32_bf16 a[224:227], a[192:195], v[184:187], a[224:227]\n"
    "v_exp_f32 v218, v134\n"
    "v_mfma_f32_16x16x32_bf16 a[228:231], a[196:199], v[184:187], a[228:231]\n"
    "v_mbcnt_hi_u32_b32 v234, -1, v234\n"
    "ds_read_b128 a[128:131], v246 offset:53248\n"
    "v_mfma_f32_16x16x32_bf16 a[232:235], a[200:203], v[184:187], a[232:235]\n"
    "v_exp_f32 v219, v135\n"
    "v_mfma_f32_16x16x32_bf16 a[236:239], a[204:207], v[184:187], a[236:239]\n"
    "v_cvt_pk_bf16_f32 v170, v222, v223\n"
    "v_cvt_pk_bf16_f32 v171, v218, v219\n"
    "s_nop 1\n"
    "v_mfma_f32_16x16x32_bf16 v[208:211], a[240:243], v[168:171], 0\n"
    "ds_read_b128 a[132:135], v246 offset:53504\n"
    "ds_read_b128 a[136:139], v247 offset:53248\n"
    "v_mfma_f32_16x16x32_bf16 v[144:147], a[80:83], a[32:35], v[196:199]\n"
    "v_exp_f32 v218, v136\n"
    "v_mfma_f32_16x16x32_bf16 v[144:147], a[84:87], a[36:39], v[144:147]\n"
    "v_exp_f32 v219, v137\n"
    "v_mfma_f32_16x16x32_bf16 v[144:147], a[88:91], a[40:43], v[144:147]\n"
    "v_exp_f32 v220, v138\n"
    "v_mfma_f32_16x16x32_bf16 v[144:147], a[92:95], a[44:47], v[144:147]\n"
    "ds_read_b128 a[140:143], v247 offset:53504\n"
    "v_lshlrev_b32 v234, 4, v234\n"
    "v_mfma_f32_16x16x32_bf16 v[148:151], a[96:99], a[32:35], v[196:199]\n"
    "v_cmp_nge_f32 vcc, s70, v208\n"
    "ds_read_b64_tr_b16 a[176:177], v248 offset:49152\n"
    "v_mfma_f32_16x16x32_bf16 v[148:151], a[100:103], a[36:39], v[148:151]\n"
    "v_exp_f32 v221, v139\n"
    "v_mfma_f32_16x16x32_bf16 v[148:151], a[104:107], a[40:43], v[148:151]\n"
    "s_cbranch_vccnz Lstub0_c_1\n"
    "Lback0_c_1:\n"
    "v_add_f32 v212, v212, v208\n"
    "v_mfma_f32_16x16x32_bf16 v[148:151], a[108:111], a[44:47], v[148:151]\n"
    "v_cvt_pk_bf16_f32 v172, v218, v219\n"
    "ds_read_b64_tr_b16 a[178:179], v248 offset:53248\n"
    "v_mfma_f32_16x16x32_bf16 v[0:3], a[144:147], v[168:171], v[0:3]\n"
    "v_exp_f32 v222, v140\n"
    "v_mfma_f32_16x16x32_bf16 v[4:7], a[148:151], v[168:171], v[4:7]\n"
    "v_exp_f32 v223, v141\n"
    "v_mfma_f32_16x16x32_bf16 v[8:11], a[152:155], v[168:171], v[8:11]\n"
    "v_cvt_pk_bf16_f32 v173, v220, v221\n"
    "ds_read_b64_tr_b16 a[180:181], v249 offset:49152\n"
    "v_mfma_f32_16x16x32_bf16 v[12:15], a[156:159], v[168:171], v[12:15]\n"
    "v_exp_f32 v218, v142\n"
    "v_mfma_f32_16x16x32_bf16 v[16:19], a[160:163], v[168:171], v[16:19]\n"
    "v_exp_f32 v219, v143\n"
    "v_mfma_f32_16x16x32_bf16 v[20:23], a[164:167], v[168:171], v[20:23]\n"
    "v_cvt_pk_bf16_f32 v174, v222, v223\n"
    "v_cvt_pk_bf16_f32 v175, v218, v219\n"
    "v_mfma_f32_16x16x32_bf16 v[24:27], a[168:171], v[168:171], v[24:27]\n"
    "s_lshl_b32 s65, s84, 1\n"
    "ds_read_b64_tr_b16 a[182:183], v249 offset:53248\n"
    "v_mfma_f32_16x16x32_bf16 v[28:31], a[172:175], v[168:171], v[28:31]\n"
    "v_mfma_f32_16x16x32_bf16 v[208:211], a[240:243], v[172:175], 0\n"
    "v_mfma_f32_16x16x32_bf16 v[152:155], a[80:83], a[48:51], v[200:203]\n"
    "v_exp_f32 v218, v144\n"
    "v_mfma_f32_16x16x32_bf16 v[152:155], a[84:87], a[52:55], v[152:155]\n"
    "v_exp_f32 v219, v145\n"
    "v_mfma_f32_16x16x32_bf16 v[152:155], a[88:91], a[56:59], v[152:155]\n"
    "ds_read_b64_tr_b16 a[184:185], v248 offset:49408\n"
    "v_mfma_f32_16x16x32_bf16 v[152:155], a[92:95], a[60:63], v[152:155]\n"
    "v_exp_f32 v220, v146\n"
    "v_mfma_f32_16x16x32_bf16 v[156:159], a[96:99], a[48:51], v[200:203]\n"
    "v_cmp_nge_f32 vcc, s70, v208\n"
    "ds_read_b64_tr_b16 a[186:187], v248 offset:53504\n"
    "v_mfma_f32_16x16x32_bf16 v[156:159], a[100:103], a[52:55], v[156:159]\n"
    "v_exp_f32 v221, v147\n"
    "v_mfma_f32_16x16x32_bf16 v[156:159], a[104:107], a[56:59], v[156:159]\n"
    "s_cbranch_vccnz Lstub0_c_2\n"
    "Lback0_c_2:\n"
    "v_add_f32 v213, v213, v208\n"
    "s_add_u32 s65, s65, s83\n"
    "v_mfma_f32_16x16x32_bf16 v[156:159], a[108:111], a[60:63], v[156:159]\n"
    "v_cvt_pk_bf16_f32 v176, v218, v219\n"
    "ds_read_b64_tr_b16 a[188:189], v249 offset:49408\n"
    "v_mfma_f32_16x16x32_bf16 v[32:35], a[144:147], v[172:175], v[32:35]\n"
    "v_exp_f32 v222, v148\n"
    "v_mfma_f32_16x16x32_bf16 v[36:39], a[148:151], v[172:175], v[36:39]\n"
    "v_exp_f32 v223, v149\n"
    "v_mfma_f32_16x16x32_bf16 v[40:43], a[152:155], v[172:175], v[40:43]\n"
    "v_cvt_pk_bf16_f32 v177, v220, v221\n"
    "ds_read_b64_tr_b16 a[190:191], v249 offset:53504\n"
    "v_mfma_f32_16x16x32_bf16 v[44:47], a[156:159], v[172:175], v[44:47]\n"
    "v_exp_f32 v218, v150\n"
    "v_mfma_f32_16x16x32_bf16 v[48:51], a[160:163], v[172:175], v[48:51]\n"
    "v_exp_f32 v219, v151\n"
    "v_mfma_f32_16x16x32_bf16 v[52:55], a[164:167], v[172:175], v[52:55]\n"
    "v_cvt_pk_bf16_f32 v178, v222, v223\n"
    "v_cvt_pk_bf16_f32 v179, v218, v219\n"
    "v_mfma_f32_16x16x32_bf16 v[56:59], a[168:171], v[172:175], v[56:59]\n"
    "s_mul_i32 s65, s65, 36864\n"
    "ds_read_b64_tr_b16 a[192:193], v248 offset:49664\n"
    "v_mfma_f32_16x16x32_bf16 v[60:63], a[172:175], v[172:175], v[60:63]\n"
    "v_mfma_f32_16x16x32_bf16 v[208:211], a[240:243], v[176:179], 0\n"
    "ds_read_b64_tr_b16 a[194:195], v248 offset:53760\n"
    "v_mfma_f32_16x16x32_bf16 v[160:163], a[80:83], a[64:67], v[204:207]\n"
    "v_exp_f32 v218, v152\n"
    "v_mfma_f32_16x16x32_bf16 v[160:163], a[84:87], a[68:71], v[160:163]\n"
    "v_exp_f32 v219, v153\n"
    "v_mfma_f32_16x16x32_bf16 v[160:163], a[88:91], a[72:75], v[160:163]\n"
    "v_exp_f32 v220, v154\n"
    "v_mfma_f32_16x16x32_bf16 v[160:163], a[92:95], a[76:79], v[160:163]\n"
    "v_exp_f32 v221, v155\n"
    "v_mfma_f32_16x16x32_bf16 v[164:167], a[96:99], a[64:67], v[204:207]\n"
    "v_cmp_nge_f32 vcc, s70, v208\n"
    "v_cvt_pk_bf16_f32 v180, v218, v219\n"
    "v_mfma_f32_16x16x32_bf16 v[164:167], a[100:103], a[68:71], v[164:167]\n"
    "ds_read_b64_tr_b16 a[196:197], v249 offset:49664\n"
    "v_mfma_f32_16x16x32_bf16 v[164:167], a[104:107], a[72:75], v[164:167]\n"
    "s_cbranch_vccnz Lstub0_c_3\n"
    "Lback0_c_3:\n"
    "v_add_f32 v214, v214, v208\n"
    "v_mfma_f32_16x16x32_bf16 v[164:167], a[108:111], a[76:79], v[164:167]\n"
    "s_cmp_eq_u32 s83, 0\n"
    "s_mov_b32 s75, 47104\n"
    "s_cselect_b32 s74, 124928, s75\n"
    "v_add_u32 v254, s74, v234\n"
    "v_mfma_f32_16x16x32_bf16 v[64:67], a[144:147], v[176:179], v[64:67]\n"
    "v_exp_f32 v222, v156\n"
    "v_mfma_f32_16x16x32_bf16 v[68:71], a[148:151], v[176:179], v[68:71]\n"
    "v_exp_f32 v223, v157\n"
    "v_mfma_f32_16x16x32_bf16 v[72:75], a[152:155], v[176:179], v[72:75]\n"
    "ds_read_b64_tr_b16 a[198:199], v249 offset:53760\n"
    "v_cvt_pk_bf16_f32 v181, v220, v221\n"
    "v_mfma_f32_16x16x32_bf16 v[76:79], a[156:159], v[176:179], v[76:79]\n"
    "v_exp_f32 v218, v158\n"
    "v_mfma_f32_16x16x32_bf16 v[80:83], a[160:163], v[176:179], v[80:83]\n"
    "v_exp_f32 v219, v159\n"
    "v_mfma_f32_16x16x32_bf16 v[84:87], a[164:167], v[176:179], v[84:87]\n"
    "ds_read_b64_tr_b16 a[200:201], v248 offset:49920\n"
    "v_cvt_pk_bf16_f32 v182, v222, v223\n"
    "v_mfma_f32_16x16x32_bf16 v[88:91], a[168:171], v[176:179], v[88:91]\n"
    "v_cvt_pk_bf16_f32 v183, v218, v219\n"
    "ds_read_b64_tr_b16 a[202:203], v248 offset:54016\n"
    "v_mfma_f32_16x16x32_bf16 v[92:95], a[172:175], v[176:179], v[92:95]\n"
    "v_mfma_f32_16x16x32_bf16 v[208:211], a[240:243], v[180:183], 0\n"
    "s_waitcnt lgkmcnt(14)\n"
    "s_cmp_eq_u32 s83, 0\n"
    "s_mov_b32 s75, 106496\n"
    "s_cselect_b32 s74, 28672, s75\n"
    "v_add_u32 v255, s74, v234\n"
    "v_mfma_f32_16x16x32_bf16 v[128:131], a[112:115], a[0:3], v[188:191]\n"
    "v_exp_f32 v218, v160\n"
    "v_mfma_f32_16x16x32_bf16 v[128:131], a[116:119], a[4:7], v[128:131]\n"
    "ds_read_b64_tr_b16 a[204:205], v249 offset:49920\n"
    "v_mfma_f32_16x16x32_bf16 v[128:131], a[120:123], a[8:11], v[128:131]\n"
    "v_exp_f32 v219, v161\n"
    "v_mfma_f32_16x16x32_bf16 v[128:131], a[124:127], a[12:15], v[128:131]\n"
    "v_exp_f32 v220, v162\n"
    "v_mfma_f32_16x16x32_bf16 v[132:135], a[128:131], a[0:3], v[188:191]\n"
    "v_cmp_nge_f32 vcc, s70, v208\n"
    "ds_read_b64_tr_b16 a[206:207], v249 offset:54016\n"
    "v_mfma_f32_16x16x32_bf16 v[132:135], a[132:135], a[4:7], v[132:135]\n"
    "v_exp_f32 v221, v163\n"
    "v_mfma_f32_16x16x32_bf16 v[132:135], a[136:139], a[8:11], v[132:135]\n"
    "s_cbranch_vccnz Lstub0_c_4\n"
    "Lback0_c_4:\n"
    "v_add_f32 v215, v215, v208\n"
    "v_mfma_f32_16x16x32_bf16 v[132:135], a[140:143], a[12:15], v[132:135]\n"
    "v_cvt_pk_bf16_f32 v184, v218, v219\n"
    "v_mfma_f32_16x16x32_bf16 v[96:99], a[144:147], v[180:183], v[96:99]\n"
    "global_load_dwordx2 v[230:231], v244, s[80:81] sc1\n"
    "v_mfma_f32_16x16x32_bf16 v[100:103], a[148:151], v[180:183], v[100:103]\n"
    "v_exp_f32 v222, v164\n"
    "v_mfma_f32_16x16x32_bf16 v[104:107], a[152:155], v[180:183], v[104:107]\n"
    "v_exp_f32 v223, v165\n"
    "v_mfma_f32_16x16x32_bf16 v[108:111], a[156:159], v[180:183], v[108:111]\n"
    "v_cvt_pk_bf16_f32 v185, v220, v221\n"
    "v_mfma_f32_16x16x32_bf16 v[112:115], a[160:163], v[180:183], v[112:115]\n"
    "v_exp_f32 v218, v166\n"
    "v_mfma_f32_16x16x32_bf16 v[116:119], a[164:167], v[180:183], v[116:119]\n"
    "v_exp_f32 v219, v167\n"
    "v_mfma_f32_16x16x32_bf16 v[120:123], a[168:171], v[180:183], v[120:123]\n"
    "v_cvt_pk_bf16_f32 v186, v222, v223\n"
    "v_cvt_pk_bf16_f32 v187, v218, v219\n"
    "v_mfma_f32_16x16x32_bf16 v[124:127], a[172:175], v[180:183], v[124:127]\n"
    "s_nop 0\n"
    "v_mfma_f32_16x16x32_bf16 v[208:211], a[240:243], v[184:187], 0\n"
    "; ---- ES31\n"
    "v_mfma_f32_16x16x32_bf16 v[136:139], a[112:115], a[16:19], v[192:195]\n"
    "s_waitcnt lgkmcnt(0)\n"
    "s_barrier\n"
    "v_mfma_f32_16x16x32_bf16 v[136:139], a[116:119], a[20:23], v[136:139]\n"
    "v_exp_f32 v218, v128\n"
    "v_mfma_f32_16x16x32_bf16 v[136:139], a[120:123], a[24:27], v[136:139]\n"
    "v_exp_f32 v219, v129\n"
    "v_mfma_f32_16x16x32_bf16 v[136:139], a[124:127], a[28:31], v[136:139]\n"
    "v_exp_f32 v220, v130\n"
    "v_mfma_f32_16x16x32_bf16 v[140:143], a[128:131], a[16:19], v[192:195]\n"
    "v_cmp_nge_f32 vcc, s70, v208\n"
    "v_mfma_f32_16x16x32_bf16 v[140:143], a[132:135], a[20:23], v[140:143]\n"
    "v_exp_f32 v221, v131\n"
    "v_mfma_f32_16x16x32_bf16 v[140:143], a[136:139], a[24:27], v[140:143]\n"
    "s_cbranch_vccnz Lstub0_d_0\n"
    "Lback0_d_0:\n"
    "v_add_f32 v216, v216, v208\n"
    "v_mfma_f32_16x16x32_bf16 v[140:143], a[140:143], a[28:31], v[140:143]\n"
    "v_cvt_pk_bf16_f32 v168, v218, v219\n"
    "v_mfma_f32_16x16x32_bf16 a[208:211], a[144:147], v[184:187], a[208:211]\n"
    "v_exp_f32 v222, v132\n"
    "v_mfma_f32_16x16x32_bf16 a[212:215], a[148:151], v[184:187], a[212:215]\n"
    "v_exp_f32 v223, v133\n"
    "v_mfma_f32_16x16x32_bf16 a[216:219], a[152:155], v[184:187], a[216:219]\n"
    "v_cvt_pk_bf16_f32 v169, v220, v221\n"
    "v_mfma_f32_16x16x32_bf16 a[220:223], a[156:159], v[184:187], a[220:223]\n"
    "v_exp_f32 v218, v134\n"
    "v_mfma_f32_16x16x32_bf16 a[224:227], a[160:163], v[184:187], a[224:227]\n"
    "v_exp_f32 v219, v135\n"
    "v_mfma_f32_16x16x32_bf16 a[228:231], a[164:167], v[184:187], a[228:231]\n"
    "v_cvt_pk_bf16_f32 v170, v222, v223\n"
    "v_cvt_pk_bf16_f32 v171, v218, v219\n"
    "v_mfma_f32_16x16x32_bf16 a[232:235], a[168:171], v[184:187], a[232:235]\n"
    "v_mfma_f32_16x16x32_bf16 a[236:239], a[172:175], v[184:187], a[236:239]\n"
    "v_mfma_f32_16x16x32_bf16 v[208:211], a[240:243], v[168:171], 0\n"
    "v_mfma_f32_16x16x32_bf16 v[144:147], a[112:115], a[32:35], v[196:199]\n"
    "v_exp_f32 v218, v136\n"
    "v_mfma_f32_16x16x32_bf16 v[144:147], a[116:119], a[36:39], v[144:147]\n"
    "v_exp_f32 v219, v137\n"
    "v_mfma_f32_16x16x32_bf16 v[144:147], a[120:123], a[40:43], v[144:147]\n"
    "v_exp_f32 v220, v138\n"
    "v_mfma_f32_16x16x32_bf16 v[144:147], a[124:127], a[44:47], v[144:147]\n"
    "v_exp_f32 v221, v139\n"
    "v_mfma_f32_16x16x32_bf16 v[148:151], a[128:131], a[32:35], v[196:199]\n"
    "v_cmp_nge_f32 vcc, s70, v208\n"
    "v_cvt_pk_bf16_f32 v172, v218, v219\n"
    "v_mfma_f32_16x16x32_bf16 v[148:151], a[132:135], a[36:39], v[148:151]\n"
    "v_exp_f32 v222, v140\n"
    "v_mfma_f32_16x16x32_bf16 v[148:151], a[136:139], a[40:43], v[148:151]\n"
    "s_cbranch_vccnz Lstub0_d_1\n"
    "Lback0_d_1:\n"
    "v_add_f32 v212, v212, v208\n"
    "v_mfma_f32_16x16x32_bf16 v[148:151], a[140:143], a[44:47], v[148:151]\n"
    "v_exp_f32 v223, v141\n"
    "v_mfma_f32_16x16x32_bf16 v[0:3], a[176:179], v[168:171], v[0:3]\n"
    "v_cvt_pk_bf16_f32 v173, v220, v221\n"
    "v_mfma_f32_16x16x32_bf16 v[4:7], a[180:183], v[168:171], v[4:7]\n"
    "v_exp_f32 v218, v142\n"
    "v_mfma_f32_16x16x32_bf16 v[8:11], a[184:187], v[168:171], v[8:11]\n"
    "v_exp_f32 v219, v143\n"
    "v_mfma_f32_16x16x32_bf16 v[12:15], a[188:191], v[168:171], v[12:15]\n"
    "v_cvt_pk_bf16_f32 v174, v222, v223\n"
    "v_cvt_pk_bf16_f32 v175, v218, v219\n"
    "v_mfma_f32_16x16x32_bf16 v[16:19], a[192:195], v[168:171], v[16:19]\n"
    "v_mfma_f32_16x16x32_bf16 v[20:23], a[196:199], v[168:171], v[20:23]\n"
    "v_mfma_f32_16x16x32_bf16 v[24:27], a[200:203], v[168:171], v[24:27]\n"
    "v_mfma_f32_16x16x32_bf16 v[28:31], a[204:207], v[168:171], v[28:31]\n"
    "v_mfma_f32_16x16x32_bf16 v[208:211], a[240:243], v[172:175], 0\n"
    "v_mfma_f32_16x16x32_bf16 v[152:155], a[112:115], a[48:51], v[200:203]\n"
    "v_exp_f32 v218, v144\n"
    "v_mfma_f32_16x16x32_bf16 v[152:155], a[116:119], a[52:55], v[152:155]\n"
    "v_rcp_f32 v188, v212\n"
    "s_nop 0\n"
    "v_pk_mul_f32 v[0:1], v[0:1], v[188:189] op_sel_hi:[1,0]\n"
    "v_pk_mul_f32 v[2:3], v[2:3], v[188:189] op_sel_hi:[1,0]\n"
    "v_mfma_f32_16x16x32_bf16 v[152:155], a[120:123], a[56:59], v[152:155]\n"
    "v_pk_mul_f32 v[4:5], v[4:5], v[188:189] op_sel_hi:[1,0]\n"
    "v_pk_mul_f32 v[6:7], v[6:7], v[188:189] op_sel_hi:[1,0]\n"
    "v_cvt_pk_bf16_f32 v0, v0, v1\n"
    "v_mfma_f32_16x16x32_bf16 v[152:155], a[124:127], a[60:63], v[152:155]\n"
    "v_cvt_pk_bf16_f32 v1, v2, v3\n"
    "v_cvt_pk_bf16_f32 v2, v4, v5\n"
    "v_mfma_f32_16x16x32_bf16 v[156:159], a[128:131], a[48:51], v[200:203]\n"
    "v_cmp_nge_f32 vcc, s70, v208\n"
    "v_cvt_pk_bf16_f32 v3, v6, v7\n"
    "buffer_store_dwordx4 v[0:3], v234, s[52:55], s65 offen sc1\n"
    "ds_write_b128 v254, v[0:3]\n"
    "v_mfma_f32_16x16x32_bf16 v[156:159], a[132:135], a[52:55], v[156:159]\n"
    "v_pk_mul_f32 v[8:9], v[8:9], v[188:189] op_sel_hi:[1,0]\n"
    "v_pk_mul_f32 v[10:11], v[10:11], v[188:189] op_sel_hi:[1,0]\n"
    "v_pk_mul_f32 v[12:13], v[12:13], v[188:189] op_sel_hi:[1,0]\n"
    "v_mfma_f32_16x16x32_bf16 v[156:159], a[136:139], a[56:59], v[156:159]\n"
    "s_cbranch_vccnz Lstub0_d_2\n"
    "Lback0_d_2:\n"
    "v_add_f32 v213, v213, v208\n"
    "v_pk_mul_f32 v[14:15], v[14:15], v[188:189] op_sel_hi:[1,0]\n"
    "v_cvt_pk_bf16_f32 v8, v8, v9\n"
    "v_mfma_f32_16x16x32_bf16 v[156:159], a[140:143], a[60:63], v[156:159]\n"
    "v_cvt_pk_bf16_f32 v9, v10, v11\n"
    "v_cvt_pk_bf16_f32 v10, v12, v13\n"
    "v_cvt_pk_bf16_f32 v11, v14, v15\n"
    "v_mfma_f32_16x16x32_bf16 v[32:35], a[176:179], v[172:175], v[32:35]\n"
    "buffer_store_dwordx4 v[8:11], v234, s[52:55], s65 offen offset:1024 sc1\n"
    "ds_write_b128 v254, v[8:11] offset:1024\n"
    "v_mfma_f32_16x16x32_bf16 v[36:39], a[180:183], v[172:175], v[36:39]\n"
    "v_pk_mul_f32 v[16:17], v[16:17], v[188:189] op_sel_hi:[1,0]\n"
    "v_pk_mul_f32 v[18:19], v[18:19], v[188:189] op_sel_hi:[1,0]\n"
    "v_pk_mul_f32 v[20:21], v[20:21], v[188:189] op_sel_hi:[1,0]\n"
    "v_mfma_f32_16x16x32_bf16 v[40:43], a[184:187], v[172:175], v[40:43]\n"
    "v_exp_f32 v219, v145\n"
    "v_pk_mul_f32 v[22:23], v[22:23], v[188:189] op_sel_hi:[1,0]\n"
    "v_cvt_pk_bf16_f32 v16, v16, v17\n"
    "v_cvt_pk_bf16_f32 v17, v18, v19\n"
    "v_mfma_f32_16x16x32_bf16 v[44:47], a[188:191], v[172:175], v[44:47]\n"
    "v_exp_f32 v220, v146\n"
    "v_exp_f32 v221, v147\n"
    "v_cvt_pk_bf16_f32 v18, v20, v21\n"
    "v_cvt_pk_bf16_f32 v19, v22, v23\n"
    "v_mfma_f32_16x16x32_bf16 v[48:51], a[192:195], v[172:175], v[48:51]\n"
    "v_cvt_pk_bf16_f32 v176, v218, v219\n"
    "v_exp_f32 v222, v148\n"
    "buffer_store_dwordx4 v[16:19], v234, s[52:55], s65 offen offset:2048 sc1\n"
    "ds_write_b128 v255, v[16:19]\n"
    "v_pk_mul_f32 v[24:25], v[24:25], v[188:189] op_sel_hi:[1,0]\n"
    "v_mfma_f32_16x16x32_bf16 v[52:55], a[196:199], v[172:175], v[52:55]\n"
    "v_exp_f32 v223, v149\n"
    "v_cvt_pk_bf16_f32 v177, v220, v221\n"
    "v_pk_mul_f32 v[26:27], v[26:27], v[188:189] op_sel_hi:[1,0]\n"
    "v_pk_mul_f32 v[28:29], v[28:29], v[188:189] op_sel_hi:[1,0]\n"
    "v_pk_mul_f32 v[30:31], v[30:31], v[188:189] op_sel_hi:[1,0]\n"
    "v_mfma_f32_16x16x32_bf16 v[56:59], a[200:203], v[172:175], v[56:59]\n"
    "v_exp_f32 v218, v150\n"
    "v_exp_f32 v219, v151\n"
    "v_cvt_pk_bf16_f32 v24, v24, v25\n"
    "v_cvt_pk_bf16_f32 v25, v26, v27\n"
    "v_mfma_f32_16x16x32_bf16 v[60:63], a[204:207], v[172:175], v[60:63]\n"
    "v_cvt_pk_bf16_f32 v178, v222, v223\n"
    "v_cvt_pk_bf16_f32 v179, v218, v219\n"
    "v_cvt_pk_bf16_f32 v26, v28, v29\n"
    "v_cvt_pk_bf16_f32 v27, v30, v31\n"
    "buffer_store_dwordx4 v[24:27], v234, s[52:55], s65 offen offset:3072 sc1\n"
    "v_mfma_f32_16x16x32_bf16 v[208:211], a[240:243], v[176:179], 0\n"
    "ds_write_b128 v255, v[24:27] offset:1024\n"
    "s_add_u32 s65, s65, 4096\n"
    "v_mfma_f32_16x16x32_bf16 v[160:163], a[112:115], a[64:67], v[204:207]\n"
    "v_exp_f32 v218, v152\n"
    "v_mfma_f32_16x16x32_bf16 v[160:163], a[116:119], a[68:71], v[160:163]\n"
    "v_rcp_f32 v192, v213\n"
    "s_nop 0\n"
    "v_pk_mul_f32 v[32:33], v[32:33], v[192:193] op_sel_hi:[1,0]\n"
    "v_pk_mul_f32 v[34:35], v[34:35], v[192:193] op_sel_hi:[1,0]\n"
    "v_mfma_f32_16x16x32_bf16 v[160:163], a[120:123], a[72:75], v[160:163]\n"
    "v_pk_mul_f32 v[36:37], v[36:37], v[192:193] op_sel_hi:[1,0]\n"
    "v_pk_mul_f32 v[38:39], v[38:39], v[192:193] op_sel_hi:[1,0]\n"
    "v_cvt_pk_bf16_f32 v32, v32, v33\n"
    "v_mfma_f32_16x16x32_bf16 v[160:163], a[124:127], a[76:79], v[160:163]\n"
    "v_cvt_pk_bf16_f32 v33, v34, v35\n"
    "v_cvt_pk_bf16_f32 v34, v36, v37\n"
    "v_mfma_f32_16x16x32_bf16 v[164:167], a[128:131], a[64:67], v[204:207]\n"
    "v_cmp_nge_f32 vcc, s70, v208\n"
    "v_cvt_pk_bf16_f32 v35, v38, v39\n"
    "buffer_store_dwordx4 v[32:35], v234, s[52:55], s65 offen sc1\n"
    "ds_write_b128 v254, v[32:35] offset:2048\n"
    "v_mfma_f32_16x16x32_bf16 v[164:167], a[132:135], a[68:71], v[164:167]\n"
    "v_pk_mul_f32 v[40:41], v[40:41], v[192:193] op_sel_hi:[1,0]\n"
    "v_pk_mul_f32 v[42:43], v[42:43], v[192:193] op_sel_hi:[1,0]\n"
    "v_pk_mul_f32 v[44:45], v[44:45], v[192:193] op_sel_hi:[1,0]\n"
    "v_mfma_f32_16x16x32_bf16 v[164:167], a[136:139], a[72:75], v[164:167]\n"
    "s_cbranch_vccnz Lstub0_d_3\n"
    "Lback0_d_3:\n"
    "v_add_f32 v214, v214, v208\n"
    "v_pk_mul_f32 v[46:47], v[46:47], v[192:193] op_sel_hi:[1,0]\n"
    "v_cvt_pk_bf16_f32 v40, v40, v41\n"
    "v_mfma_f32_16x16x32_bf16 v[164:167], a[140:143], a[76:79], v[164:167]\n"
    "v_cvt_pk_bf16_f32 v41, v42, v43\n"
    "v_cvt_pk_bf16_f32 v42, v44, v45\n"
    "v_cvt_pk_bf16_f32 v43, v46, v47\n"
    "v_mfma_f32_16x16x32_bf16 v[64:67], a[176:179], v[176:179], v[64:67]\n"
    "buffer_store_dwordx4 v[40:43], v234, s[52:55], s65 offen offset:1024 sc1\n"
    "ds_write_b128 v254, v[40:43] offset:3072\n"
    "v_mfma_f32_16x16x32_bf16 v[68:71], a[180:183], v[176:179], v[68:71]\n"
    "v_pk_mul_f32 v[48:49], v[48:49], v[192:193] op_sel_hi:[1,0]\n"
    "v_pk_mul_f32 v[50:51], v[50:51], v[192:193] op_sel_hi:[1,0]\n"
    "v_pk_mul_f32 v[52:53], v[52:53], v[192:193] op_sel_hi:[1,0]\n"
    "v_mfma_f32_16x16x32_bf16 v[72:75], a[184:187], v[176:179], v[72:75]\n"
    "v_exp_f32 v219, v153\n"
    "v_pk_mul_f32 v[54:55], v[54:55], v[192:193] op_sel_hi:[1,0]\n"
    "v_cvt_pk_bf16_f32 v48, v48, v49\n"
    "v_cvt_pk_bf16_f32 v49, v50, v51\n"
    "v_mfma_f32_16x16x32_bf16 v[76:79], a[188:191], v[176:179], v[76:79]\n"
    "v_exp_f32 v220, v154\n"
    "v_exp_f32 v221, v155\n"
    "v_cvt_pk_bf16_f32 v50, v52, v53\n"
    "v_cvt_pk_bf16_f32 v51, v54, v55\n"
    "v_mfma_f32_16x16x32_bf16 v[80:83], a[192:195], v[176:179], v[80:83]\n"
    "v_cvt_pk_bf16_f32 v180, v218, v219\n"
    "v_exp_f32 v222, v156\n"
    "buffer_store_dwordx4 v[48:51], v234, s[52:55], s65 offen offset:2048 sc1\n"
    "ds_write_b128 v255, v[48:51] offset:2048\n"
    "v_pk_mul_f32 v[56:57], v[56:57], v[192:193] op_sel_hi:[1,0]\n"
    "v_mfma_f32_16x16x32_bf16 v[84:87], a[196:199], v[176:179], v[84:87]\n"
    "v_exp_f32 v223, v157\n"
    "v_cvt_pk_bf16_f32 v181, v220, v221\n"
    "v_pk_mul_f32 v[58:59], v[58:59], v[192:193] op_sel_hi:[1,0]\n"
    "v_pk_mul_f32 v[60:61], v[60:61], v[192:193] op_sel_hi:[1,0]\n"
    "v_pk_mul_f32 v[62:63], v[62:63], v[192:193] op_sel_hi:[1,0]\n"
    "v_mfma_f32_16x16x32_bf16 v[88:91], a[200:203], v[176:179], v[88:91]\n"
    "v_exp_f32 v218, v158\n"
    "v_exp_f32 v219, v159\n"
    "v_cvt_pk_bf16_f32 v56, v56, v57\n"
    "v_cvt_pk_bf16_f32 v57, v58, v59\n"
    "v_mfma_f32_16x16x32_bf16 v[92:95], a[204:207], v[176:179], v[92:95]\n"
    "v_cvt_pk_bf16_f32 v182, v222, v223\n"
    "v_cvt_pk_bf16_f32 v183, v218, v219\n"
    "v_cvt_pk_bf16_f32 v58, v60, v61\n"
    "v_cvt_pk_bf16_f32 v59, v62, v63\n"
    "buffer_store_dwordx4 v[56:59], v234, s[52:55], s65 offen offset:3072 sc1\n"
    "v_mfma_f32_16x16x32_bf16 v[208:211], a[240:243], v[180:183], 0\n"
    "ds_write_b128 v255, v[56:59] offset:3072\n"
    "s_add_u32 s65, s65, 4096\n"
    "v_exp_f32 v218, v160\n"
    "v_rcp_f32 v196, v214\n"
    "s_nop 0\n"
    "v_pk_mul_f32 v[64:65], v[64:65], v[196:197] op_sel_hi:[1,0]\n"
    "v_pk_mul_f32 v[66:67], v[66:67], v[196:197] op_sel_hi:[1,0]\n"
    "v_pk_mul_f32 v[68:69], v[68:69], v[196:197] op_sel_hi:[1,0]\n"
    "v_pk_mul_f32 v[70:71], v[70:71], v[196:197] op_sel_hi:[1,0]\n"
    "v_cvt_pk_bf16_f32 v64, v64, v65\n"
    "v_cvt_pk_bf16_f32 v65, v66, v67\n"
    "v_cvt_pk_bf16_f32 v66, v68, v69\n"
    "v_cmp_nge_f32 vcc, s70, v208\n"
    "v_cvt_pk_bf16_f32 v67, v70, v71\n"
    "buffer_store_dwordx4 v[64:67], v234, s[52:55], s65 offen sc1\n"
    "ds_write_b128 v254, v[64:67] offset:4096\n"
    "v_pk_mul_f32 v[72:73], v[72:73], v[196:197] op_sel_hi:[1,0]\n"
    "v_pk_mul_f32 v[74:75], v[74:75], v[196:197] op_sel_hi:[1,0]\n"
    "v_pk_mul_f32 v[76:77], v[76:77], v[196:197] op_sel_hi:[1,0]\n"
    "s_cbranch_vccnz Lstub0_d_4\n"
    "Lback0_d_4:\n"
    "v_add_f32 v215, v215, v208\n"
    "v_pk_mul_f32 v[78:79], v[78:79], v[196:197] op_sel_hi:[1,0]\n"
    "v_cvt_pk_bf16_f32 v72, v72, v73\n"
    "v_cvt_pk_bf16_f32 v73, v74, v75\n"
    "v_cvt_pk_bf16_f32 v74, v76, v77\n"
    "v_cvt_pk_bf16_f32 v75, v78, v79\n"
    "v_mfma_f32_16x16x32_bf16 v[96:99], a[176:179], v[180:183], v[96:99]\n"
    "buffer_store_dwordx4 v[72:75], v234, s[52:55], s65 offen offset:1024 sc1\n"
    "ds_write_b128 v254, v[72:75] offset:5120\n"
    "v_mfma_f32_16x16x32_bf16 v[100:103], a[180:183], v[180:183], v[100:103]\n"
    "v_pk_mul_f32 v[80:81], v[80:81], v[196:197] op_sel_hi:[1,0]\n"
    "v_pk_mul_f32 v[82:83], v[82:83], v[196:197] op_sel_hi:[1,0]\n"
    "v_pk_mul_f32 v[84:85], v[84:85], v[196:197] op_sel_hi:[1,0]\n"
    "v_mfma_f32_16x16x32_bf16 v[104:107], a[184:187], v[180:183], v[104:107]\n"
    "v_exp_f32 v219, v161\n"
    "v_pk_mul_f32 v[86:87], v[86:87], v[196:197] op_sel_hi:[1,0]\n"
    "v_cvt_pk_bf16_f32 v80, v80, v81\n"
    "v_cvt_pk_bf16_f32 v81, v82, v83\n"
    "v_mfma_f32_16x16x32_bf16 v[108:111], a[188:191], v[180:183], v[108:111]\n"
    "v_exp_f32 v220, v162\n"
    "v_exp_f32 v221, v163\n"
    "v_cvt_pk_bf16_f32 v82, v84, v85\n"
    "v_cvt_pk_bf16_f32 v83, v86, v87\n"
    "v_mfma_f32_16x16x32_bf16 v[112:115], a[192:195], v[180:183], v[112:115]\n"
    "v_cvt_pk_bf16_f32 v184, v218, v219\n"
    "v_exp_f32 v222, v164\n"
    "buffer_store_dwordx4 v[80:83], v234, s[52:55], s65 offen offset:2048 sc1\n"
    "ds_write_b128 v255, v[80:83] offset:4096\n"
    "v_pk_mul_f32 v[88:89], v[88:89], v[196:197] op_sel_hi:[1,0]\n"
    "v_mfma_f32_16x16x32_bf16 v[116:119], a[196:199], v[180:183], v[116:119]\n"
    "v_exp_f32 v223, v165\n"
    "v_cvt_pk_bf16_f32 v185, v220, v221\n"
    "v_pk_mul_f32 v[90:91], v[90:91], v[196:197] op_sel_hi:[1,0]\n"
    "v_pk_mul_f32 v[92:93], v[92:93], v[196:197] op_sel_hi:[1,0]\n"
    "v_pk_mul_f32 v[94:95], v[94:95], v[196:197] op_sel_hi:[1,0]\n"
    "v_mfma_f32_16x16x32_bf16 v[120:123], a[200:203], v[180:183], v[120:123]\n"
    "v_exp_f32 v218, v166\n"
    "v_exp_f32 v219, v167\n"
    "v_cvt_pk_bf16_f32 v88, v88, v89\n"
    "v_cvt_pk_bf16_f32 v89, v90, v91\n"
    "v_mfma_f32_16x16x32_bf16 v[124:127], a[204:207], v[180:183], v[124:127]\n"
    "v_cvt_pk_bf16_f32 v186, v222, v223\n"
    "v_cvt_pk_bf16_f32 v187, v218, v219\n"
    "v_cvt_pk_bf16_f32 v90, v92, v93\n"
    "v_cvt_pk_bf16_f32 v91, v94, v95\n"
    "buffer_store_dwordx4 v[88:91], v234, s[52:55], s65 offen offset:3072 sc1\n"
    "v_mfma_f32_16x16x32_bf16 v[208:211], a[240:243], v[184:187], 0\n"
    "ds_write_b128 v255, v[88:91] offset:5120\n"
    "s_add_u32 s65, s65, 4096\n"
    "; ---- EEND\n"
    "v_rcp_f32 v200, v215\n"
    "s_nop 0\n"
    "v_pk_mul_f32 v[96:97], v[96:97], v[200:201] op_sel_hi:[1,0]\n"
    "v_pk_mul_f32 v[98:99], v[98:99], v[200:201] op_sel_hi:[1,0]\n"
    "v_pk_mul_f32 v[100:101], v[100:101], v[200:201] op_sel_hi:[1,0]\n"
    "v_pk_mul_f32 v[102:103], v[102:103], v[200:201] op_sel_hi:[1,0]\n"
    "v_cvt_pk_bf16_f32 v96, v96, v97\n"
    "v_cmp_nge_f32 vcc, s70, v208\n"
    "s_cbranch_vccnz Lstub0_e\n"
    "Lback0_e:\n"
    "v_add_f32 v216, v216, v208\n"
    "v_mfma_f32_16x16x32_bf16 a[208:211], a[176:179], v[184:187], a[208:211]\n"
    "v_cvt_pk_bf16_f32 v97, v98, v99\n"
    "v_cvt_pk_bf16_f32 v98, v100, v101\n"
    "v_cvt_pk_bf16_f32 v99, v102, v103\n"
    "buffer_store_dwordx4 v[96:99], v234, s[52:55], s65 offen sc1\n"
    "ds_write_b128 v254, v[96:99] offset:6144\n"
    "v_mfma_f32_16x16x32_bf16 a[212:215], a[180:183], v[184:187], a[212:215]\n"
    "v_pk_mul_f32 v[104:105], v[104:105], v[200:201] op_sel_hi:[1,0]\n"
    "v_pk_mul_f32 v[106:107], v[106:107], v[200:201] op_sel_hi:[1,0]\n"
    "v_pk_mul_f32 v[108:109], v[108:109], v[200:201] op_sel_hi:[1,0]\n"
    "v_pk_mul_f32 v[110:111], v[110:111], v[200:201] op_sel_hi:[1,0]\n"
    "v_mfma_f32_16x16x32_bf16 a[216:219], a[184:187], v[184:187], a[216:219]\n"
    "v_cvt_pk_bf16_f32 v104, v104, v105\n"
    "v_cvt_pk_bf16_f32 v105, v106, v107\n"
    "v_cvt_pk_bf16_f32 v106, v108, v109\n"
    "v_cvt_pk_bf16_f32 v107, v110, v111\n"
    "buffer_store_dwordx4 v[104:107], v234, s[52:55], s65 offen offset:1024 sc1\n"
    "v_mfma_f32_16x16x32_bf16 a[220:223], a[188:191], v[184:187], a[220:223]\n"
    "ds_write_b128 v254, v[104:107] offset:7168\n"
    "v_pk_mul_f32 v[112:113], v[112:113], v[200:201] op_sel_hi:[1,0]\n"
    "v_pk_mul_f32 v[114:115], v[114:115], v[200:201] op_sel_hi:[1,0]\n"
    "v_pk_mul_f32 v[116:117], v[116:117], v[200:201] op_sel_hi:[1,0]\n"
    "v_mfma_f32_16x16x32_bf16 a[224:227], a[192:195], v[184:187], a[224:227]\n"
    "v_pk_mul_f32 v[118:119], v[118:119], v[200:201] op_sel_hi:[1,0]\n"
    "v_cvt_pk_bf16_f32 v112, v112, v113\n"
    "v_cvt_pk_bf16_f32 v113, v114, v115\n"
    "v_cvt_pk_bf16_f32 v114, v116, v117\n"
    "v_cvt_pk_bf16_f32 v115, v118, v119\n"
    "v_mfma_f32_16x16x32_bf16 a[228:231], a[196:199], v[184:187], a[228:231]\n"
    "buffer_store_dwordx4 v[112:115], v234, s[52:55], s65 offen offset:2048 sc1\n"
    "ds_write_b128 v255, v[112:115] offset:6144\n"
    "v_pk_mul_f32 v[120:121], v[120:121], v[200:201] op_sel_hi:[1,0]\n"
    "v_pk_mul_f32 v[122:123], v[122:123], v[200:201] op_sel_hi:[1,0]\n"
    "v_mfma_f32_16x16x32_bf16 a[232:235], a[200:203], v[184:187], a[232:235]\n"
    "v_pk_mul_f32 v[124:125], v[124:125], v[200:201] op_sel_hi:[1,0]\n"
    "v_pk_mul_f32 v[126:127], v[126:127], v[200:201] op_sel_hi:[1,0]\n"
    "v_cvt_pk_bf16_f32 v120, v120, v121\n"
    "v_cvt_pk_bf16_f32 v121, v122, v123\n"
    "v_cvt_pk_bf16_f32 v122, v124, v125\n"
    "v_mfma_f32_16x16x32_bf16 a[236:239], a[204:207], v[184:187], a[236:239]\n"
    "v_cvt_pk_bf16_f32 v123, v126, v127\n"
    "buffer_store_dwordx4 v[120:123], v234, s[52:55], s65 offen offset:3072 sc1\n"
    "ds_write_b128 v255, v[120:123] offset:7168\n"
    "s_add_u32 s65, s65, 4096\n"
    "v_rcp_f32 v204, v216\n"
    "v_accvgpr_read_b32 v0, a208\n"
    "v_accvgpr_read_b32 v1, a209\n"
    "v_accvgpr_read_b32 v2, a210\n"
    "v_accvgpr_read_b32 v3, a211\n"
    "v_accvgpr_read_b32 v4, a212\n"
    "v_accvgpr_read_b32 v5, a213\n"
    "v_accvgpr_read_b32 v6, a214\n"
    "v_accvgpr_read_b32 v7, a215\n"
    "v_accvgpr_read_b32 v8, a216\n"
    "v_accvgpr_read_b32 v9, a217\n"
    "v_accvgpr_read_b32 v10, a218\n"
    "v_accvgpr_read_b32 v11, a219\n"
    "v_accvgpr_read_b32 v12, a220\n"
    "v_accvgpr_read_b32 v13, a221\n"
    "v_accvgpr_read_b32 v14, a222\n"
    "v_accvgpr_read_b32 v15, a223\n"
    "v_accvgpr_read_b32 v16, a224\n"
    "v_accvgpr_read_b32 v17, a225\n"
    "v_accvgpr_read_b32 v18, a226\n"
    "v_accvgpr_read_b32 v19, a227\n"
    "v_accvgpr_read_b32 v20, a228\n"
    "v_accvgpr_read_b32 v21, a229\n"
    "v_accvgpr_read_b32 v22, a230\n"
    "v_accvgpr_read_b32 v23, a231\n"
    "v_accvgpr_read_b32 v24, a232\n"
    "v_accvgpr_read_b32 v25, a233\n"
    "v_accvgpr_read_b32 v26, a234\n"
    "v_accvgpr_read_b32 v27, a235\n"
    "v_accvgpr_read_b32 v28, a236\n"
    "v_accvgpr_read_b32 v29, a237\n"
    "v_accvgpr_read_b32 v30, a238\n"
    "v_accvgpr_read_b32 v31, a239\n"
    "s_add_u32 s65, s65, 16384\n"
    "v_pk_mul_f32 v[0:1], v[0:1], v[204:205] op_sel_hi:[1,0]\n"
    "v_pk_mul_f32 v[2:3], v[2:3], v[204:205] op_sel_hi:[1,0]\n"
    "v_pk_mul_f32 v[4:5], v[4:5], v[204:205] op_sel_hi:[1,0]\n"
    "v_pk_mul_f32 v[6:7], v[6:7], v[204:205] op_sel_hi:[1,0]\n"
    "v_cvt_pk_bf16_f32 v0, v0, v1\n"
    "v_cvt_pk_bf16_f32 v1, v2, v3\n"
    "v_cvt_pk_bf16_f32 v2, v4, v5\n"
    "v_cvt_pk_bf16_f32 v3, v6, v7\n"
    "buffer_store_dwordx4 v[0:3], v234, s[52:55], s65 offen sc1\n"
    "ds_write_b128 v254, v[0:3] offset:16384\n"
    "v_pk_mul_f32 v[8:9], v[8:9], v[204:205] op_sel_hi:[1,0]\n"
    "v_pk_mul_f32 v[10:11], v[10:11], v[204:205] op_sel_hi:[1,0]\n"
    "v_pk_mul_f32 v[12:13], v[12:13], v[204:205] op_sel_hi:[1,0]\n"
    "v_pk_mul_f32 v[14:15], v[14:15], v[204:205] op_sel_hi:[1,0]\n"
    "v_cvt_pk_bf16_f32 v8, v8, v9\n"
    "v_cvt_pk_bf16_f32 v9, v10, v11\n"
    "v_cvt_pk_bf16_f32 v10, v12, v13\n"
    "v_cvt_pk_bf16_f32 v11, v14, v15\n"
    "buffer_store_dwordx4 v[8:11], v234, s[52:55], s65 offen offset:1024 sc1\n"
    "ds_write_b128 v254, v[8:11] offset:17408\n"
    "v_pk_mul_f32 v[16:17], v[16:17], v[204:205] op_sel_hi:[1,0]\n"
    "v_pk_mul_f32 v[18:19], v[18:19], v[204:205] op_sel_hi:[1,0]\n"
    "v_pk_mul_f32 v[20:21], v[20:21], v[204:205] op_sel_hi:[1,0]\n"
    "v_pk_mul_f32 v[22:23], v[22:23], v[204:205] op_sel_hi:[1,0]\n"
    "v_cvt_pk_bf16_f32 v16, v16, v17\n"
    "v_cvt_pk_bf16_f32 v17, v18, v19\n"
    "v_cvt_pk_bf16_f32 v18, v20, v21\n"
    "v_cvt_pk_bf16_f32 v19, v22, v23\n"
    "buffer_store_dwordx4 v[16:19], v234, s[52:55], s65 offen offset:2048 sc1\n"
    "ds_write_b128 v255, v[16:19] offset:16384\n"
    "v_pk_mul_f32 v[24:25], v[24:25], v[204:205] op_sel_hi:[1,0]\n"
    "v_pk_mul_f32 v[26:27], v[26:27], v[204:205] op_sel_hi:[1,0]\n"
    "v_pk_mul_f32 v[28:29], v[28:29], v[204:205] op_sel_hi:[1,0]\n"
    "v_pk_mul_f32 v[30:31], v[30:31], v[204:205] op_sel_hi:[1,0]\n"
    "v_cvt_pk_bf16_f32 v24, v24, v25\n"
    "v_cvt_pk_bf16_f32 v25, v26, v27\n"
    "v_cvt_pk_bf16_f32 v26, v28, v29\n"
    "v_cvt_pk_bf16_f32 v27, v30, v31\n"
    "buffer_store_dwordx4 v[24:27], v234, s[52:55], s65 offen offset:3072 sc1\n"
    "ds_write_b128 v255, v[24:27] offset:17408\n"
    "s_add_u32 s65, s65, 4096\n"
    "s_waitcnt vmcnt(20)\n"
    "s_branch Lgemm\n"
    "; ---- LPRE\n"
    "Llight:\n"
    "v_mov_b32 v224, 0x3f803f80\n"
    "v_accvgpr_write_b32 a240, v224\n"
    "v_accvgpr_write_b32 a241, v224\n"
    "v_accvgpr_write_b32 a242, v224\n"
    "v_accvgpr_write_b32 a243, v224\n"
    "s_mov_b32 s70, 0x45800000\n"
    "s_mov_b64 s[36:37], s[16:17]\n"
    "s_and_b32 s37, s37, 0xffff\n"
    "s_mov_b32 s38, 1313856\n"
    "s_mov_b32 s39, 0x20000\n"
    "v_add_u32 v245, 1, v245\n"
    "s_mov_b32 exec_lo, s93\n"
    "s_mov_b32 exec_hi, 0\n"
    "global_store_dword v243, v245, s[80:81] sc1\n"
    "s_mov_b64 exec, -1\n"
    "s_add_u32 s59, s67, 24576\n"
    "s_and_b32 s59, s59, 0x3ffff\n"
    "s_add_u32 s60, s59, 4096\n"
    "v_mfma_f32_16x16x32_bf16 v[128:131], a[80:83], a[0:3], 0\n"
    "v_mfma_f32_16x16x32_bf16 v[128:131], a[84:87], a[4:7], v[128:131]\n"
    "v_mfma_f32_16x16x32_bf16 v[128:131], a[88:91], a[8:11], v[128:131]\n"
    "v_mfma_f32_16x16x32_bf16 v[128:131], a[92:95], a[12:15], v[128:131]\n"
    "v_mfma_f32_16x16x32_bf16 v[132:135], a[96:99], a[0:3], 0\n"
    "v_mfma_f32_16x16x32_bf16 v[132:135], a[100:103], a[4:7], v[132:135]\n"
    "v_mfma_f32_16x16x32_bf16 v[132:135], a[104:107], a[8:11], v[132:135]\n"
    "v_mfma_f32_16x16x32_bf16 v[132:135], a[108:111], a[12:15], v[132:135]\n"
    "; ---- LS0\n"
    "v_mfma_f32_16x16x32_bf16 v[136:139], a[80:83], a[16:19], 0\n"
    "s_nop 2\n"
    "v_max3_f32 v224, v128, v129, v130\n"
    "s_nop 2\n"
    "v_max3_f32 v225, v131, v132, v133\n"
    "v_max3_f32 v224, v224, v134, v135\n"
    "v_mfma_f32_16x16x32_bf16 v[136:139], a[84:87], a[20:23], v[136:139]\n"
    "v_max_f32 v224, v224, v225\n"
    "v_mov_b32 v225, v224\n"
    "v_mfma_f32_16x16x32_bf16 v[136:139], a[88:91], a[24:27], v[136:139]\n"
    "s_nop 0\n"
    "v_permlane16_swap_b32 v224, v225\n"
    "v_max_f32 v224, v224, v225\n"
    "v_mov_b32 v225, v224\n"
    "v_mfma_f32_16x16x32_bf16 v[136:139], a[92:95], a[28:31], v[136:139]\n"
    "s_add_u32 m0, s76, 114688\n"
    "v_permlane32_swap_b32 v224, v225\n"
    "v_max_f32 v224, v224, v225\n"
    "v_mfma_f32_16x16x32_bf16 v[140:143], a[96:99], a[16:19], 0\n"
    "buffer_load_dwordx4 v250, s[48:51], s59 offen nt lds\n"
    "v_sub_f32 v128, v128, v224\n"
    "v_sub_f32 v129, v129, v224\n"
    "v_sub_f32 v130, v130, v224\n"
    "v_mfma_f32_16x16x32_bf16 v[140:143], a[100:103], a[20:23], v[140:143]\n"
    "v_sub_f32 v131, v131, v224\n"
    "v_sub_f32 v132, v132, v224\n"
    "v_mfma_f32_16x16x32_bf16 v[140:143], a[104:107], a[24:27], v[140:143]\n"
    "v_sub_f32 v133, v133, v224\n"
    "v_sub_f32 v134, v134, v224\n"
    "v_mfma_f32_16x16x32_bf16 v[140:143], a[108:111], a[28:31], v[140:143]\n"
    "buffer_load_dwordx4 v252, s[48:51], s59 offen offset:1024 nt lds\n"
    "v_sub_f32 v135, v135, v224\n"
    "v_sub_f32 v188, 0, v224\n"
    "v_sub_f32 v189, 0, v224\n"
    "v_sub_f32 v190, 0, v224\n"
    "v_sub_f32 v191, 0, v224\n"
    "v_exp_f32 v218, v128\n"
    "v_exp_f32 v219, v129\n"
    "v_exp_f32 v220, v130\n"
    "v_exp_f32 v221, v131\n"
    "v_cvt_pk_bf16_f32 v168, v218, v219\n"
    "buffer_load_dwordx4 v250, s[48:51], s59 offen offset:2048 nt lds\n"
    "v_exp_f32 v222, v132\n"
    "v_exp_f32 v223, v133\n"
    "v_cvt_pk_bf16_f32 v169, v220, v221\n"
    "v_exp_f32 v218, v134\n"
    "v_exp_f32 v219, v135\n"
    "v_cvt_pk_bf16_f32 v170, v222, v223\n"
    "v_cvt_pk_bf16_f32 v171, v218, v219\n"
    "buffer_load_dwordx4 v252, s[48:51], s59 offen offset:3072 nt lds\n"
    "s_add_u32 m0, s76, 118784\n"
    "v_mfma_f32_16x16x32_bf16 v[208:211], a[240:243], v[168:171], 0\n"
    "buffer_load_dwordx4 v250, s[48:51], s60 offen nt lds\n"
    "v_mfma_f32_16x16x32_bf16 v[144:147], a[80:83], a[32:35], 0\n"
    "v_max3_f32 v224, v136, v137, v138\n"
    "v_max3_f32 v225, v139, v140, v141\n"
    "v_max3_f32 v224, v224, v142, v143\n"
    "v_mfma_f32_16x16x32_bf16 v[144:147], a[84:87], a[36:39], v[144:147]\n"
    "v_max_f32 v224, v224, v225\n"
    "v_mov_b32 v225, v224\n"
    "v_mfma_f32_16x16x32_bf16 v[144:147], a[88:91], a[40:43], v[144:147]\n"
    "s_nop 0\n"
    "v_permlane16_swap_b32 v224, v225\n"
    "v_max_f32 v224, v224, v225\n"
    "v_mov_b32 v225, v224\n"
    "v_mfma_f32_16x16x32_bf16 v[144:147], a[92:95], a[44:47], v[144:147]\n"
    "s_nop 0\n"
    "v_permlane32_swap_b32 v224, v225\n"
    "v_max_f32 v224, v224, v225\n"
    "v_mfma_f32_16x16x32_bf16 v[148:151], a[96:99], a[32:35], 0\n"
    "v_sub_f32 v136, v136, v224\n"
    "v_sub_f32 v137, v137, v224\n"
    "v_sub_f32 v138, v138, v224\n"
    "v_mfma_f32_16x16x32_bf16 v[148:151], a[100:103], a[36:39], v[148:151]\n"
    "v_sub_f32 v139, v139, v224\n"
    "v_sub_f32 v140, v140, v224\n"
    "v_mfma_f32_16x16x32_bf16 v[148:151], a[104:107], a[40:43], v[148:151]\n"
    "v_mov_b32 v212, v208\n"
    "buffer_load_dwordx4 v252, s[48:51], s60 offen offset:1024 nt lds\n"
    "v_sub_f32 v141, v141, v224\n"
    "v_sub_f32 v142, v142, v224\n"
    "v_mfma_f32_16x16x32_bf16 v[148:151], a[108:111], a[44:47], v[148:151]\n"
    "v_sub_f32 v143, v143, v224\n"
    "v_sub_f32 v192, 0, v224\n"
    "v_sub_f32 v193, 0, v224\n"
    "v_mfma_f32_16x16x32_bf16 v[0:3], a[144:147], v[168:171], 0\n"
    "v_sub_f32 v194, 0, v224\n"
    "v_sub_f32 v195, 0, v224\n"
    "v_mfma_f32_16x16x32_bf16 v[4:7], a[148:151], v[168:171], 0\n"
    "v_exp_f32 v218, v136\n"
    "v_exp_f32 v219, v137\n"
    "v_exp_f32 v220, v138\n"
    "v_mfma_f32_16x16x32_bf16 v[8:11], a[152:155], v[168:171], 0\n"
    "buffer_load_dwordx4 v250, s[48:51], s60 offen offset:2048 nt lds\n"
    "v_exp_f32 v221, v139\n"
    "v_cvt_pk_bf16_f32 v172, v218, v219\n"
    "v_mfma_f32_16x16x32_bf16 v[12:15], a[156:159], v[168:171], 0\n"
    "v_exp_f32 v222, v140\n"
    "v_exp_f32 v223, v141\n"
    "v_cvt_pk_bf16_f32 v173, v220, v221\n"
    "v_mfma_f32_16x16x32_bf16 v[16:19], a[160:163], v[168:171], 0\n"
    "v_exp_f32 v218, v142\n"
    "v_exp_f32 v219, v143\n"
    "v_mfma_f32_16x16x32_bf16 v[20:23], a[164:167], v[168:171], 0\n"
    "v_cvt_pk_bf16_f32 v174, v222, v223\n"
    "v_cvt_pk_bf16_f32 v175, v218, v219\n"
    "v_mfma_f32_16x16x32_bf16 v[24:27], a[168:171], v[168:171], 0\n"
    "buffer_load_dwordx4 v252, s[48:51], s60 offen offset:3072 nt lds\n"
    "v_mfma_f32_16x16x32_bf16 v[28:31], a[172:175], v[168:171], 0\n"
    "s_add_u32 s59, s67, 40960\n"
    "s_and_b32 s59, s59, 0x3ffff\n"
    "v_mfma_f32_16x16x32_bf16 v[208:211], a[240:243], v[172:175], 0\n"
    "s_add_u32 s60, s59, 4096\n"
    "s_add_u32 m0, s76, 16384\n"
    "v_mfma_f32_16x16x32_bf16 v[152:155], a[80:83], a[48:51], 0\n"
    "v_max3_f32 v224, v144, v145, v146\n"
    "v_max3_f32 v225, v147, v148, v149\n"
    "v_max3_f32 v224, v224, v150, v151\n"
    "v_mfma_f32_16x16x32_bf16 v[152:155], a[84:87], a[52:55], v[152:155]\n"
    "buffer_load_dwordx4 v250, s[44:47], s59 offen nt lds\n"
    "v_max_f32 v224, v224, v225\n"
    "v_mov_b32 v225, v224\n"
    "v_mfma_f32_16x16x32_bf16 v[152:155], a[88:91], a[56:59], v[152:155]\n"
    "s_nop 0\n"
    "v_permlane16_swap_b32 v224, v225\n"
    "v_max_f32 v224, v224, v225\n"
    "v_mov_b32 v225, v224\n"
    "v_mfma_f32_16x16x32_bf16 v[152:155], a[92:95], a[60:63], v[152:155]\n"
    "s_nop 0\n"
    "v_permlane32_swap_b32 v224, v225\n"
    "v_max_f32 v224, v224, v225\n"
    "v_mfma_f32_16x16x32_bf16 v[156:159], a[96:99], a[48:51], 0\n"
    "v_sub_f32 v144, v144, v224\n"
    "v_sub_f32 v145, v145, v224\n"
    "v_sub_f32 v146, v146, v224\n"
    "v_mfma_f32_16x16x32_bf16 v[156:159], a[100:103], a[52:55], v[156:159]\n"
    "buffer_load_dwordx4 v251, s[44:47], s59 offen offset:1024 nt lds\n"
    "v_sub_f32 v147, v147, v224\n"
    "v_sub_f32 v148, v148, v224\n"
    "v_mfma_f32_16x16x32_bf16 v[156:159], a[104:107], a[56:59], v[156:159]\n"
    "v_mov_b32 v213, v208\n"
    "v_sub_f32 v149, v149, v224\n"
    "v_sub_f32 v150, v150, v224\n"
    "v_mfma_f32_16x16x32_bf16 v[156:159], a[108:111], a[60:63], v[156:159]\n"
    "v_sub_f32 v151, v151, v224\n"
    "v_sub_f32 v196, 0, v224\n"
    "v_sub_f32 v197, 0, v224\n"
    "v_mfma_f32_16x16x32_bf16 v[32:35], a[144:147], v[172:175], 0\n"
    "v_sub_f32 v198, 0, v224\n"
    "v_sub_f32 v199, 0, v224\n"
    "v_mfma_f32_16x16x32_bf16 v[36:39], a[148:151], v[172:175], 0\n"
    "buffer_load_dwordx4 v252, s[44:47], s59 offen offset:2048 nt lds\n"
    "v_exp_f32 v218, v144\n"
    "v_exp_f32 v219, v145\n"
    "v_exp_f32 v220, v146\n"
    "v_mfma_f32_16x16x32_bf16 v[40:43], a[152:155], v[172:175], 0\n"
    "v_exp_f32 v221, v147\n"
    "v_cvt_pk_bf16_f32 v176, v218, v219\n"
    "v_mfma_f32_16x16x32_bf16 v[44:47], a[156:159], v[172:175], 0\n"
    "v_exp_f32 v222, v148\n"
    "v_exp_f32 v223, v149\n"
    "v_cvt_pk_bf16_f32 v177, v220, v221\n"
    "v_mfma_f32_16x16x32_bf16 v[48:51], a[160:163], v[172:175], 0\n"
    "v_exp_f32 v218, v150\n"
    "v_exp_f32 v219, v151\n"
    "v_mfma_f32_16x16x32_bf16 v[52:55], a[164:167], v[172:175], 0\n"
    "buffer_load_dwordx4 v253, s[44:47], s59 offen offset:3072 nt lds\n"
    "v_cvt_pk_bf16_f32 v178, v222, v223\n"
    "v_cvt_pk_bf16_f32 v179, v218, v219\n"
    "v_mfma_f32_16x16x32_bf16 v[56:59], a[168:171], v[172:175], 0\n"
    "s_add_u32 m0, s76, 20480\n"
    "v_mfma_f32_16x16x32_bf16 v[60:63], a[172:175], v[172:175], 0\n"
    "buffer_load_dwordx4 v250, s[44:47], s60 offen nt lds\n"
    "v_mfma_f32_16x16x32_bf16 v[208:211], a[240:243], v[176:179], 0\n"
    "v_mfma_f32_16x16x32_bf16 v[128:131], a[112:115], a[0:3], v[188:191]\n"
    "v_max3_f32 v224, v152, v153, v154\n"
    "v_max3_f32 v225, v155, v156, v157\n"
    "v_max3_f32 v224, v224, v158, v159\n"
    "v_mfma_f32_16x16x32_bf16 v[128:131], a[116:119], a[4:7], v[128:131]\n"
    "v_max_f32 v224, v224, v225\n"
    "v_mov_b32 v225, v224\n"
    "v_mfma_f32_16x16x32_bf16 v[128:131], a[120:123], a[8:11], v[128:131]\n"
    "s_nop 0\n"
    "v_permlane16_swap_b32 v224, v225\n"
    "v_max_f32 v224, v224, v225\n"
    "v_mov_b32 v225, v224\n"
    "v_mfma_f32_16x16x32_bf16 v[128:131], a[124:127], a[12:15], v[128:131]\n"
    "s_nop 0\n"
    "v_permlane32_swap_b32 v224, v225\n"
    "v_max_f32 v224, v224, v225\n"
    "v_mfma_f32_16x16x32_bf16 v[132:135], a[128:131], a[0:3], v[188:191]\n"
    "buffer_load_dwordx4 v251, s[44:47], s60 offen offset:1024 nt lds\n"
    "v_sub_f32 v152, v152, v224\n"
    "v_sub_f32 v153, v153, v224\n"
    "v_sub_f32 v154, v154, v224\n"
    "v_mfma_f32_16x16x32_bf16 v[132:135], a[132:135], a[4:7], v[132:135]\n"
    "v_sub_f32 v155, v155, v224\n"
    "v_sub_f32 v156, v156, v224\n"
    "v_mfma_f32_16x16x32_bf16 v[132:135], a[136:139], a[8:11], v[132:135]\n"
    "v_mov_b32 v214, v208\n"
    "v_sub_f32 v157, v157, v224\n"
    "v_sub_f32 v158, v158, v224\n"
    "v_mfma_f32_16x16x32_bf16 v[132:135], a[140:143], a[12:15], v[132:135]\n"
    "v_sub_f32 v159, v159, v224\n"
    "v_sub_f32 v200, 0, v224\n"
    "v_sub_f32 v201, 0, v224\n"
    "v_mfma_f32_16x16x32_bf16 v[64:67], a[144:147], v[176:179], 0\n"
    "buffer_load_dwordx4 v252, s[44:47], s60 offen offset:2048 nt lds\n"
    "v_sub_f32 v202, 0, v224\n"
    "v_sub_f32 v203, 0, v224\n"
    "v_mfma_f32_16x16x32_bf16 v[68:71], a[148:151], v[176:179], 0\n"
    "v_exp_f32 v218, v152\n"
    "v_exp_f32 v219, v153\n"
    "v_exp_f32 v220, v154\n"
    "v_mfma_f32_16x16x32_bf16 v[72:75], a[152:155], v[176:179], 0\n"
    "v_exp_f32 v221, v155\n"
    "v_cvt_pk_bf16_f32 v180, v218, v219\n"
    "v_mfma_f32_16x16x32_bf16 v[76:79], a[156:159], v[176:179], 0\n"
    "v_exp_f32 v222, v156\n"
    "v_exp_f32 v223, v157\n"
    "v_cvt_pk_bf16_f32 v181, v220, v221\n"
    "v_mfma_f32_16x16x32_bf16 v[80:83], a[160:163], v[176:179], 0\n"
    "buffer_load_dwordx4 v253, s[44:47], s60 offen offset:3072 nt lds\n"
    "v_exp_f32 v218, v158\n"
    "v_exp_f32 v219, v159\n"
    "v_mfma_f32_16x16x32_bf16 v[84:87], a[164:167], v[176:179], 0\n"
    "v_cvt_pk_bf16_f32 v182, v222, v223\n"
    "v_cvt_pk_bf16_f32 v183, v218, v219\n"
    "v_mfma_f32_16x16x32_bf16 v[88:91], a[168:171], v[176:179], 0\n"
    "v_mfma_f32_16x16x32_bf16 v[92:95], a[172:175], v[176:179], 0\n"
    "v_mfma_f32_16x16x32_bf16 v[208:211], a[240:243], v[180:183], 0\n"
    "; ---- LS1\n"
    "v_mfma_f32_16x16x32_bf16 v[136:139], a[112:115], a[16:19], v[192:195]\n"
    "s_waitcnt vmcnt(21) lgkmcnt(0)\n"
    "s_barrier\n"
    "v_mfma_f32_16x16x32_bf16 v[136:139], a[116:119], a[20:23], v[136:139]\n"
    "v_exp_f32 v218, v128\n"
    "v_mfma_f32_16x16x32_bf16 v[136:139], a[120:123], a[24:27], v[136:139]\n"
    "v_exp_f32 v219, v129\n"
    "v_mfma_f32_16x16x32_bf16 v[136:139], a[124:127], a[28:31], v[136:139]\n"
    "ds_read_b128 a[80:83], v246 offset:32768\n"
    "s_add_u32 m0, s76, 81920\n"
    "v_mfma_f32_16x16x32_bf16 v[140:143], a[128:131], a[16:19], v[192:195]\n"
    "v_exp_f32 v220, v130\n"
    "v_mfma_f32_16x16x32_bf16 v[140:143], a[132:135], a[20:23], v[140:143]\n"
    "buffer_load_dwordx4 v250, s[48:51], s59 offen nt lds\n"
    "v_mfma_f32_16x16x32_bf16 v[140:143], a[136:139], a[24:27], v[140:143]\n"
    "v_mov_b32 v215, v208\n"
    "ds_read_b128 a[84:87], v246 offset:33024\n"
    "v_mfma_f32_16x16x32_bf16 v[140:143], a[140:143], a[28:31], v[140:143]\n"
    "v_exp_f32 v221, v131\n"
    "v_mfma_f32_16x16x32_bf16 v[96:99], a[144:147], v[180:183], 0\n"
    "buffer_load_dwordx4 v252, s[48:51], s59 offen offset:1024 nt lds\n"
    "v_mfma_f32_16x16x32_bf16 v[100:103], a[148:151], v[180:183], 0\n"
    "v_cvt_pk_bf16_f32 v168, v218, v219\n"
    "ds_read_b128 a[88:91], v247 offset:32768\n"
    "v_mfma_f32_16x16x32_bf16 v[104:107], a[152:155], v[180:183], 0\n"
    "v_exp_f32 v222, v132\n"
    "v_mfma_f32_16x16x32_bf16 v[108:111], a[156:159], v[180:183], 0\n"
    "v_exp_f32 v223, v133\n"
    "v_mfma_f32_16x16x32_bf16 v[112:115], a[160:163], v[180:183], 0\n"
    "buffer_load_dwordx4 v250, s[48:51], s59 offen offset:2048 nt lds\n"
    "v_mfma_f32_16x16x32_bf16 v[116:119], a[164:167], v[180:183], 0\n"
    "v_cvt_pk_bf16_f32 v169, v220, v221\n"
    "ds_read_b128 a[92:95], v247 offset:33024\n"
    "v_mfma_f32_16x16x32_bf16 v[120:123], a[168:171], v[180:183], 0\n"
    "v_exp_f32 v218, v134\n"
    "v_exp_f32 v219, v135\n"
    "v_mfma_f32_16x16x32_bf16 v[124:127], a[172:175], v[180:183], 0\n"
    "v_cvt_pk_bf16_f32 v170, v222, v223\n"
    "v_cvt_pk_bf16_f32 v171, v218, v219\n"
    "buffer_load_dwordx4 v252, s[48:51], s59 offen offset:3072 nt lds\n"
    "s_nop 0\n"
    "v_mfma_f32_16x16x32_bf16 v[208:211], a[240:243], v[168:171], 0\n"
    "ds_read_b128 a[96:99], v246 offset:36864\n"
    "s_add_u32 m0, s76, 86016\n"
    "v_mfma_f32_16x16x32_bf16 v[144:147], a[112:115], a[32:35], v[196:199]\n"
    "ds_read_b128 a[100:103], v246 offset:37120\n"
    "ds_read_b128 a[104:107], v247 offset:36864\n"
    "v_mfma_f32_16x16x32_bf16 v[144:147], a[116:119], a[36:39], v[144:147]\n"
    "buffer_load_dwordx4 v250, s[48:51], s60 offen nt lds\n"
    "v_mfma_f32_16x16x32_bf16 v[144:147], a[120:123], a[40:43], v[144:147]\n"
    "v_exp_f32 v218, v136\n"
    "v_mfma_f32_16x16x32_bf16 v[144:147], a[124:127], a[44:47], v[144:147]\n"
    "v_exp_f32 v219, v137\n"
    "v_mfma_f32_16x16x32_bf16 v[148:151], a[128:131], a[32:35], v[196:199]\n"
    "v_cmp_nge_f32 vcc, s70, v208\n"
    "ds_read_b128 a[108:111], v247 offset:37120\n"
    "v_mfma_f32_16x16x32_bf16 v[148:151], a[132:135], a[36:39], v[148:151]\n"
    "buffer_load_dwordx4 v252, s[48:51], s60 offen offset:1024 nt lds\n"
    "v_mfma_f32_16x16x32_bf16 v[148:151], a[136:139], a[40:43], v[148:151]\n"
    "s_cbranch_vccnz Lstub1_b_1\n"
    "Lback1_b_1:\n"
    "v_add_f32 v212, v212, v208\n"
    "v_mfma_f32_16x16x32_bf16 v[148:151], a[140:143], a[44:47], v[148:151]\n"
    "v_exp_f32 v220, v138\n"
    "v_mfma_f32_16x16x32_bf16 v[0:3], a[176:179], v[168:171], v[0:3]\n"
    "v_exp_f32 v221, v139\n"
    "v_mfma_f32_16x16x32_bf16 v[4:7], a[180:183], v[168:171], v[4:7]\n"
    "buffer_load_dwordx4 v250, s[48:51], s60 offen offset:2048 nt lds\n"
    "v_mfma_f32_16x16x32_bf16 v[8:11], a[184:187], v[168:171], v[8:11]\n"
    "v_cvt_pk_bf16_f32 v172, v218, v219\n"
    "ds_read_b64_tr_b16 a[144:145], v248 offset:32768\n"
    "v_mfma_f32_16x16x32_bf16 v[12:15], a[188:191], v[168:171], v[12:15]\n"
    "v_exp_f32 v222, v140\n"
    "v_mfma_f32_16x16x32_bf16 v[16:19], a[192:195], v[168:171], v[16:19]\n"
    "buffer_load_dwordx4 v252, s[48:51], s60 offen offset:3072 nt lds\n"
    "v_mfma_f32_16x16x32_bf16 v[20:23], a[196:199], v[168:171], v[20:23]\n"
    "v_exp_f32 v223, v141\n"
    "v_cvt_pk_bf16_f32 v173, v220, v221\n"
    "v_mfma_f32_16x16x32_bf16 v[24:27], a[200:203], v[168:171], v[24:27]\n"
    "v_exp_f32 v218, v142\n"
    "v_exp_f32 v219, v143\n"
    "v_mfma_f32_16x16x32_bf16 v[28:31], a[204:207], v[168:171], v[28:31]\n"
    "s_add_u32 s59, s59, 8192\n"
    "s_and_b32 s59, s59, 0x3ffff\n"
    "s_add_u32 s60, s59, 4096\n"
    "s_lshl_b32 s58, s84, 2\n"
    "s_add_u32 s58, s58, 0\n"
    "s_mul_i32 s58, s58, 81920\n"
    "s_add_u32 s58, s58, s88\n"
    "s_add_u32 m0, s85, 143360\n"
    "v_cvt_pk_bf16_f32 v174, v222, v223\n"
    "v_cvt_pk_bf16_f32 v175, v218, v219\n"
    "s_nop 1\n"
    "v_mfma_f32_16x16x32_bf16 v[208:211], a[240:243], v[172:175], 0\n"
    "buffer_load_dwordx4 v236, s[36:39], s58 offen lds\n"
    "v_mfma_f32_16x16x32_bf16 v[152:155], a[112:115], a[48:51], v[200:203]\n"
    "ds_read_b64_tr_b16 a[146:147], v248 offset:36864\n"
    "ds_read_b64_tr_b16 a[148:149], v249 offset:32768\n"
    "v_mfma_f32_16x16x32_bf16 v[152:155], a[116:119], a[52:55], v[152:155]\n"
    "ds_read_b64_tr_b16 a[150:151], v249 offset:36864\n"
    "ds_read_b64_tr_b16 a[152:153], v248 offset:33024\n"
    "v_mfma_f32_16x16x32_bf16 v[152:155], a[120:123], a[56:59], v[152:155]\n"
    "ds_read_b64_tr_b16 a[154:155], v248 offset:37120\n"
    "ds_read_b64_tr_b16 a[156:157], v249 offset:33024\n"
    "v_mfma_f32_16x16x32_bf16 v[152:155], a[124:127], a[60:63], v[152:155]\n"
    "buffer_load_dwordx4 v237, s[36:39], s58 offen offset:1024 lds\n"
    "v_mfma_f32_16x16x32_bf16 v[156:159], a[128:131], a[48:51], v[200:203]\n"
    "v_cmp_nge_f32 vcc, s70, v208\n"
    "ds_read_b64_tr_b16 a[158:159], v249 offset:37120\n"
    "v_mfma_f32_16x16x32_bf16 v[156:159], a[132:135], a[52:55], v[156:159]\n"
    "v_exp_f32 v218, v144\n"
    "v_mfma_f32_16x16x32_bf16 v[156:159], a[136:139], a[56:59], v[156:159]\n"
    "s_cbranch_vccnz Lstub1_b_2\n"
    "Lback1_b_2:\n"
    "v_add_f32 v213, v213, v208\n"
    "buffer_load_dwordx4 v238, s[36:39], s58 offen offset:2048 lds\n"
    "v_mfma_f32_16x16x32_bf16 v[156:159], a[140:143], a[60:63], v[156:159]\n"
    "v_exp_f32 v219, v145\n"
    "v_mfma_f32_16x16x32_bf16 v[32:35], a[176:179], v[172:175], v[32:35]\n"
    "v_exp_f32 v220, v146\n"
    "v_mfma_f32_16x16x32_bf16 v[36:39], a[180:183], v[172:175], v[36:39]\n"
    "v_exp_f32 v221, v147\n"
    "v_mfma_f32_16x16x32_bf16 v[40:43], a[184:187], v[172:175], v[40:43]\n"
    "buffer_load_dwordx4 v239, s[36:39], s58 offen offset:3072 lds\n"
    "v_mfma_f32_16x16x32_bf16 v[44:47], a[188:191], v[172:175], v[44:47]\n"
    "v_cvt_pk_bf16_f32 v176, v218, v219\n"
    "ds_read_b64_tr_b16 a[160:161], v248 offset:33280\n"
    "v_mfma_f32_16x16x32_bf16 v[48:51], a[192:195], v[172:175], v[48:51]\n"
    "v_exp_f32 v222, v148\n"
    "s_add_u32 m0, s85, 147456\n"
    "v_mfma_f32_16x16x32_bf16 v[52:55], a[196:199], v[172:175], v[52:55]\n"
    "v_exp_f32 v223, v149\n"
    "v_cvt_pk_bf16_f32 v177, v220, v221\n"
    "buffer_load_dwordx4 v240, s[36:39], s58 offen lds\n"
    "v_mfma_f32_16x16x32_bf16 v[56:59], a[200:203], v[172:175], v[56:59]\n"
    "v_exp_f32 v218, v150\n"
    "v_exp_f32 v219, v151\n"
    "v_mfma_f32_16x16x32_bf16 v[60:63], a[204:207], v[172:175], v[60:63]\n"
    "v_cvt_pk_bf16_f32 v178, v222, v223\n"
    "v_cvt_pk_bf16_f32 v179, v218, v219\n"
    "s_nop 1\n"
    "v_mfma_f32_16x16x32_bf16 v[208:211], a[240:243], v[176:179], 0\n"
    "s_waitcnt lgkmcnt(9)\n"
    "s_sub_u32 s58, s58, 20480\n"
    "s_add_u32 m0, s85, 138240\n"
    "v_mfma_f32_16x16x32_bf16 v[128:131], a[80:83], a[0:3], v[188:191]\n"
    "buffer_load_dwordx4 v236, s[36:39], s58 offen lds\n"
    "v_mfma_f32_16x16x32_bf16 v[128:131], a[84:87], a[4:7], v[128:131]\n"
    "ds_read_b64_tr_b16 a[162:163], v248 offset:37376\n"
    "ds_read_b64_tr_b16 a[164:165], v249 offset:33280\n"
    "v_mfma_f32_16x16x32_bf16 v[128:131], a[88:91], a[8:11], v[128:131]\n"
    "ds_read_b64_tr_b16 a[166:167], v249 offset:37376\n"
    "ds_read_b64_tr_b16 a[168:169], v248 offset:33536\n"
    "v_mfma_f32_16x16x32_bf16 v[128:131], a[92:95], a[12:15], v[128:131]\n"
    "buffer_load_dwordx4 v237, s[36:39], s58 offen offset:1024 lds\n"
    "v_mfma_f32_16x16x32_bf16 v[132:135], a[96:99], a[0:3], v[188:191]\n"
    "v_cmp_nge_f32 vcc, s70, v208\n"
    "ds_read_b64_tr_b16 a[170:171], v248 offset:37632\n"
    "v_mfma_f32_16x16x32_bf16 v[132:135], a[100:103], a[4:7], v[132:135]\n"
    "v_exp_f32 v218, v152\n"
    "v_mfma_f32_16x16x32_bf16 v[132:135], a[104:107], a[8:11], v[132:135]\n"
    "s_cbranch_vccnz Lstub1_b_3\n"
    "Lback1_b_3:\n"
    "v_add_f32 v214, v214, v208\n"
    "v_mfma_f32_16x16x32_bf16 v[132:135], a[108:111], a[12:15], v[132:135]\n"
    "buffer_load_dwordx4 v238, s[36:39], s58 offen offset:2048 lds\n"
    "v_mfma_f32_16x16x32_bf16 v[64:67], a[176:179], v[176:179], v[64:67]\n"
    "v_exp_f32 v219, v153\n"
    "v_mfma_f32_16x16x32_bf16 v[68:71], a[180:183], v[176:179], v[68:71]\n"
    "v_exp_f32 v220, v154\n"
    "v_mfma_f32_16x16x32_bf16 v[72:75], a[184:187], v[176:179], v[72:75]\n"
    "ds_read_b64_tr_b16 a[172:173], v249 offset:33536\n"
    "ds_read_b64_tr_b16 a[174:175], v249 offset:37632\n"
    "buffer_load_dwordx4 v239, s[36:39], s58 offen offset:3072 lds\n"
    "v_mfma_f32_16x16x32_bf16 v[76:79], a[188:191], v[176:179], v[76:79]\n"
    "v_exp_f32 v221, v155\n"
    "v_mfma_f32_16x16x32_bf16 v[80:83], a[192:195], v[176:179], v[80:83]\n"
    "v_cvt_pk_bf16_f32 v180, v218, v219\n"
    "v_exp_f32 v222, v156\n"
    "v_mfma_f32_16x16x32_bf16 v[84:87], a[196:199], v[176:179], v[84:87]\n"
    "v_exp_f32 v223, v157\n"
    "s_add_u32 m0, s85, 142336\n"
    "v_cvt_pk_bf16_f32 v181, v220, v221\n"
    "v_mfma_f32_16x16x32_bf16 v[88:91], a[200:203], v[176:179], v[88:91]\n"
    "v_exp_f32 v218, v158\n"
    "v_exp_f32 v219, v159\n"
    "buffer_load_dwordx4 v240, s[36:39], s58 offen lds\n"
    "v_mfma_f32_16x16x32_bf16 v[92:95], a[204:207], v[176:179], v[92:95]\n"
    "v_cvt_pk_bf16_f32 v182, v222, v223\n"
    "v_cvt_pk_bf16_f32 v183, v218, v219\n"
    "s_nop 1\n"
    "v_mfma_f32_16x16x32_bf16 v[208:211], a[240:243], v[180:183], 0\n"
    "s_mov_b32 s63, 7\n"
    "s_waitcnt vmcnt(26)\n"
    "s_memrealtime s[98:99]\n"
    "s_waitcnt lgkmcnt(0)\n"
    "s_mov_b32 s79, s98\n"
    "Lloop1:\n"
    "; ---- LS6\n"
    "v_mfma_f32_16x16x32_bf16 v[136:139], a[80:83], a[16:19], v[192:195]\n"
    "Lpace2:\n"
    "s_memrealtime s[98:99]\n"
    "s_waitcnt vmcnt(32) lgkmcnt(0)\n"
    "s_sub_u32 s95, s98, s79\n"
    "s_cmp_lt_i32 s95, 0\n"
    "s_cbranch_scc1 Lpace2\n"
    "s_min_u32 s95, s95, 12\n"
    "s_sub_u32 s79, s98, s95\n"
    "s_add_u32 s79, s79, 110\n"
    "s_barrier\n"
    "v_mfma_f32_16x16x32_bf16 v[136:139], a[84:87], a[20:23], v[136:139]\n"
    "v_exp_f32 v218, v128\n"
    "v_mfma_f32_16x16x32_bf16 v[136:139], a[88:91], a[24:27], v[136:139]\n"
    "v_exp_f32 v219, v129\n"
    "v_mfma_f32_16x16x32_bf16 v[136:139], a[92:95], a[28:31], v[136:139]\n"
    "ds_read_b128 a[112:115], v246 offset:49152\n"
    "s_add_u32 m0, s76, 32768\n"
    "v_mfma_f32_16x16x32_bf16 v[140:143], a[96:99], a[16:19], v[192:195]\n"
    "v_cmp_nge_f32 vcc, s70, v208\n"
    "ds_read_b128 a[116:119], v246 offset:49408\n"
    "v_mfma_f32_16x16x32_bf16 v[140:143], a[100:103], a[20:23], v[140:143]\n"
    "buffer_load_dwordx4 v250, s[44:47], s59 offen nt lds\n"
    "v_mfma_f32_16x16x32_bf16 v[140:143], a[104:107], a[24:27], v[140:143]\n"
    "s_cbranch_vccnz Lstub1_2_0\n"
    "Lback1_2_0:\n"
    "v_add_f32 v215, v215, v208\n"
    "v_mfma_f32_16x16x32_bf16 v[140:143], a[108:111], a[28:31], v[140:143]\n"
    "v_exp_f32 v220, v130\n"
    "v_mfma_f32_16x16x32_bf16 v[96:99], a[176:179], v[180:183], v[96:99]\n"
    "s_add_u32 m0, s76, 98304\n"
    "ds_read_b128 a[120:123], v247 offset:49152\n"
    "v_mfma_f32_16x16x32_bf16 v[100:103], a[180:183], v[180:183], v[100:103]\n"
    "buffer_load_dwordx4 v250, s[48:51], s59 offen nt lds\n"
    "v_mfma_f32_16x16x32_bf16 v[104:107], a[184:187], v[180:183], v[104:107]\n"
    "v_exp_f32 v221, v131\n"
    "v_mfma_f32_16x16x32_bf16 v[108:111], a[188:191], v[180:183], v[108:111]\n"
    "v_cvt_pk_bf16_f32 v168, v218, v219\n"
    "s_add_u32 m0, s76, 32768\n"
    "v_mfma_f32_16x16x32_bf16 v[112:115], a[192:195], v[180:183], v[112:115]\n"
    "v_exp_f32 v222, v132\n"
    "v_mfma_f32_16x16x32_bf16 v[116:119], a[196:199], v[180:183], v[116:119]\n"
    "v_exp_f32 v223, v133\n"
    "v_cvt_pk_bf16_f32 v169, v220, v221\n"
    "buffer_load_dwordx4 v251, s[44:47], s59 offen offset:1024 nt lds\n"
    "v_mfma_f32_16x16x32_bf16 v[120:123], a[200:203], v[180:183], v[120:123]\n"
    "v_exp_f32 v218, v134\n"
    "v_exp_f32 v219, v135\n"
    "v_mfma_f32_16x16x32_bf16 v[124:127], a[204:207], v[180:183], v[124:127]\n"
    "v_cvt_pk_bf16_f32 v170, v222, v223\n"
    "v_cvt_pk_bf16_f32 v171, v218, v219\n"
    "s_nop 1\n"
    "v_mfma_f32_16x16x32_bf16 v[208:211], a[240:243], v[168:171], 0\n"
    "s_add_u32 m0, s76, 98304\n"
    "ds_read_b128 a[124:127], v247 offset:49408\n"
    "v_mfma_f32_16x16x32_bf16 v[144:147], a[80:83], a[32:35], v[196:199]\n"
    "buffer_load_dwordx4 v252, s[48:51], s59 offen offset:1024 nt lds\n"
    "v_mfma_f32_16x16x32_bf16 v[144:147], a[84:87], a[36:39], v[144:147]\n"
    "ds_read_b128 a[128:131], v246 offset:53248\n"
    "ds_read_b128 a[132:135], v246 offset:53504\n"
    "v_mfma_f32_16x16x32_bf16 v[144:147], a[88:91], a[40:43], v[144:147]\n"
    "s_add_u32 m0, s76, 32768\n"
    "ds_read_b128 a[136:139], v247 offset:53248\n"
    "v_mfma_f32_16x16x32_bf16 v[144:147], a[92:95], a[44:47], v[144:147]\n"
    "v_exp_f32 v218, v136\n"
    "v_mfma_f32_16x16x32_bf16 v[148:151], a[96:99], a[32:35], v[196:199]\n"
    "v_cmp_nge_f32 vcc, s70, v208\n"
    "buffer_load_dwordx4 v252, s[44:47], s59 offen offset:2048 nt lds\n"
    "v_mfma_f32_16x16x32_bf16 v[148:151], a[100:103], a[36:39], v[148:151]\n"
    "v_exp_f32 v219, v137\n"
    "v_mfma_f32_16x16x32_bf16 v[148:151], a[104:107], a[40:43], v[148:151]\n"
    "s_cbranch_vccnz Lstub1_2_1\n"
    "Lback1_2_1:\n"
    "v_add_f32 v212, v212, v208\n"
    "v_mfma_f32_16x16x32_bf16 v[148:151], a[108:111], a[44:47], v[148:151]\n"
    "s_add_u32 m0, s76, 98304\n"
    "ds_read_b128 a[140:143], v247 offset:53504\n"
    "v_mfma_f32_16x16x32_bf16 v[0:3], a[144:147], v[168:171], v[0:3]\n"
    "buffer_load_dwordx4 v250, s[48:51], s59 offen offset:2048 nt lds\n"
    "v_mfma_f32_16x16x32_bf16 v[4:7], a[148:151], v[168:171], v[4:7]\n"
    "v_exp_f32 v220, v138\n"
    "v_mfma_f32_16x16x32_bf16 v[8:11], a[152:155], v[168:171], v[8:11]\n"
    "v_exp_f32 v221, v139\n"
    "v_mfma_f32_16x16x32_bf16 v[12:15], a[156:159], v[168:171], v[12:15]\n"
    "s_add_u32 m0, s76, 32768\n"
    "v_cvt_pk_bf16_f32 v172, v218, v219\n"
    "v_mfma_f32_16x16x32_bf16 v[16:19], a[160:163], v[168:171], v[16:19]\n"
    "v_exp_f32 v222, v140\n"
    "buffer_load_dwordx4 v253, s[44:47], s59 offen offset:3072 nt lds\n"
    "v_mfma_f32_16x16x32_bf16 v[20:23], a[164:167], v[168:171], v[20:23]\n"
    "v_exp_f32 v223, v141\n"
    "v_cvt_pk_bf16_f32 v173, v220, v221\n"
    "v_mfma_f32_16x16x32_bf16 v[24:27], a[168:171], v[168:171], v[24:27]\n"
    "v_exp_f32 v218, v142\n"
    "v_exp_f32 v219, v143\n"
    "v_mfma_f32_16x16x32_bf16 v[28:31], a[172:175], v[168:171], v[28:31]\n"
    "s_add_u32 m0, s76, 98304\n"
    "v_cvt_pk_bf16_f32 v174, v222, v223\n"
    "v_cvt_pk_bf16_f32 v175, v218, v219\n"
    "s_nop 1\n"
    "v_mfma_f32_16x16x32_bf16 v[208:211], a[240:243], v[172:175], 0\n"
    "buffer_load_dwordx4 v252, s[48:51], s59 offen offset:3072 nt lds\n"
    "v_mfma_f32_16x16x32_bf16 v[152:155], a[80:83], a[48:51], v[200:203]\n"
    "ds_read_b64_tr_b16 a[176:177], v248 offset:49152\n"
    "ds_read_b64_tr_b16 a[178:179], v248 offset:53248\n"
    "v_mfma_f32_16x16x32_bf16 v[152:155], a[84:87], a[52:55], v[152:155]\n"
    "ds_read_b64_tr_b16 a[180:181], v249 offset:49152\n"
    "ds_read_b64_tr_b16 a[182:183], v249 offset:53248\n"
    "v_mfma_f32_16x16x32_bf16 v[152:155], a[88:91], a[56:59], v[152:155]\n"
    "s_add_u32 m0, s76, 36864\n"
    "ds_read_b64_tr_b16 a[184:185], v248 offset:49408\n"
    "v_mfma_f32_16x16x32_bf16 v[152:155], a[92:95], a[60:63], v[152:155]\n"
    "ds_read_b64_tr_b16 a[186:187], v248 offset:53504\n"
    "ds_read_b64_tr_b16 a[188:189], v249 offset:49408\n"
    "v_mfma_f32_16x16x32_bf16 v[156:159], a[96:99], a[48:51], v[200:203]\n"
    "v_cmp_nge_f32 vcc, s70, v208\n"
    "buffer_load_dwordx4 v250, s[44:47], s60 offen nt lds\n"
    "v_mfma_f32_16x16x32_bf16 v[156:159], a[100:103], a[52:55], v[156:159]\n"
    "v_exp_f32 v218, v144\n"
    "v_mfma_f32_16x16x32_bf16 v[156:159], a[104:107], a[56:59], v[156:159]\n"
    "s_cbranch_vccnz Lstub1_2_2\n"
    "Lback1_2_2:\n"
    "v_add_f32 v213, v213, v208\n"
    "v_mfma_f32_16x16x32_bf16 v[156:159], a[108:111], a[60:63], v[156:159]\n"
    "s_add_u32 m0, s76, 102400\n"
    "ds_read_b64_tr_b16 a[190:191], v249 offset:53504\n"
    "v_mfma_f32_16x16x32_bf16 v[32:35], a[144:147], v[172:175], v[32:35]\n"
    "buffer_load_dwordx4 v250, s[48:51], s60 offen nt lds\n"
    "v_mfma_f32_16x16x32_bf16 v[36:39], a[148:151], v[172:175], v[36:39]\n"
    "v_exp_f32 v219, v145\n"
    "v_mfma_f32_16x16x32_bf16 v[40:43], a[152:155], v[172:175], v[40:43]\n"
    "v_exp_f32 v220, v146\n"
    "v_mfma_f32_16x16x32_bf16 v[44:47], a[156:159], v[172:175], v[44:47]\n"
    "v_exp_f32 v221, v147\n"
    "s_add_u32 m0, s76, 36864\n"
    "v_mfma_f32_16x16x32_bf16 v[48:51], a[160:163], v[172:175], v[48:51]\n"
    "v_cvt_pk_bf16_f32 v176, v218, v219\n"
    "v_exp_f32 v222, v148\n"
    "buffer_load_dwordx4 v251, s[44:47], s60 offen offset:1024 nt lds\n"
    "v_mfma_f32_16x16x32_bf16 v[52:55], a[164:167], v[172:175], v[52:55]\n"
    "v_exp_f32 v223, v149\n"
    "v_cvt_pk_bf16_f32 v177, v220, v221\n"
    "v_mfma_f32_16x16x32_bf16 v[56:59], a[168:171], v[172:175], v[56:59]\n"
    "v_exp_f32 v218, v150\n"
    "v_exp_f32 v219, v151\n"
    "v_mfma_f32_16x16x32_bf16 v[60:63], a[172:175], v[172:175], v[60:63]\n"
    "s_add_u32 m0, s76, 102400\n"
    "v_cvt_pk_bf16_f32 v178, v222, v223\n"
    "v_cvt_pk_bf16_f32 v179, v218, v219\n"
    "s_nop 1\n"
    "v_mfma_f32_16x16x32_bf16 v[208:211], a[240:243], v[176:179], 0\n"
    "s_waitcnt lgkmcnt(8)\n"
    "buffer_load_dwordx4 v252, s[48:51], s60 offen offset:1024 nt lds\n"
    "v_mfma_f32_16x16x32_bf16 v[128:131], a[112:115], a[0:3], v[188:191]\n"
    "ds_read_b64_tr_b16 a[192:193], v248 offset:49664\n"
    "ds_read_b64_tr_b16 a[194:195], v248 offset:53760\n"
    "v_mfma_f32_16x16x32_bf16 v[128:131], a[116:119], a[4:7], v[128:131]\n"
    "ds_read_b64_tr_b16 a[196:197], v249 offset:49664\n"
    "ds_read_b64_tr_b16 a[198:199], v249 offset:53760\n"
    "v_mfma_f32_16x16x32_bf16 v[128:131], a[120:123], a[8:11], v[128:131]\n"
    "s_add_u32 m0, s76, 36864\n"
    "ds_read_b64_tr_b16 a[200:201], v248 offset:49920\n"
    "v_mfma_f32_16x16x32_bf16 v[128:131], a[124:127], a[12:15], v[128:131]\n"
    "buffer_load_dwordx4 v252, s[44:47], s60 offen offset:2048 nt lds\n"
    "v_mfma_f32_16x16x32_bf16 v[132:135], a[128:131], a[0:3], v[188:191]\n"
    "v_cmp_nge_f32 vcc, s70, v208\n"
    "ds_read_b64_tr_b16 a[202:203], v248 offset:54016\n"
    "v_mfma_f32_16x16x32_bf16 v[132:135], a[132:135], a[4:7], v[132:135]\n"
    "v_exp_f32 v218, v152\n"
    "v_mfma_f32_16x16x32_bf16 v[132:135], a[136:139], a[8:11], v[132:135]\n"
    "s_cbranch_vccnz Lstub1_2_3\n"
    "Lback1_2_3:\n"
    "v_add_f32 v214, v214, v208\n"
    "s_add_u32 m0, s76, 102400\n"
    "v_mfma_f32_16x16x32_bf16 v[132:135], a[140:143], a[12:15], v[132:135]\n"
    "buffer_load_dwordx4 v250, s[48:51], s60 offen offset:2048 nt lds\n"
    "v_mfma_f32_16x16x32_bf16 v[64:67], a[144:147], v[176:179], v[64:67]\n"
    "v_exp_f32 v219, v153\n"
    "v_mfma_f32_16x16x32_bf16 v[68:71], a[148:151], v[176:179], v[68:71]\n"
    "v_exp_f32 v220, v154\n"
    "v_mfma_f32_16x16x32_bf16 v[72:75], a[152:155], v[176:179], v[72:75]\n"
    "ds_read_b64_tr_b16 a[204:205], v249 offset:49920\n"
    "ds_read_b64_tr_b16 a[206:207], v249 offset:54016\n"
    "s_add_u32 m0, s76, 36864\n"
    "v_mfma_f32_16x16x32_bf16 v[76:79], a[156:159], v[176:179], v[76:79]\n"
    "v_exp_f32 v221, v155\n"
    "buffer_load_dwordx4 v253, s[44:47], s60 offen offset:3072 nt lds\n"
    "v_mfma_f32_16x16x32_bf16 v[80:83], a[160:163], v[176:179], v[80:83]\n"
    "v_cvt_pk_bf16_f32 v180, v218, v219\n"
    "v_exp_f32 v222, v156\n"
    "v_mfma_f32_16x16x32_bf16 v[84:87], a[164:167], v[176:179], v[84:87]\n"
    "v_exp_f32 v223, v157\n"
    "v_cvt_pk_bf16_f32 v181, v220, v221\n"
    "v_mfma_f32_16x16x32_bf16 v[88:91], a[168:171], v[176:179], v[88:91]\n"
    "s_add_u32 m0, s76, 102400\n"
    "v_exp_f32 v218, v158\n"
    "v_exp_f32 v219, v159\n"
    "v_mfma_f32_16x16x32_bf16 v[92:95], a[172:175], v[176:179], v[92:95]\n"
    "v_cvt_pk_bf16_f32 v182, v222, v223\n"
    "v_cvt_pk_bf16_f32 v183, v218, v219\n"
    "buffer_load_dwordx4 v252, s[48:51], s60 offen offset:3072 nt lds\n"
    "s_nop 0\n"
    "v_mfma_f32_16x16x32_bf16 v[208:211], a[240:243], v[180:183], 0\n"
    "s_add_u32 s59, s59, 8192\n"
    "s_and_b32 s59, s59, 0x3ffff\n"
    "s_add_u32 s60, s59, 4096\n"
    "; ---- LS7\n"
    "v_mfma_f32_16x16x32_bf16 v[136:139], a[112:115], a[16:19], v[192:195]\n"
    "Lpace3:\n"
    "s_memrealtime s[98:99]\n"
    "s_waitcnt vmcnt(32) lgkmcnt(0)\n"
    "s_sub_u32 s95, s98, s79\n"
    "s_cmp_lt_i32 s95, 0\n"
    "s_cbranch_scc1 Lpace3\n"
    "s_min_u32 s95, s95, 12\n"
    "s_sub_u32 s79, s98, s95\n"
    "s_add_u32 s79, s79, 110\n"
    "s_cmp_eq_u32 s63, 1\n"
    "s_cselect_b32 s79, s98, s79\n"
    "s_barrier\n"
    "v_mfma_f32_16x16x32_bf16 v[136:139], a[116:119], a[20:23], v[136:139]\n"
    "v_exp_f32 v218, v128\n"
    "v_mfma_f32_16x16x32_bf16 v[136:139], a[120:123], a[24:27], v[136:139]\n"
    "v_exp_f32 v219, v129\n"
    "v_mfma_f32_16x16x32_bf16 v[136:139], a[124:127], a[28:31], v[136:139]\n"
    "ds_read_b128 a[80:83], v246\n"
    "s_add_u32 m0, s76, 49152\n"
    "v_mfma_f32_16x16x32_bf16 v[140:143], a[128:131], a[16:19], v[192:195]\n"
    "v_cmp_nge_f32 vcc, s70, v208\n"
    "ds_read_b128 a[84:87], v246 offset:256\n"
    "v_mfma_f32_16x16x32_bf16 v[140:143], a[132:135], a[20:23], v[140:143]\n"
    "buffer_load_dwordx4 v250, s[44:47], s59 offen nt lds\n"
    "v_mfma_f32_16x16x32_bf16 v[140:143], a[136:139], a[24:27], v[140:143]\n"
    "s_cbranch_vccnz Lstub1_3_0\n"
    "Lback1_3_0:\n"
    "v_add_f32 v215, v215, v208\n"
    "v_mfma_f32_16x16x32_bf16 v[140:143], a[140:143], a[28:31], v[140:143]\n"
    "v_exp_f32 v220, v130\n"
    "v_mfma_f32_16x16x32_bf16 v[96:99], a[144:147], v[180:183], v[96:99]\n"
    "s_add_u32 m0, s76, 114688\n"
    "ds_read_b128 a[88:91], v247\n"
    "v_mfma_f32_16x16x32_bf16 v[100:103], a[148:151], v[180:183], v[100:103]\n"
    "buffer_load_dwordx4 v250, s[48:51], s59 offen nt lds\n"
    "v_mfma_f32_16x16x32_bf16 v[104:107], a[152:155], v[180:183], v[104:107]\n"
    "v_exp_f32 v221, v131\n"
    "v_mfma_f32_16x16x32_bf16 v[108:111], a[156:159], v[180:183], v[108:111]\n"
    "v_cvt_pk_bf16_f32 v168, v218, v219\n"
    "s_add_u32 m0, s76, 49152\n"
    "v_mfma_f32_16x16x32_bf16 v[112:115], a[160:163], v[180:183], v[112:115]\n"
    "v_exp_f32 v222, v132\n"
    "v_mfma_f32_16x16x32_bf16 v[116:119], a[164:167], v[180:183], v[116:119]\n"
    "v_exp_f32 v223, v133\n"
    "v_cvt_pk_bf16_f32 v169, v220, v221\n"
    "buffer_load_dwordx4 v251, s[44:47], s59 offen offset:1024 nt lds\n"
    "v_mfma_f32_16x16x32_bf16 v[120:123], a[168:171], v[180:183], v[120:123]\n"
    "v_exp_f32 v218, v134\n"
    "v_exp_f32 v219, v135\n"
    "v_mfma_f32_16x16x32_bf16 v[124:127], a[172:175], v[180:183], v[124:127]\n"
    "v_cvt_pk_bf16_f32 v170, v222, v223\n"
    "v_cvt_pk_bf16_f32 v171, v218, v219\n"
    "s_nop 1\n"
    "v_mfma_f32_16x16x32_bf16 v[208:211], a[240:243], v[168:171], 0\n"
    "s_add_u32 m0, s76, 114688\n"
    "ds_read_b128 a[92:95], v247 offset:256\n"
    "v_mfma_f32_16x16x32_bf16 v[144:147], a[112:115], a[32:35], v[196:199]\n"
    "buffer_load_dwordx4 v252, s[48:51], s59 offen offset:1024 nt lds\n"
    "v_mfma_f32_16x16x32_bf16 v[144:147], a[116:119], a[36:39], v[144:147]\n"
    "ds_read_b128 a[96:99], v246 offset:4096\n"
    "ds_read_b128 a[100:103], v246 offset:4352\n"
    "v_mfma_f32_16x16x32_bf16 v[144:147], a[120:123], a[40:43], v[144:147]\n"
    "s_add_u32 m0, s76, 49152\n"
    "ds_read_b128 a[104:107], v247 offset:4096\n"
    "v_mfma_f32_16x16x32_bf16 v[144:147], a[124:127], a[44:47], v[144:147]\n"
    "v_exp_f32 v218, v136\n"
    "v_mfma_f32_16x16x32_bf16 v[148:151], a[128:131], a[32:35], v[196:199]\n"
    "v_cmp_nge_f32 vcc, s70, v208\n"
    "buffer_load_dwordx4 v252, s[44:47], s59 offen offset:2048 nt lds\n"
    "v_mfma_f32_16x16x32_bf16 v[148:151], a[132:135], a[36:39], v[148:151]\n"
    "v_exp_f32 v219, v137\n"
    "v_mfma_f32_16x16x32_bf16 v[148:151], a[136:139], a[40:43], v[148:151]\n"
    "s_cbranch_vccnz Lstub1_3_1\n"
    "Lback1_3_1:\n"
    "v_add_f32 v212, v212, v208\n"
    "v_mfma_f32_16x16x32_bf16 v[148:151], a[140:143], a[44:47], v[148:151]\n"
    "s_add_u32 m0, s76, 114688\n"
    "ds_read_b128 a[108:111], v247 offset:4352\n"
    "v_mfma_f32_16x16x32_bf16 v[0:3], a[176:179], v[168:171], v[0:3]\n"
    "buffer_load_dwordx4 v250, s[48:51], s59 offen offset:2048 nt lds\n"
    "v_mfma_f32_16x16x32_bf16 v[4:7], a[180:183], v[168:171], v[4:7]\n"
    "v_exp_f32 v220, v138\n"
    "v_mfma_f32_16x16x32_bf16 v[8:11], a[184:187], v[168:171], v[8:11]\n"
    "v_exp_f32 v221, v139\n"
    "v_mfma_f32_16x16x32_bf16 v[12:15], a[188:191], v[168:171], v[12:15]\n"
    "s_add_u32 m0, s76, 49152\n"
    "v_cvt_pk_bf16_f32 v172, v218, v219\n"
    "v_mfma_f32_16x16x32_bf16 v[16:19], a[192:195], v[168:171], v[16:19]\n"
    "v_exp_f32 v222, v140\n"
    "buffer_load_dwordx4 v253, s[44:47], s59 offen offset:3072 nt lds\n"
    "v_mfma_f32_16x16x32_bf16 v[20:23], a[196:199], v[168:171], v[20:23]\n"
    "v_exp_f32 v223, v141\n"
    "v_cvt_pk_bf16_f32 v173, v220, v221\n"
    "v_mfma_f32_16x16x32_bf16 v[24:27], a[200:203], v[168:171], v[24:27]\n"
    "v_exp_f32 v218, v142\n"
    "v_exp_f32 v219, v143\n"
    "v_mfma_f32_16x16x32_bf16 v[28:31], a[204:207], v[168:171], v[28:31]\n"
    "s_add_u32 m0, s76, 114688\n"
    "v_cvt_pk_bf16_f32 v174, v222, v223\n"
    "v_cvt_pk_bf16_f32 v175, v218, v219\n"
    "s_nop 1\n"
    "v_mfma_f32_16x16x32_bf16 v[208:211], a[240:243], v[172:175], 0\n"
    "buffer_load_dwordx4 v252, s[48:51], s59 offen offset:3072 nt lds\n"
    "v_mfma_f32_16x16x32_bf16 v[152:155], a[112:115], a[48:51], v[200:203]\n"
    "ds_read_b64_tr_b16 a[144:145], v248\n"
    "ds_read_b64_tr_b16 a[146:147], v248 offset:4096\n"
    "v_mfma_f32_16x16x32_bf16 v[152:155], a[116:119], a[52:55], v[152:155]\n"
    "ds_read_b64_tr_b16 a[148:149], v249\n"
    "ds_read_b64_tr_b16 a[150:151], v249 offset:4096\n"
    "v_mfma_f32_16x16x32_bf16 v[152:155], a[120:123], a[56:59], v[152:155]\n"
    "s_add_u32 m0, s76, 53248\n"
    "ds_read_b64_tr_b16 a[152:153], v248 offset:256\n"
    "v_mfma_f32_16x16x32_bf16 v[152:155], a[124:127], a[60:63], v[152:155]\n"
    "ds_read_b64_tr_b16 a[154:155], v248 offset:4352\n"
    "ds_read_b64_tr_b16 a[156:157], v249 offset:256\n"
    "v_mfma_f32_16x16x32_bf16 v[156:159], a[128:131], a[48:51], v[200:203]\n"
    "v_cmp_nge_f32 vcc, s70, v208\n"
    "buffer_load_dwordx4 v250, s[44:47], s60 offen nt lds\n"
    "v_mfma_f32_16x16x32_bf16 v[156:159], a[132:135], a[52:55], v[156:159]\n"
    "v_exp_f32 v218, v144\n"
    "v_mfma_f32_16x16x32_bf16 v[156:159], a[136:139], a[56:59], v[156:159]\n"
    "s_cbranch_vccnz Lstub1_3_2\n"
    "Lback1_3_2:\n"
    "v_add_f32 v213, v213, v208\n"
    "v_mfma_f32_16x16x32_bf16 v[156:159], a[140:143], a[60:63], v[156:159]\n"
    "s_add_u32 m0, s76, 118784\n"
    "ds_read_b64_tr_b16 a[158:159], v249 offset:4352\n"
    "v_mfma_f32_16x16x32_bf16 v[32:35], a[176:179], v[172:175], v[32:35]\n"
    "buffer_load_dwordx4 v250, s[48:51], s60 offen nt lds\n"
    "v_mfma_f32_16x16x32_bf16 v[36:39], a[180:183], v[172:175], v[36:39]\n"
    "v_exp_f32 v219, v145\n"
    "v_mfma_f32_16x16x32_bf16 v[40:43], a[184:187], v[172:175], v[40:43]\n"
    "v_exp_f32 v220, v146\n"
    "v_mfma_f32_16x16x32_bf16 v[44:47], a[188:191], v[172:175], v[44:47]\n"
    "v_exp_f32 v221, v147\n"
    "s_add_u32 m0, s76, 53248\n"
    "v_mfma_f32_16x16x32_bf16 v[48:51], a[192:195], v[172:175], v[48:51]\n"
    "v_cvt_pk_bf16_f32 v176, v218, v219\n"
    "v_exp_f32 v222, v148\n"
    "buffer_load_dwordx4 v251, s[44:47], s60 offen offset:1024 nt lds\n"
    "v_mfma_f32_16x16x32_bf16 v[52:55], a[196:199], v[172:175], v[52:55]\n"
    "v_exp_f32 v223, v149\n"
    "v_cvt_pk_bf16_f32 v177, v220, v221\n"
    "v_mfma_f32_16x16x32_bf16 v[56:59], a[200:203], v[172:175], v[56:59]\n"
    "v_exp_f32 v218, v150\n"
    "v_exp_f32 v219, v151\n"
    "v_mfma_f32_16x16x32_bf16 v[60:63], a[204:207], v[172:175], v[60:63]\n"
    "s_add_u32 m0, s76, 118784\n"
    "v_cvt_pk_bf16_f32 v178, v222, v223\n"
    "v_cvt_pk_bf16_f32 v179, v218, v219\n"
    "s_nop 1\n"
    "v_mfma_f32_16x16x32_bf16 v[208:211], a[240:243], v[176:179], 0\n"
    "s_waitcnt lgkmcnt(8)\n"
    "buffer_load_dwordx4 v252, s[48:51], s60 offen offset:1024 nt lds\n"
    "v_mfma_f32_16x16x32_bf16 v[128:131], a[80:83], a[0:3], v[188:191]\n"
    "ds_read_b64_tr_b16 a[160:161], v248 offset:512\n"
    "ds_read_b64_tr_b16 a[162:163], v248 offset:4608\n"
    "v_mfma_f32_16x16x32_bf16 v[128:131], a[84:87], a[4:7], v[128:131]\n"
    "ds_read_b64_tr_b16 a[164:165], v249 offset:512\n"
    "ds_read_b64_tr_b16 a[166:167], v249 offset:4608\n"
    "v_mfma_f32_16x16x32_bf16 v[128:131], a[88:91], a[8:11], v[128:131]\n"
    "s_add_u32 m0, s76, 53248\n"
    "ds_read_b64_tr_b16 a[168:169], v248 offset:768\n"
    "v_mfma_f32_16x16x32_bf16 v[128:131], a[92:95], a[12:15], v[128:131]\n"
    "buffer_load_dwordx4 v252, s[44:47], s60 offen offset:2048 nt lds\n"
    "v_mfma_f32_16x16x32_bf16 v[132:135], a[96:99], a[0:3], v[188:191]\n"
    "v_cmp_nge_f32 vcc, s70, v208\n"
    "ds_read_b64_tr_b16 a[170:171], v248 offset:4864\n"
    "v_mfma_f32_16x16x32_bf16 v[132:135], a[100:103], a[4:7], v[132:135]\n"
    "v_exp_f32 v218, v152\n"
    "v_mfma_f32_16x16x32_bf16 v[132:135], a[104:107], a[8:11], v[132:135]\n"
    "s_cbranch_vccnz Lstub1_3_3\n"
    "Lback1_3_3:\n"
    "v_add_f32 v214, v214, v208\n"
    "s_add_u32 m0, s76, 118784\n"
    "v_mfma_f32_16x16x32_bf16 v[132:135], a[108:111], a[12:15], v[132:135]\n"
    "buffer_load_dwordx4 v250, s[48:51], s60 offen offset:2048 nt lds\n"
    "v_mfma_f32_16x16x32_bf16 v[64:67], a[176:179], v[176:179], v[64:67]\n"
    "v_exp_f32 v219, v153\n"
    "v_mfma_f32_16x16x32_bf16 v[68:71], a[180:183], v[176:179], v[68:71]\n"
    "v_exp_f32 v220, v154\n"
    "v_mfma_f32_16x16x32_bf16 v[72:75], a[184:187], v[176:179], v[72:75]\n"
    "ds_read_b64_tr_b16 a[172:173], v249 offset:768\n"
    "ds_read_b64_tr_b16 a[174:175], v249 offset:4864\n"
    "s_add_u32 m0, s76, 53248\n"
    "v_mfma_f32_16x16x32_bf16 v[76:79], a[188:191], v[176:179], v[76:79]\n"
    "v_exp_f32 v221, v155\n"
    "buffer_load_dwordx4 v253, s[44:47], s60 offen offset:3072 nt lds\n"
    "v_mfma_f32_16x16x32_bf16 v[80:83], a[192:195], v[176:179], v[80:83]\n"
    "v_cvt_pk_bf16_f32 v180, v218, v219\n"
    "v_exp_f32 v222, v156\n"
    "v_mfma_f32_16x16x32_bf16 v[84:87], a[196:199], v[176:179], v[84:87]\n"
    "v_exp_f32 v223, v157\n"
    "v_cvt_pk_bf16_f32 v181, v220, v221\n"
    "v_mfma_f32_16x16x32_bf16 v[88:91], a[200:203], v[176:179], v[88:91]\n"
    "s_add_u32 m0, s76, 118784\n"
    "v_exp_f32 v218, v158\n"
    "v_exp_f32 v219, v159\n"
    "v_mfma_f32_16x16x32_bf16 v[92:95], a[204:207], v[176:179], v[92:95]\n"
    "v_cvt_pk_bf16_f32 v182, v222, v223\n"
    "v_cvt_pk_bf16_f32 v183, v218, v219\n"
    "buffer_load_dwordx4 v252, s[48:51], s60 offen offset:3072 nt lds\n"
    "s_nop 0\n"
    "v_mfma_f32_16x16x32_bf16 v[208:211], a[240:243], v[180:183], 0\n"
    "s_add_u32 s59, s59, 8192\n"
    "s_and_b32 s59, s59, 0x3ffff\n"
    "s_add_u32 s60, s59, 4096\n"
    "; ---- LS8\n"
    "v_mfma_f32_16x16x32_bf16 v[136:139], a[80:83], a[16:19], v[192:195]\n"
    "Lpace0:\n"
    "s_memrealtime s[98:99]\n"
    "s_waitcnt vmcnt(32) lgkmcnt(0)\n"
    "s_sub_u32 s95, s98, s79\n"
    "s_cmp_lt_i32 s95, 0\n"
    "s_cbranch_scc1 Lpace0\n"
    "s_min_u32 s95, s95, 12\n"
    "s_sub_u32 s79, s98, s95\n"
    "s_add_u32 s79, s79, 110\n"
    "s_cmp_eq_u32 s63, 1\n"
    "s_cselect_b32 s79, s98, s79\n"
    "s_barrier\n"
    "s_cmp_eq_u32 s63, 1\n"
    "s_cselect_b32 s46, 0, s46\n"
    "s_mov_b32 s50, s46\n"
    "v_mfma_f32_16x16x32_bf16 v[136:139], a[84:87], a[20:23], v[136:139]\n"
    "v_exp_f32 v218, v128\n"
    "v_mfma_f32_16x16x32_bf16 v[136:139], a[88:91], a[24:27], v[136:139]\n"
    "v_exp_f32 v219, v129\n"
    "v_mfma_f32_16x16x32_bf16 v[136:139], a[92:95], a[28:31], v[136:139]\n"
    "ds_read_b128 a[112:115], v246 offset:16384\n"
    "s_add_u32 m0, s76, 0\n"
    "v_mfma_f32_16x16x32_bf16 v[140:143], a[96:99], a[16:19], v[192:195]\n"
    "v_cmp_nge_f32 vcc, s70, v208\n"
    "ds_read_b128 a[116:119], v246 offset:16640\n"
    "v_mfma_f32_16x16x32_bf16 v[140:143], a[100:103], a[20:23], v[140:143]\n"
    "buffer_load_dwordx4 v250, s[44:47], s59 offen nt lds\n"
    "v_mfma_f32_16x16x32_bf16 v[140:143], a[104:107], a[24:27], v[140:143]\n"
    "s_cbranch_vccnz Lstub1_0_0\n"
    "Lback1_0_0:\n"
    "v_add_f32 v215, v215, v208\n"
    "v_mfma_f32_16x16x32_bf16 v[140:143], a[108:111], a[28:31], v[140:143]\n"
    "v_exp_f32 v220, v130\n"
    "v_mfma_f32_16x16x32_bf16 v[96:99], a[176:179], v[180:183], v[96:99]\n"
    "s_add_u32 m0, s76, 65536\n"
    "ds_read_b128 a[120:123], v247 offset:16384\n"
    "v_mfma_f32_16x16x32_bf16 v[100:103], a[180:183], v[180:183], v[100:103]\n"
    "buffer_load_dwordx4 v250, s[48:51], s59 offen nt lds\n"
    "v_mfma_f32_16x16x32_bf16 v[104:107], a[184:187], v[180:183], v[104:107]\n"
    "v_exp_f32 v221, v131\n"
    "v_mfma_f32_16x16x32_bf16 v[108:111], a[188:191], v[180:183], v[108:111]\n"
    "v_cvt_pk_bf16_f32 v168, v218, v219\n"
    "s_add_u32 m0, s76, 0\n"
    "v_mfma_f32_16x16x32_bf16 v[112:115], a[192:195], v[180:183], v[112:115]\n"
    "v_exp_f32 v222, v132\n"
    "v_mfma_f32_16x16x32_bf16 v[116:119], a[196:199], v[180:183], v[116:119]\n"
    "v_exp_f32 v223, v133\n"
    "v_cvt_pk_bf16_f32 v169, v220, v221\n"
    "buffer_load_dwordx4 v251, s[44:47], s59 offen offset:1024 nt lds\n"
    "v_mfma_f32_16x16x32_bf16 v[120:123], a[200:203], v[180:183], v[120:123]\n"
    "v_exp_f32 v218, v134\n"
    "v_exp_f32 v219, v135\n"
    "v_mfma_f32_16x16x32_bf16 v[124:127], a[204:207], v[180:183], v[124:127]\n"
    "v_cvt_pk_bf16_f32 v170, v222, v223\n"
    "v_cvt_pk_bf16_f32 v171, v218, v219\n"
    "s_nop 1\n"
    "v_mfma_f32_16x16x32_bf16 v[208:211], a[240:243], v[168:171], 0\n"
    "s_add_u32 m0, s76, 65536\n"
    "ds_read_b128 a[124:127], v247 offset:16640\n"
    "v_mfma_f32_16x16x32_bf16 v[144:147], a[80:83], a[32:35], v[196:199]\n"
    "buffer_load_dwordx4 v252, s[48:51], s59 offen offset:1024 nt lds\n"
    "v_mfma_f32_16x16x32_bf16 v[144:147], a[84:87], a[36:39], v[144:147]\n"
    "ds_read_b128 a[128:131], v246 offset:20480\n"
    "ds_read_b128 a[132:135], v246 offset:20736\n"
    "v_mfma_f32_16x16x32_bf16 v[144:147], a[88:91], a[40:43], v[144:147]\n"
    "s_add_u32 m0, s76, 0\n"
    "ds_read_b128 a[136:139], v247 offset:20480\n"
    "v_mfma_f32_16x16x32_bf16 v[144:147], a[92:95], a[44:47], v[144:147]\n"
    "v_exp_f32 v218, v136\n"
    "v_mfma_f32_16x16x32_bf16 v[148:151], a[96:99], a[32:35], v[196:199]\n"
    "v_cmp_nge_f32 vcc, s70, v208\n"
    "buffer_load_dwordx4 v252, s[44:47], s59 offen offset:2048 nt lds\n"
    "v_mfma_f32_16x16x32_bf16 v[148:151], a[100:103], a[36:39], v[148:151]\n"
    "v_exp_f32 v219, v137\n"
    "v_mfma_f32_16x16x32_bf16 v[148:151], a[104:107], a[40:43], v[148:151]\n"
    "s_cbranch_vccnz Lstub1_0_1\n"
    "Lback1_0_1:\n"
    "v_add_f32 v212, v212, v208\n"
    "v_mfma_f32_16x16x32_bf16 v[148:151], a[108:111], a[44:47], v[148:151]\n"
    "s_add_u32 m0, s76, 65536\n"
    "ds_read_b128 a[140:143], v247 offset:20736\n"
    "v_mfma_f32_16x16x32_bf16 v[0:3], a[144:147], v[168:171], v[0:3]\n"
    "buffer_load_dwordx4 v250, s[48:51], s59 offen offset:2048 nt lds\n"
    "v_mfma_f32_16x16x32_bf16 v[4:7], a[148:151], v[168:171], v[4:7]\n"
    "v_exp_f32 v220, v138\n"
    "v_mfma_f32_16x16x32_bf16 v[8:11], a[152:155], v[168:171], v[8:11]\n"
    "v_exp_f32 v221, v139\n"
    "v_mfma_f32_16x16x32_bf16 v[12:15], a[156:159], v[168:171], v[12:15]\n"
    "s_add_u32 m0, s76, 0\n"
    "v_cvt_pk_bf16_f32 v172, v218, v219\n"
    "v_mfma_f32_16x16x32_bf16 v[16:19], a[160:163], v[168:171], v[16:19]\n"
    "v_exp_f32 v222, v140\n"
    "buffer_load_dwordx4 v253, s[44:47], s59 offen offset:3072 nt lds\n"
    "v_mfma_f32_16x16x32_bf16 v[20:23], a[164:167], v[168:171], v[20:23]\n"
    "v_exp_f32 v223, v141\n"
    "v_cvt_pk_bf16_f32 v173, v220, v221\n"
    "v_mfma_f32_16x16x32_bf16 v[24:27], a[168:171], v[168:171], v[24:27]\n"
    "v_exp_f32 v218, v142\n"
    "v_exp_f32 v219, v143\n"
    "v_mfma_f32_16x16x32_bf16 v[28:31], a[172:175], v[168:171], v[28:31]\n"
    "s_add_u32 m0, s76, 65536\n"
    "v_cvt_pk_bf16_f32 v174, v222, v223\n"
    "v_cvt_pk_bf16_f32 v175, v218, v219\n"
    "s_nop 1\n"
    "v_mfma_f32_16x16x32_bf16 v[208:211], a[240:243], v[172:175], 0\n"
    "buffer_load_dwordx4 v252, s[48:51], s59 offen offset:3072 nt lds\n"
    "v_mfma_f32_16x16x32_bf16 v[152:155], a[80:83], a[48:51], v[200:203]\n"
    "ds_read_b64_tr_b16 a[176:177], v248 offset:16384\n"
    "ds_read_b64_tr_b16 a[178:179], v248 offset:20480\n"
    "v_mfma_f32_16x16x32_bf16 v[152:155], a[84:87], a[52:55], v[152:155]\n"
    "ds_read_b64_tr_b16 a[180:181], v249 offset:16384\n"
    "ds_read_b64_tr_b16 a[182:183], v249 offset:20480\n"
    "v_mfma_f32_16x16x32_bf16 v[152:155], a[88:91], a[56:59], v[152:155]\n"
    "s_add_u32 m0, s76, 4096\n"
    "ds_read_b64_tr_b16 a[184:185], v248 offset:16640\n"
    "v_mfma_f32_16x16x32_bf16 v[152:155], a[92:95], a[60:63], v[152:155]\n"
    "ds_read_b64_tr_b16 a[186:187], v248 offset:20736\n"
    "ds_read_b64_tr_b16 a[188:189], v249 offset:16640\n"
    "v_mfma_f32_16x16x32_bf16 v[156:159], a[96:99], a[48:51], v[200:203]\n"
    "v_cmp_nge_f32 vcc, s70, v208\n"
    "buffer_load_dwordx4 v250, s[44:47], s60 offen nt lds\n"
    "v_mfma_f32_16x16x32_bf16 v[156:159], a[100:103], a[52:55], v[156:159]\n"
    "v_exp_f32 v218, v144\n"
    "v_mfma_f32_16x16x32_bf16 v[156:159], a[104:107], a[56:59], v[156:159]\n"
    "s_cbranch_vccnz Lstub1_0_2\n"
    "Lback1_0_2:\n"
    "v_add_f32 v213, v213, v208\n"
    "v_mfma_f32_16x16x32_bf16 v[156:159], a[108:111], a[60:63], v[156:159]\n"
    "s_add_u32 m0, s76, 69632\n"
    "ds_read_b64_tr_b16 a[190:191], v249 offset:20736\n"
    "v_mfma_f32_16x16x32_bf16 v[32:35], a[144:147], v[172:175], v[32:35]\n"
    "buffer_load_dwordx4 v250, s[48:51], s60 offen nt lds\n"
    "v_mfma_f32_16x16x32_bf16 v[36:39], a[148:151], v[172:175], v[36:39]\n"
    "v_exp_f32 v219, v145\n"
    "v_mfma_f32_16x16x32_bf16 v[40:43], a[152:155], v[172:175], v[40:43]\n"
    "v_exp_f32 v220, v146\n"
    "v_mfma_f32_16x16x32_bf16 v[44:47], a[156:159], v[172:175], v[44:47]\n"
    "v_exp_f32 v221, v147\n"
    "s_add_u32 m0, s76, 4096\n"
    "v_mfma_f32_16x16x32_bf16 v[48:51], a[160:163], v[172:175], v[48:51]\n"
    "v_cvt_pk_bf16_f32 v176, v218, v219\n"
    "v_exp_f32 v222, v148\n"
    "buffer_load_dwordx4 v251, s[44:47], s60 offen offset:1024 nt lds\n"
    "v_mfma_f32_16x16x32_bf16 v[52:55], a[164:167], v[172:175], v[52:55]\n"
    "v_exp_f32 v223, v149\n"
    "v_cvt_pk_bf16_f32 v177, v220, v221\n"
    "v_mfma_f32_16x16x32_bf16 v[56:59], a[168:171], v[172:175], v[56:59]\n"
    "v_exp_f32 v218, v150\n"
    "v_exp_f32 v219, v151\n"
    "v_mfma_f32_16x16x32_bf16 v[60:63], a[172:175], v[172:175], v[60:63]\n"
    "s_add_u32 m0, s76, 69632\n"
    "v_cvt_pk_bf16_f32 v178, v222, v223\n"
    "v_cvt_pk_bf16_f32 v179, v218, v219\n"
    "s_nop 1\n"
    "v_mfma_f32_16x16x32_bf16 v[208:211], a[240:243], v[176:179], 0\n"
    "s_waitcnt lgkmcnt(8)\n"
    "buffer_load_dwordx4 v252, s[48:51], s60 offen offset:1024 nt lds\n"
    "v_mfma_f32_16x16x32_bf16 v[128:131], a[112:115], a[0:3], v[188:191]\n"
    "ds_read_b64_tr_b16 a[192:193], v248 offset:16896\n"
    "ds_read_b64_tr_b16 a[194:195], v248 offset:20992\n"
    "v_mfma_f32_16x16x32_bf16 v[128:131], a[116:119], a[4:7], v[128:131]\n"
    "ds_read_b64_tr_b16 a[196:197], v249 offset:16896\n"
    "ds_read_b64_tr_b16 a[198:199], v249 offset:20992\n"
    "v_mfma_f32_16x16x32_bf16 v[128:131], a[120:123], a[8:11], v[128:131]\n"
    "s_add_u32 m0, s76, 4096\n"
    "ds_read_b64_tr_b16 a[200:201], v248 offset:17152\n"
    "v_mfma_f32_16x16x32_bf16 v[128:131], a[124:127], a[12:15], v[128:131]\n"
    "buffer_load_dwordx4 v252, s[44:47], s60 offen offset:2048 nt lds\n"
    "v_mfma_f32_16x16x32_bf16 v[132:135], a[128:131], a[0:3], v[188:191]\n"
    "v_cmp_nge_f32 vcc, s70, v208\n"
    "ds_read_b64_tr_b16 a[202:203], v248 offset:21248\n"
    "v_mfma_f32_16x16x32_bf16 v[132:135], a[132:135], a[4:7], v[132:135]\n"
    "v_exp_f32 v218, v152\n"
    "v_mfma_f32_16x16x32_bf16 v[132:135], a[136:139], a[8:11], v[132:135]\n"
    "s_cbranch_vccnz Lstub1_0_3\n"
    "Lback1_0_3:\n"
    "v_add_f32 v214, v214, v208\n"
    "s_add_u32 m0, s76, 69632\n"
    "v_mfma_f32_16x16x32_bf16 v[132:135], a[140:143], a[12:15], v[132:135]\n"
    "buffer_load_dwordx4 v250, s[48:51], s60 offen offset:2048 nt lds\n"
    "v_mfma_f32_16x16x32_bf16 v[64:67], a[144:147], v[176:179], v[64:67]\n"
    "v_exp_f32 v219, v153\n"
    "v_mfma_f32_16x16x32_bf16 v[68:71], a[148:151], v[176:179], v[68:71]\n"
    "v_exp_f32 v220, v154\n"
    "v_mfma_f32_16x16x32_bf16 v[72:75], a[152:155], v[176:179], v[72:75]\n"
    "ds_read_b64_tr_b16 a[204:205], v249 offset:17152\n"
    "ds_read_b64_tr_b16 a[206:207], v249 offset:21248\n"
    "s_add_u32 m0, s76, 4096\n"
    "v_mfma_f32_16x16x32_bf16 v[76:79], a[156:159], v[176:179], v[76:79]\n"
    "v_exp_f32 v221, v155\n"
    "buffer_load_dwordx4 v253, s[44:47], s60 offen offset:3072 nt lds\n"
    "v_mfma_f32_16x16x32_bf16 v[80:83], a[160:163], v[176:179], v[80:83]\n"
    "v_cvt_pk_bf16_f32 v180, v218, v219\n"
    "v_exp_f32 v222, v156\n"
    "v_mfma_f32_16x16x32_bf16 v[84:87], a[164:167], v[176:179], v[84:87]\n"
    "v_exp_f32 v223, v157\n"
    "v_cvt_pk_bf16_f32 v181, v220, v221\n"
    "v_mfma_f32_16x16x32_bf16 v[88:91], a[168:171], v[176:179], v[88:91]\n"
    "s_add_u32 m0, s76, 69632\n"
    "v_exp_f32 v218, v158\n"
    "v_exp_f32 v219, v159\n"
    "v_mfma_f32_16x16x32_bf16 v[92:95], a[172:175], v[176:179], v[92:95]\n"
    "v_cvt_pk_bf16_f32 v182, v222, v223\n"
    "v_cvt_pk_bf16_f32 v183, v218, v219\n"
    "buffer_load_dwordx4 v252, s[48:51], s60 offen offset:3072 nt lds\n"
    "s_nop 0\n"
    "v_mfma_f32_16x16x32_bf16 v[208:211], a[240:243], v[180:183], 0\n"
    "s_add_u32 s59, s59, 8192\n"
    "s_and_b32 s59, s59, 0x3ffff\n"
    "s_add_u32 s60, s59, 4096\n"
    "; ---- LS9\n"
    "v_mfma_f32_16x16x32_bf16 v[136:139], a[112:115], a[16:19], v[192:195]\n"
    "Lpace1:\n"
    "s_memrealtime s[98:99]\n"
    "s_waitcnt vmcnt(32) lgkmcnt(0)\n"
    "s_sub_u32 s95, s98, s79\n"
    "s_cmp_lt_i32 s95, 0\n"
    "s_cbranch_scc1 Lpace1\n"
    "s_min_u32 s95, s95, 12\n"
    "s_sub_u32 s79, s98, s95\n"
    "s_add_u32 s79, s79, 110\n"
    "s_barrier\n"
    "v_mfma_f32_16x16x32_bf16 v[136:139], a[116:119], a[20:23], v[136:139]\n"
    "v_exp_f32 v218, v128\n"
    "v_mfma_f32_16x16x32_bf16 v[136:139], a[120:123], a[24:27], v[136:139]\n"
    "v_exp_f32 v219, v129\n"
    "v_mfma_f32_16x16x32_bf16 v[136:139], a[124:127], a[28:31], v[136:139]\n"
    "ds_read_b128 a[80:83], v246 offset:32768\n"
    "s_add_u32 m0, s76, 16384\n"
    "v_mfma_f32_16x16x32_bf16 v[140:143], a[128:131], a[16:19], v[192:195]\n"
    "v_cmp_nge_f32 vcc, s70, v208\n"
    "ds_read_b128 a[84:87], v246 offset:33024\n"
    "v_mfma_f32_16x16x32_bf16 v[140:143], a[132:135], a[20:23], v[140:143]\n"
    "buffer_load_dwordx4 v250, s[44:47], s59 offen nt lds\n"
    "v_mfma_f32_16x16x32_bf16 v[140:143], a[136:139], a[24:27], v[140:143]\n"
    "s_cbranch_vccnz Lstub1_1_0\n"
    "Lback1_1_0:\n"
    "v_add_f32 v215, v215, v208\n"
    "v_mfma_f32_16x16x32_bf16 v[140:143], a[140:143], a[28:31], v[140:143]\n"
    "v_exp_f32 v220, v130\n"
    "v_mfma_f32_16x16x32_bf16 v[96:99], a[144:147], v[180:183], v[96:99]\n"
    "s_add_u32 m0, s76, 81920\n"
    "ds_read_b128 a[88:91], v247 offset:32768\n"
    "v_mfma_f32_16x16x32_bf16 v[100:103], a[148:151], v[180:183], v[100:103]\n"
    "buffer_load_dwordx4 v250, s[48:51], s59 offen nt lds\n"
    "v_mfma_f32_16x16x32_bf16 v[104:107], a[152:155], v[180:183], v[104:107]\n"
    "v_exp_f32 v221, v131\n"
    "v_mfma_f32_16x16x32_bf16 v[108:111], a[156:159], v[180:183], v[108:111]\n"
    "v_cvt_pk_bf16_f32 v168, v218, v219\n"
    "s_add_u32 m0, s76, 16384\n"
    "v_mfma_f32_16x16x32_bf16 v[112:115], a[160:163], v[180:183], v[112:115]\n"
    "v_exp_f32 v222, v132\n"
    "v_mfma_f32_16x16x32_bf16 v[116:119], a[164:167], v[180:183], v[116:119]\n"
    "v_exp_f32 v223, v133\n"
    "v_cvt_pk_bf16_f32 v169, v220, v221\n"
    "buffer_load_dwordx4 v251, s[44:47], s59 offen offset:1024 nt lds\n"
    "v_mfma_f32_16x16x32_bf16 v[120:123], a[168:171], v[180:183], v[120:123]\n"
    "v_exp_f32 v218, v134\n"
    "v_exp_f32 v219, v135\n"
    "v_mfma_f32_16x16x32_bf16 v[124:127], a[172:175], v[180:183], v[124:127]\n"
    "v_cvt_pk_bf16_f32 v170, v222, v223\n"
    "v_cvt_pk_bf16_f32 v171, v218, v219\n"
    "s_nop 1\n"
    "v_mfma_f32_16x16x32_bf16 v[208:211], a[240:243], v[168:171], 0\n"
    "s_add_u32 m0, s76, 81920\n"
    "ds_read_b128 a[92:95], v247 offset:33024\n"
    "v_mfma_f32_16x16x32_bf16 v[144:147], a[112:115], a[32:35], v[196:199]\n"
    "buffer_load_dwordx4 v252, s[48:51], s59 offen offset:1024 nt lds\n"
    "v_mfma_f32_16x16x32_bf16 v[144:147], a[116:119], a[36:39], v[144:147]\n"
    "ds_read_b128 a[96:99], v246 offset:36864\n"
    "ds_read_b128 a[100:103], v246 offset:37120\n"
    "v_mfma_f32_16x16x32_bf16 v[144:147], a[120:123], a[40:43], v[144:147]\n"
    "s_add_u32 m0, s76, 16384\n"
    "ds_read_b128 a[104:107], v247 offset:36864\n"
    "v_mfma_f32_16x16x32_bf16 v[144:147], a[124:127], a[44:47], v[144:147]\n"
    "v_exp_f32 v218, v136\n"
    "v_mfma_f32_16x16x32_bf16 v[148:151], a[128:131], a[32:35], v[196:199]\n"
    "v_cmp_nge_f32 vcc, s70, v208\n"
    "buffer_load_dwordx4 v252, s[44:47], s59 offen offset:2048 nt lds\n"
    "v_mfma_f32_16x16x32_bf16 v[148:151], a[132:135], a[36:39], v[148:151]\n"
    "v_exp_f32 v219, v137\n"
    "v_mfma_f32_16x16x32_bf16 v[148:151], a[136:139], a[40:43], v[148:151]\n"
    "s_cbranch_vccnz Lstub1_1_1\n"
    "Lback1_1_1:\n"
    "v_add_f32 v212, v212, v208\n"
    "v_mfma_f32_16x16x32_bf16 v[148:151], a[140:143], a[44:47], v[148:151]\n"
    "s_add_u32 m0, s76, 81920\n"
    "ds_read_b128 a[108:111], v247 offset:37120\n"
    "v_mfma_f32_16x16x32_bf16 v[0:3], a[176:179], v[168:171], v[0:3]\n"
    "buffer_load_dwordx4 v250, s[48:51], s59 offen offset:2048 nt lds\n"
    "v_mfma_f32_16x16x32_bf16 v[4:7], a[180:183], v[168:171], v[4:7]\n"
    "v_exp_f32 v220, v138\n"
    "v_mfma_f32_16x16x32_bf16 v[8:11], a[184:187], v[168:171], v[8:11]\n"
    "v_exp_f32 v221, v139\n"
    "v_mfma_f32_16x16x32_bf16 v[12:15], a[188:191], v[168:171], v[12:15]\n"
    "s_add_u32 m0, s76, 16384\n"
    "v_cvt_pk_bf16_f32 v172, v218, v219\n"
    "v_mfma_f32_16x16x32_bf16 v[16:19], a[192:195], v[168:171], v[16:19]\n"
    "v_exp_f32 v222, v140\n"
    "buffer_load_dwordx4 v253, s[44:47], s59 offen offset:3072 nt lds\n"
    "v_mfma_f32_16x16x32_bf16 v[20:23], a[196:199], v[168:171], v[20:23]\n"
    "v_exp_f32 v223, v141\n"
    "v_cvt_pk_bf16_f32 v173, v220, v221\n"
    "v_mfma_f32_16x16x32_bf16 v[24:27], a[200:203], v[168:171], v[24:27]\n"
    "v_exp_f32 v218, v142\n"
    "v_exp_f32 v219, v143\n"
    "v_mfma_f32_16x16x32_bf16 v[28:31], a[204:207], v[168:171], v[28:31]\n"
    "s_add_u32 m0, s76, 81920\n"
    "v_cvt_pk_bf16_f32 v174, v222, v223\n"
    "v_cvt_pk_bf16_f32 v175, v218, v219\n"
    "s_nop 1\n"
    "v_mfma_f32_16x16x32_bf16 v[208:211], a[240:243], v[172:175], 0\n"
    "buffer_load_dwordx4 v252, s[48:51], s59 offen offset:3072 nt lds\n"
    "v_mfma_f32_16x16x32_bf16 v[152:155], a[112:115], a[48:51], v[200:203]\n"
    "ds_read_b64_tr_b16 a[144:145], v248 offset:32768\n"
    "ds_read_b64_tr_b16 a[146:147], v248 offset:36864\n"
    "v_mfma_f32_16x16x32_bf16 v[152:155], a[116:119], a[52:55], v[152:155]\n"
    "ds_read_b64_tr_b16 a[148:149], v249 offset:32768\n"
    "ds_read_b64_tr_b16 a[150:151], v249 offset:36864\n"
    "v_mfma_f32_16x16x32_bf16 v[152:155], a[120:123], a[56:59], v[152:155]\n"
    "s_add_u32 m0, s76, 20480\n"
    "ds_read_b64_tr_b16 a[152:153], v248 offset:33024\n"
    "v_mfma_f32_16x16x32_bf16 v[152:155], a[124:127], a[60:63], v[152:155]\n"
    "ds_read_b64_tr_b16 a[154:155], v248 offset:37120\n"
    "ds_read_b64_tr_b16 a[156:157], v249 offset:33024\n"
    "v_mfma_f32_16x16x32_bf16 v[156:159], a[128:131], a[48:51], v[200:203]\n"
    "v_cmp_nge_f32 vcc, s70, v208\n"
    "buffer_load_dwordx4 v250, s[44:47], s60 offen nt lds\n"
    "v_mfma_f32_16x16x32_bf16 v[156:159], a[132:135], a[52:55], v[156:159]\n"
    "v_exp_f32 v218, v144\n"
    "v_mfma_f32_16x16x32_bf16 v[156:159], a[136:139], a[56:59], v[156:159]\n"
    "s_cbranch_vccnz Lstub1_1_2\n"
    "Lback1_1_2:\n"
    "v_add_f32 v213, v213, v208\n"
    "v_mfma_f32_16x16x32_bf16 v[156:159], a[140:143], a[60:63], v[156:159]\n"
    "s_add_u32 m0, s76, 86016\n"
    "ds_read_b64_tr_b16 a[158:159], v249 offset:37120\n"
    "v_mfma_f32_16x16x32_bf16 v[32:35], a[176:179], v[172:175], v[32:35]\n"
    "buffer_load_dwordx4 v250, s[48:51], s60 offen nt lds\n"
    "v_mfma_f32_16x16x32_bf16 v[36:39], a[180:183], v[172:175], v[36:39]\n"
    "v_exp_f32 v219, v145\n"
    "v_mfma_f32_16x16x32_bf16 v[40:43], a[184:187], v[172:175], v[40:43]\n"
    "v_exp_f32 v220, v146\n"
    "v_mfma_f32_16x16x32_bf16 v[44:47], a[188:191], v[172:175], v[44:47]\n"
    "v_exp_f32 v221, v147\n"
    "s_add_u32 m0, s76, 20480\n"
    "v_mfma_f32_16x16x32_bf16 v[48:51], a[192:195], v[172:175], v[48:51]\n"
    "v_cvt_pk_bf16_f32 v176, v218, v219\n"
    "v_exp_f32 v222, v148\n"
    "buffer_load_dwordx4 v251, s[44:47], s60 offen offset:1024 nt lds\n"
    "v_mfma_f32_16x16x32_bf16 v[52:55], a[196:199], v[172:175], v[52:55]\n"
    "v_exp_f32 v223, v149\n"
    "v_cvt_pk_bf16_f32 v177, v220, v221\n"
    "v_mfma_f32_16x16x32_bf16 v[56:59], a[200:203], v[172:175], v[56:59]\n"
    "v_exp_f32 v218, v150\n"
    "v_exp_f32 v219, v151\n"
    "v_mfma_f32_16x16x32_bf16 v[60:63], a[204:207], v[172:175], v[60:63]\n"
    "s_add_u32 m0, s76, 86016\n"
    "v_cvt_pk_bf16_f32 v178, v222, v223\n"
    "v_cvt_pk_bf16_f32 v179, v218, v219\n"
    "s_nop 1\n"
    "v_mfma_f32_16x16x32_bf16 v[208:211], a[240:243], v[176:179], 0\n"
    "s_waitcnt lgkmcnt(8)\n"
    "buffer_load_dwordx4 v252, s[48:51], s60 offen offset:1024 nt lds\n"
    "v_mfma_f32_16x16x32_bf16 v[128:131], a[80:83], a[0:3], v[188:191]\n"
    "ds_read_b64_tr_b16 a[160:161], v248 offset:33280\n"
    "ds_read_b64_tr_b16 a[162:163], v248 offset:37376\n"
    "v_mfma_f32_16x16x32_bf16 v[128:131], a[84:87], a[4:7], v[128:131]\n"
    "ds_read_b64_tr_b16 a[164:165], v249 offset:33280\n"
    "ds_read_b64_tr_b16 a[166:167], v249 offset:37376\n"
    "v_mfma_f32_16x16x32_bf16 v[128:131], a[88:91], a[8:11], v[128:131]\n"
    "s_add_u32 m0, s76, 20480\n"
    "ds_read_b64_tr_b16 a[168:169], v248 offset:33536\n"
    "v_mfma_f32_16x16x32_bf16 v[128:131], a[92:95], a[12:15], v[128:131]\n"
    "buffer_load_dwordx4 v252, s[44:47], s60 offen offset:2048 nt lds\n"
    "v_mfma_f32_16x16x32_bf16 v[132:135], a[96:99], a[0:3], v[188:191]\n"
    "v_cmp_nge_f32 vcc, s70, v208\n"
    "ds_read_b64_tr_b16 a[170:171], v248 offset:37632\n"
    "v_mfma_f32_16x16x32_bf16 v[132:135], a[100:103], a[4:7], v[132:135]\n"
    "v_exp_f32 v218, v152\n"
    "v_mfma_f32_16x16x32_bf16 v[132:135], a[104:107], a[8:11], v[132:135]\n"
    "s_cbranch_vccnz Lstub1_1_3\n"
    "Lback1_1_3:\n"
    "v_add_f32 v214, v214, v208\n"
    "s_add_u32 m0, s76, 86016\n"
    "v_mfma_f32_16x16x32_bf16 v[132:135], a[108:111], a[12:15], v[132:135]\n"
    "buffer_load_dwordx4 v250, s[48:51], s60 offen offset:2048 nt lds\n"
    "v_mfma_f32_16x16x32_bf16 v[64:67], a[176:179], v[176:179], v[64:67]\n"
    "v_exp_f32 v219, v153\n"
    "v_mfma_f32_16x16x32_bf16 v[68:71], a[180:183], v[176:179], v[68:71]\n"
    "v_exp_f32 v220, v154\n"
    "v_mfma_f32_16x16x32_bf16 v[72:75], a[184:187], v[176:179], v[72:75]\n"
    "ds_read_b64_tr_b16 a[172:173], v249 offset:33536\n"
    "ds_read_b64_tr_b16 a[174:175], v249 offset:37632\n"
    "s_add_u32 m0, s76, 20480\n"
    "v_mfma_f32_16x16x32_bf16 v[76:79], a[188:191], v[176:179], v[76:79]\n"
    "v_exp_f32 v221, v155\n"
    "buffer_load_dwordx4 v253, s[44:47], s60 offen offset:3072 nt lds\n"
    "v_mfma_f32_16x16x32_bf16 v[80:83], a[192:195], v[176:179], v[80:83]\n"
    "v_cvt_pk_bf16_f32 v180, v218, v219\n"
    "v_exp_f32 v222, v156\n"
    "v_mfma_f32_16x16x32_bf16 v[84:87], a[196:199], v[176:179], v[84:87]\n"
    "v_exp_f32 v223, v157\n"
    "v_cvt_pk_bf16_f32 v181, v220, v221\n"
    "v_mfma_f32_16x16x32_bf16 v[88:91], a[200:203], v[176:179], v[88:91]\n"
    "s_add_u32 m0, s76, 86016\n"
    "v_exp_f32 v218, v158\n"
    "v_exp_f32 v219, v159\n"
    "v_mfma_f32_16x16x32_bf16 v[92:95], a[204:207], v[176:179], v[92:95]\n"
    "v_cvt_pk_bf16_f32 v182, v222, v223\n"
    "v_cvt_pk_bf16_f32 v183, v218, v219\n"
    "buffer_load_dwordx4 v252, s[48:51], s60 offen offset:3072 nt lds\n"
    "s_nop 0\n"
    "v_mfma_f32_16x16x32_bf16 v[208:211], a[240:243], v[180:183], 0\n"
    "s_add_u32 s59, s59, 8192\n"
    "s_and_b32 s59, s59, 0x3ffff\n"
    "s_add_u32 s60, s59, 4096\n"
    "s_sub_u32 s63, s63, 1\n"
    "s_cmp_lg_u32 s63, 0\n"
    "s_cbranch_scc1 Lloop1\n"
    "; ---- LS30\n"
    "v_mfma_f32_16x16x32_bf16 v[136:139], a[80:83], a[16:19], v[192:195]\n"
    "s_waitcnt vmcnt(0) lgkmcnt(0)\n"
    "s_barrier\n"
    "v_mfma_f32_16x16x32_bf16 v[136:139], a[84:87], a[20:23], v[136:139]\n"
    "v_exp_f32 v218, v128\n"
    "v_mfma_f32_16x16x32_bf16 v[136:139], a[88:91], a[24:27], v[136:139]\n"
    "v_exp_f32 v219, v129\n"
    "v_mfma_f32_16x16x32_bf16 v[136:139], a[92:95], a[28:31], v[136:139]\n"
    "ds_read_b128 a[112:115], v246 offset:49152\n"
    "s_lshl_b32 s58, s84, 2\n"
    "v_mfma_f32_16x16x32_bf16 v[140:143], a[96:99], a[16:19], v[192:195]\n"
    "v_cmp_nge_f32 vcc, s70, v208\n"
    "v_mbcnt_lo_u32_b32 v234, -1, 0\n"
    "v_mfma_f32_16x16x32_bf16 v[140:143], a[100:103], a[20:23], v[140:143]\n"
    "s_add_u32 s58, s58, 1\n"
    "s_mul_i32 s58, s58, 81920\n"
    "v_mfma_f32_16x16x32_bf16 v[140:143], a[104:107], a[24:27], v[140:143]\n"
    "s_cbranch_vccnz Lstub1_c_0\n"
    "Lback1_c_0:\n"
    "v_add_f32 v215, v215, v208\n"
    "s_add_u32 s58, s58, s88\n"
    "s_add_u32 m0, s85, 8192\n"
    "v_mfma_f32_16x16x32_bf16 v[140:143], a[108:111], a[28:31], v[140:143]\n"
    "buffer_load_dwordx4 v236, s[36:39], s58 offen lds\n"
    "v_mfma_f32_16x16x32_bf16 v[96:99], a[176:179], v[180:183], v[96:99]\n"
    "v_exp_f32 v220, v130\n"
    "v_mfma_f32_16x16x32_bf16 v[100:103], a[180:183], v[180:183], v[100:103]\n"
    "v_exp_f32 v221, v131\n"
    "v_mfma_f32_16x16x32_bf16 v[104:107], a[184:187], v[180:183], v[104:107]\n"
    "buffer_load_dwordx4 v237, s[36:39], s58 offen offset:1024 lds\n"
    "v_mbcnt_hi_u32_b32 v234, -1, v234\n"
    "v_mfma_f32_16x16x32_bf16 v[108:111], a[188:191], v[180:183], v[108:111]\n"
    "ds_read_b128 a[116:119], v246 offset:49408\n"
    "v_cvt_pk_bf16_f32 v168, v218, v219\n"
    "v_mfma_f32_16x16x32_bf16 v[112:115], a[192:195], v[180:183], v[112:115]\n"
    "v_exp_f32 v222, v132\n"
    "v_mfma_f32_16x16x32_bf16 v[116:119], a[196:199], v[180:183], v[116:119]\n"
    "v_exp_f32 v223, v133\n"
    "v_cvt_pk_bf16_f32 v169, v220, v221\n"
    "buffer_load_dwordx4 v238, s[36:39], s58 offen offset:2048 lds\n"
    "v_mfma_f32_16x16x32_bf16 v[120:123], a[200:203], v[180:183], v[120:123]\n"
    "v_exp_f32 v218, v134\n"
    "v_exp_f32 v219, v135\n"
    "v_mfma_f32_16x16x32_bf16 v[124:127], a[204:207], v[180:183], v[124:127]\n"
    "v_cvt_pk_bf16_f32 v170, v222, v223\n"
    "v_cvt_pk_bf16_f32 v171, v218, v219\n"
    "s_nop 1\n"
    "v_mfma_f32_16x16x32_bf16 v[208:211], a[240:243], v[168:171], 0\n"
    "buffer_load_dwordx4 v239, s[36:39], s58 offen offset:3072 lds\n"
    "v_lshlrev_b32 v234, 4, v234\n"
    "v_mfma_f32_16x16x32_bf16 v[144:147], a[80:83], a[32:35], v[196:199]\n"
    "ds_read_b128 a[120:123], v247 offset:49152\n"
    "ds_read_b128 a[124:127], v247 offset:49408\n"
    "v_mfma_f32_16x16x32_bf16 v[144:147], a[84:87], a[36:39], v[144:147]\n"
    "s_add_u32 m0, s85, 12288\n"
    "ds_read_b128 a[128:131], v246 offset:53248\n"
    "v_mfma_f32_16x16x32_bf16 v[144:147], a[88:91], a[40:43], v[144:147]\n"
    "buffer_load_dwordx4 v240, s[36:39], s58 offen lds\n"
    "v_mfma_f32_16x16x32_bf16 v[144:147], a[92:95], a[44:47], v[144:147]\n"
    "ds_read_b128 a[132:135], v246 offset:53504\n"
    "ds_read_b128 a[136:139], v247 offset:53248\n"
    "v_mfma_f32_16x16x32_bf16 v[148:151], a[96:99], a[32:35], v[196:199]\n"
    "v_cmp_nge_f32 vcc, s70, v208\n"
    "s_sub_u32 s58, s58, 20480\n"
    "v_mfma_f32_16x16x32_bf16 v[148:151], a[100:103], a[36:39], v[148:151]\n"
    "s_add_u32 m0, s85, 3072\n"
    "ds_read_b128 a[140:143], v247 offset:53504\n"
    "v_mfma_f32_16x16x32_bf16 v[148:151], a[104:107], a[40:43], v[148:151]\n"
    "s_cbranch_vccnz Lstub1_c_1\n"
    "Lback1_c_1:\n"
    "v_add_f32 v212, v212, v208\n"
    "buffer_load_dwordx4 v236, s[36:39], s58 offen lds\n"
    "s_lshl_b32 s65, s84, 1\n"
    "v_mfma_f32_16x16x32_bf16 v[148:151], a[108:111], a[44:47], v[148:151]\n"
    "v_exp_f32 v218, v136\n"
    "v_mfma_f32_16x16x32_bf16 v[0:3], a[144:147], v[168:171], v[0:3]\n"
    "v_exp_f32 v219, v137\n"
    "v_mfma_f32_16x16x32_bf16 v[4:7], a[148:151], v[168:171], v[4:7]\n"
    "buffer_load_dwordx4 v237, s[36:39], s58 offen offset:1024 lds\n"
    "v_mfma_f32_16x16x32_bf16 v[8:11], a[152:155], v[168:171], v[8:11]\n"
    "v_exp_f32 v220, v138\n"
    "v_mfma_f32_16x16x32_bf16 v[12:15], a[156:159], v[168:171], v[12:15]\n"
    "v_exp_f32 v221, v139\n"
    "v_mfma_f32_16x16x32_bf16 v[16:19], a[160:163], v[168:171], v[16:19]\n"
    "v_cvt_pk_bf16_f32 v172, v218, v219\n"
    "v_exp_f32 v222, v140\n"
    "buffer_load_dwordx4 v238, s[36:39], s58 offen offset:2048 lds\n"
    "s_add_u32 s65, s65, s83\n"
    "v_mfma_f32_16x16x32_bf16 v[20:23], a[164:167], v[168:171], v[20:23]\n"
    "v_exp_f32 v223, v141\n"
    "v_cvt_pk_bf16_f32 v173, v220, v221\n"
    "v_mfma_f32_16x16x32_bf16 v[24:27], a[168:171], v[168:171], v[24:27]\n"
    "v_exp_f32 v218, v142\n"
    "v_exp_f32 v219, v143\n"
    "v_mfma_f32_16x16x32_bf16 v[28:31], a[172:175], v[168:171], v[28:31]\n"
    "v_cvt_pk_bf16_f32 v174, v222, v223\n"
    "v_cvt_pk_bf16_f32 v175, v218, v219\n"
    "buffer_load_dwordx4 v239, s[36:39], s58 offen offset:3072 lds\n"
    "s_nop 0\n"
    "v_mfma_f32_16x16x32_bf16 v[208:211], a[240:243], v[172:175], 0\n"
    "ds_read_b64_tr_b16 a[176:177], v248 offset:49152\n"
    "ds_read_b64_tr_b16 a[178:179], v248 offset:53248\n"
    "v_mfma_f32_16x16x32_bf16 v[152:155], a[80:83], a[48:51], v[200:203]\n"
    "s_add_u32 m0, s85, 7168\n"
    "ds_read_b64_tr_b16 a[180:181], v249 offset:49152\n"
    "v_mfma_f32_16x16x32_bf16 v[152:155], a[84:87], a[52:55], v[152:155]\n"
    "buffer_load_dwordx4 v240, s[36:39], s58 offen lds\n"
    "v_mfma_f32_16x16x32_bf16 v[152:155], a[88:91], a[56:59], v[152:155]\n"
    "s_mul_i32 s65, s65, 36864\n"
    "ds_read_b64_tr_b16 a[182:183], v249 offset:53248\n"
    "v_mfma_f32_16x16x32_bf16 v[152:155], a[92:95], a[60:63], v[152:155]\n"
    "ds_read_b64_tr_b16 a[184:185], v248 offset:49408\n"
    "s_lshl_b32 s58, s84, 2\n"
    "v_mfma_f32_16x16x32_bf16 v[156:159], a[96:99], a[48:51], v[200:203]\n"
    "v_cmp_nge_f32 vcc, s70, v208\n"
    "s_add_u32 s58, s58, 2\n"
    "s_mul_i32 s58, s58, 81920\n"
    "s_add_u32 s58, s58, s88\n"
    "s_add_u32 m0, s85, 65536\n"
    "v_mfma_f32_16x16x32_bf16 v[156:159], a[100:103], a[52:55], v[156:159]\n"
    "buffer_load_dwordx4 v236, s[36:39], s58 offen lds\n"
    "v_mfma_f32_16x16x32_bf16 v[156:159], a[104:107], a[56:59], v[156:159]\n"
    "s_cbranch_vccnz Lstub1_c_2\n"
    "Lback1_c_2:\n"
    "v_add_f32 v213, v213, v208\n"
    "v_mfma_f32_16x16x32_bf16 v[156:159], a[108:111], a[60:63], v[156:159]\n"
    "ds_read_b64_tr_b16 a[186:187], v248 offset:53504\n"
    "ds_read_b64_tr_b16 a[188:189], v249 offset:49408\n"
    "v_mfma_f32_16x16x32_bf16 v[32:35], a[144:147], v[172:175], v[32:35]\n"
    "buffer_load_dwordx4 v237, s[36:39], s58 offen offset:1024 lds\n"
    "s_add_u32 s65, s65, 16384\n"
    "v_mfma_f32_16x16x32_bf16 v[36:39], a[148:151], v[172:175], v[36:39]\n"
    "v_exp_f32 v218, v144\n"
    "v_mfma_f32_16x16x32_bf16 v[40:43], a[152:155], v[172:175], v[40:43]\n"
    "v_exp_f32 v219, v145\n"
    "v_mfma_f32_16x16x32_bf16 v[44:47], a[156:159], v[172:175], v[44:47]\n"
    "v_exp_f32 v220, v146\n"
    "v_exp_f32 v221, v147\n"
    "buffer_load_dwordx4 v238, s[36:39], s58 offen offset:2048 lds\n"
    "v_mfma_f32_16x16x32_bf16 v[48:51], a[160:163], v[172:175], v[48:51]\n"
    "v_cvt_pk_bf16_f32 v176, v218, v219\n"
    "v_exp_f32 v222, v148\n"
    "v_mfma_f32_16x16x32_bf16 v[52:55], a[164:167], v[172:175], v[52:55]\n"
    "v_exp_f32 v223, v149\n"
    "v_cvt_pk_bf16_f32 v177, v220, v221\n"
    "v_mfma_f32_16x16x32_bf16 v[56:59], a[168:171], v[172:175], v[56:59]\n"
    "v_exp_f32 v218, v150\n"
    "v_exp_f32 v219, v151\n"
    "buffer_load_dwordx4 v239, s[36:39], s58 offen offset:3072 lds\n"
    "s_cmp_eq_u32 s83, 0\n"
    "s_mov_b32 s75, 55296\n"
    "s_cselect_b32 s74, 133120, s75\n"
    "v_add_u32 v254, s74, v234\n"
    "v_mfma_f32_16x16x32_bf16 v[60:63], a[172:175], v[172:175], v[60:63]\n"
    "v_cvt_pk_bf16_f32 v178, v222, v223\n"
    "v_cvt_pk_bf16_f32 v179, v218, v219\n"
    "s_nop 1\n"
    "v_mfma_f32_16x16x32_bf16 v[208:211], a[240:243], v[176:179], 0\n"
    "s_waitcnt lgkmcnt(7)\n"
    "s_add_u32 m0, s85, 69632\n"
    "v_mfma_f32_16x16x32_bf16 v[128:131], a[112:115], a[0:3], v[188:191]\n"
    "buffer_load_dwordx4 v240, s[36:39], s58 offen lds\n"
    "v_mfma_f32_16x16x32_bf16 v[128:131], a[116:119], a[4:7], v[128:131]\n"
    "ds_read_b64_tr_b16 a[190:191], v249 offset:53504\n"
    "ds_read_b64_tr_b16 a[192:193], v248 offset:49664\n"
    "v_mfma_f32_16x16x32_bf16 v[128:131], a[120:123], a[8:11], v[128:131]\n"
    "ds_read_b64_tr_b16 a[194:195], v248 offset:53760\n"
    "ds_read_b64_tr_b16 a[196:197], v249 offset:49664\n"
    "v_mfma_f32_16x16x32_bf16 v[128:131], a[124:127], a[12:15], v[128:131]\n"
    "s_sub_u32 s58, s58, 20480\n"
    "s_add_u32 m0, s85, 60416\n"
    "v_mfma_f32_16x16x32_bf16 v[132:135], a[128:131], a[0:3], v[188:191]\n"
    "v_cmp_nge_f32 vcc, s70, v208\n"
    "buffer_load_dwordx4 v236, s[36:39], s58 offen lds\n"
    "s_cmp_eq_u32 s83, 0\n"
    "s_mov_b32 s75, 114688\n"
    "s_cselect_b32 s74, 36864, s75\n"
    "v_add_u32 v255, s74, v234\n"
    "v_mfma_f32_16x16x32_bf16 v[132:135], a[132:135], a[4:7], v[132:135]\n"
    "ds_read_b64_tr_b16 a[198:199], v249 offset:53760\n"
    "ds_read_b64_tr_b16 a[200:201], v248 offset:49920\n"
    "v_mfma_f32_16x16x32_bf16 v[132:135], a[136:139], a[8:11], v[132:135]\n"
    "s_cbranch_vccnz Lstub1_c_3\n"
    "Lback1_c_3:\n"
    "v_add_f32 v214, v214, v208\n"
    "v_mfma_f32_16x16x32_bf16 v[132:135], a[140:143], a[12:15], v[132:135]\n"
    "buffer_load_dwordx4 v237, s[36:39], s58 offen offset:1024 lds\n"
    "v_mfma_f32_16x16x32_bf16 v[64:67], a[144:147], v[176:179], v[64:67]\n"
    "v_exp_f32 v218, v152\n"
    "v_mfma_f32_16x16x32_bf16 v[68:71], a[148:151], v[176:179], v[68:71]\n"
    "ds_read_b64_tr_b16 a[202:203], v248 offset:54016\n"
    "ds_read_b64_tr_b16 a[204:205], v249 offset:49920\n"
    "v_mfma_f32_16x16x32_bf16 v[72:75], a[152:155], v[176:179], v[72:75]\n"
    "v_exp_f32 v219, v153\n"
    "ds_read_b64_tr_b16 a[206:207], v249 offset:54016\n"
    "buffer_load_dwordx4 v238, s[36:39], s58 offen offset:2048 lds\n"
    "global_load_dwordx2 v[230:231], v244, s[80:81] sc1\n"
    "v_mfma_f32_16x16x32_bf16 v[76:79], a[156:159], v[176:179], v[76:79]\n"
    "v_exp_f32 v220, v154\n"
    "v_exp_f32 v221, v155\n"
    "v_mfma_f32_16x16x32_bf16 v[80:83], a[160:163], v[176:179], v[80:83]\n"
    "v_cvt_pk_bf16_f32 v180, v218, v219\n"
    "v_exp_f32 v222, v156\n"
    "v_mfma_f32_16x16x32_bf16 v[84:87], a[164:167], v[176:179], v[84:87]\n"
    "v_exp_f32 v223, v157\n"
    "v_cvt_pk_bf16_f32 v181, v220, v221\n"
    "buffer_load_dwordx4 v239, s[36:39], s58 offen offset:3072 lds\n"
    "v_mfma_f32_16x16x32_bf16 v[88:91], a[168:171], v[176:179], v[88:91]\n"
    "v_exp_f32 v218, v158\n"
    "v_exp_f32 v219, v159\n"
    "v_mfma_f32_16x16x32_bf16 v[92:95], a[172:175], v[176:179], v[92:95]\n"
    "s_add_u32 m0, s85, 64512\n"
    "v_cvt_pk_bf16_f32 v182, v222, v223\n"
    "v_cvt_pk_bf16_f32 v183, v218, v219\n"
    "s_nop 1\n"
    "v_mfma_f32_16x16x32_bf16 v[208:211], a[240:243], v[180:183], 0\n"
    "buffer_load_dwordx4 v240, s[36:39], s58 offen lds\n"
    "; ---- LS31\n"
    "v_mfma_f32_16x16x32_bf16 v[136:139], a[112:115], a[16:19], v[192:195]\n"
    "s_waitcnt lgkmcnt(0)\n"
    "s_barrier\n"
    "v_mfma_f32_16x16x32_bf16 v[136:139], a[116:119], a[20:23], v[136:139]\n"
    "v_exp_f32 v218, v128\n"
    "v_mfma_f32_16x16x32_bf16 v[136:139], a[120:123], a[24:27], v[136:139]\n"
    "v_exp_f32 v219, v129\n"
    "v_mfma_f32_16x16x32_bf16 v[136:139], a[124:127], a[28:31], v[136:139]\n"
    "s_lshl_b32 s58, s84, 2\n"
    "s_add_u32 s58, s58, 3\n"
    "v_mfma_f32_16x16x32_bf16 v[140:143], a[128:131], a[16:19], v[192:195]\n"
    "v_cmp_nge_f32 vcc, s70, v208\n"
    "s_mul_i32 s58, s58, 81920\n"
    "v_mfma_f32_16x16x32_bf16 v[140:143], a[132:135], a[20:23], v[140:143]\n"
    "s_add_u32 s58, s58, s88\n"
    "s_add_u32 m0, s85, 86016\n"
    "v_mfma_f32_16x16x32_bf16 v[140:143], a[136:139], a[24:27], v[140:143]\n"
    "s_cbranch_vccnz Lstub1_d_0\n"
    "Lback1_d_0:\n"
    "v_add_f32 v215, v215, v208\n"
    "v_mfma_f32_16x16x32_bf16 v[140:143], a[140:143], a[28:31], v[140:143]\n"
    "buffer_load_dwordx4 v236, s[36:39], s58 offen lds\n"
    "v_mfma_f32_16x16x32_bf16 v[96:99], a[144:147], v[180:183], v[96:99]\n"
    "v_exp_f32 v220, v130\n"
    "v_mfma_f32_16x16x32_bf16 v[100:103], a[148:151], v[180:183], v[100:103]\n"
    "v_exp_f32 v221, v131\n"
    "v_mfma_f32_16x16x32_bf16 v[104:107], a[152:155], v[180:183], v[104:107]\n"
    "buffer_load_dwordx4 v237, s[36:39], s58 offen offset:1024 lds\n"
    "v_mfma_f32_16x16x32_bf16 v[108:111], a[156:159], v[180:183], v[108:111]\n"
    "v_cvt_pk_bf16_f32 v168, v218, v219\n"
    "v_mfma_f32_16x16x32_bf16 v[112:115], a[160:163], v[180:183], v[112:115]\n"
    "v_exp_f32 v222, v132\n"
    "v_mfma_f32_16x16x32_bf16 v[116:119], a[164:167], v[180:183], v[116:119]\n"
    "v_exp_f32 v223, v133\n"
    "v_cvt_pk_bf16_f32 v169, v220, v221\n"
    "buffer_load_dwordx4 v238, s[36:39], s58 offen offset:2048 lds\n"
    "v_mfma_f32_16x16x32_bf16 v[120:123], a[168:171], v[180:183], v[120:123]\n"
    "v_exp_f32 v218, v134\n"
    "v_exp_f32 v219, v135\n"
    "v_mfma_f32_16x16x32_bf16 v[124:127], a[172:175], v[180:183], v[124:127]\n"
    "v_cvt_pk_bf16_f32 v170, v222, v223\n"
    "v_cvt_pk_bf16_f32 v171, v218, v219\n"
    "s_nop 1\n"
    "v_mfma_f32_16x16x32_bf16 v[208:211], a[240:243], v[168:171], 0\n"
    "buffer_load_dwordx4 v239, s[36:39], s58 offen offset:3072 lds\n"
    "v_mfma_f32_16x16x32_bf16 v[144:147], a[112:115], a[32:35], v[196:199]\n"
    "s_add_u32 m0, s85, 90112\n"
    "v_mfma_f32_16x16x32_bf16 v[144:147], a[116:119], a[36:39], v[144:147]\n"
    "buffer_load_dwordx4 v240, s[36:39], s58 offen lds\n"
    "v_mfma_f32_16x16x32_bf16 v[144:147], a[120:123], a[40:43], v[144:147]\n"
    "s_sub_u32 s58, s58, 20480\n"
    "s_add_u32 m0, s85, 80896\n"
    "v_mfma_f32_16x16x32_bf16 v[144:147], a[124:127], a[44:47], v[144:147]\n"
    "v_exp_f32 v218, v136\n"
    "v_mfma_f32_16x16x32_bf16 v[148:151], a[128:131], a[32:35], v[196:199]\n"
    "v_cmp_nge_f32 vcc, s70, v208\n"
    "v_mfma_f32_16x16x32_bf16 v[148:151], a[132:135], a[36:39], v[148:151]\n"
    "buffer_load_dwordx4 v236, s[36:39], s58 offen lds\n"
    "v_mfma_f32_16x16x32_bf16 v[148:151], a[136:139], a[40:43], v[148:151]\n"
    "s_cbranch_vccnz Lstub1_d_1\n"
    "Lback1_d_1:\n"
    "v_add_f32 v212, v212, v208\n"
    "v_mfma_f32_16x16x32_bf16 v[148:151], a[140:143], a[44:47], v[148:151]\n"
    "v_exp_f32 v219, v137\n"
    "v_mfma_f32_16x16x32_bf16 v[0:3], a[176:179], v[168:171], v[0:3]\n"
    "buffer_load_dwordx4 v237, s[36:39], s58 offen offset:1024 lds\n"
    "v_mfma_f32_16x16x32_bf16 v[4:7], a[180:183], v[168:171], v[4:7]\n"
    "v_exp_f32 v220, v138\n"
    "v_mfma_f32_16x16x32_bf16 v[8:11], a[184:187], v[168:171], v[8:11]\n"
    "v_exp_f32 v221, v139\n"
    "v_mfma_f32_16x16x32_bf16 v[12:15], a[188:191], v[168:171], v[12:15]\n"
    "buffer_load_dwordx4 v238, s[36:39], s58 offen offset:2048 lds\n"
    "v_mfma_f32_16x16x32_bf16 v[16:19], a[192:195], v[168:171], v[16:19]\n"
    "v_cvt_pk_bf16_f32 v172, v218, v219\n"
    "v_exp_f32 v222, v140\n"
    "v_mfma_f32_16x16x32_bf16 v[20:23], a[196:199], v[168:171], v[20:23]\n"
    "v_exp_f32 v223, v141\n"
    "v_cvt_pk_bf16_f32 v173, v220, v221\n"
    "v_mfma_f32_16x16x32_bf16 v[24:27], a[200:203], v[168:171], v[24:27]\n"
    "v_exp_f32 v218, v142\n"
    "v_exp_f32 v219, v143\n"
    "buffer_load_dwordx4 v239, s[36:39], s58 offen offset:3072 lds\n"
    "v_mfma_f32_16x16x32_bf16 v[28:31], a[204:207], v[168:171], v[28:31]\n"
    "v_cvt_pk_bf16_f32 v174, v222, v223\n"
    "v_cvt_pk_bf16_f32 v175, v218, v219\n"
    "s_nop 1\n"
    "v_mfma_f32_16x16x32_bf16 v[208:211], a[240:243], v[172:175], 0\n"
    "s_add_u32 m0, s85, 84992\n"
    "s_nop 0\n"
    "buffer_load_dwordx4 v240, s[36:39], s58 offen lds\n"
    "v_mfma_f32_16x16x32_bf16 v[152:155], a[112:115], a[48:51], v[200:203]\n"
    "v_exp_f32 v218, v144\n"
    "v_mfma_f32_16x16x32_bf16 v[152:155], a[116:119], a[52:55], v[152:155]\n"
    "v_rcp_f32 v188, v212\n"
    "s_nop 0\n"
    "v_pk_mul_f32 v[0:1], v[0:1], v[188:189] op_sel_hi:[1,0]\n"
    "v_pk_mul_f32 v[2:3], v[2:3], v[188:189] op_sel_hi:[1,0]\n"
    "v_mfma_f32_16x16x32_bf16 v[152:155], a[120:123], a[56:59], v[152:155]\n"
    "v_pk_mul_f32 v[4:5], v[4:5], v[188:189] op_sel_hi:[1,0]\n"
    "v_pk_mul_f32 v[6:7], v[6:7], v[188:189] op_sel_hi:[1,0]\n"
    "v_cvt_pk_bf16_f32 v0, v0, v1\n"
    "v_mfma_f32_16x16x32_bf16 v[152:155], a[124:127], a[60:63], v[152:155]\n"
    "v_cvt_pk_bf16_f32 v1, v2, v3\n"
    "v_cvt_pk_bf16_f32 v2, v4, v5\n"
    "v_mfma_f32_16x16x32_bf16 v[156:159], a[128:131], a[48:51], v[200:203]\n"
    "v_cmp_nge_f32 vcc, s70, v208\n"
    "v_cvt_pk_bf16_f32 v3, v6, v7\n"
    "buffer_store_dwordx4 v[0:3], v234, s[52:55], s65 offen sc1\n"
    "ds_write_b128 v254, v[0:3]\n"
    "v_mfma_f32_16x16x32_bf16 v[156:159], a[132:135], a[52:55], v[156:159]\n"
    "v_pk_mul_f32 v[8:9], v[8:9], v[188:189] op_sel_hi:[1,0]\n"
    "v_pk_mul_f32 v[10:11], v[10:11], v[188:189] op_sel_hi:[1,0]\n"
    "v_pk_mul_f32 v[12:13], v[12:13], v[188:189] op_sel_hi:[1,0]\n"
    "v_mfma_f32_16x16x32_bf16 v[156:159], a[136:139], a[56:59], v[156:159]\n"
    "s_cbranch_vccnz Lstub1_d_2\n"
    "Lback1_d_2:\n"
    "v_add_f32 v213, v213, v208\n"
    "v_pk_mul_f32 v[14:15], v[14:15], v[188:189] op_sel_hi:[1,0]\n"
    "v_cvt_pk_bf16_f32 v8, v8, v9\n"
    "v_mfma_f32_16x16x32_bf16 v[156:159], a[140:143], a[60:63], v[156:159]\n"
    "v_cvt_pk_bf16_f32 v9, v10, v11\n"
    "v_cvt_pk_bf16_f32 v10, v12, v13\n"
    "v_cvt_pk_bf16_f32 v11, v14, v15\n"
    "v_mfma_f32_16x16x32_bf16 v[32:35], a[176:179], v[172:175], v[32:35]\n"
    "buffer_store_dwordx4 v[8:11], v234, s[52:55], s65 offen offset:1024 sc1\n"
    "ds_write_b128 v254, v[8:11] offset:1024\n"
    "v_mfma_f32_16x16x32_bf16 v[36:39], a[180:183], v[172:175], v[36:39]\n"
    "v_pk_mul_f32 v[16:17], v[16:17], v[188:189] op_sel_hi:[1,0]\n"
    "v_pk_mul_f32 v[18:19], v[18:19], v[188:189] op_sel_hi:[1,0]\n"
    "v_pk_mul_f32 v[20:21], v[20:21], v[188:189] op_sel_hi:[1,0]\n"
    "v_mfma_f32_16x16x32_bf16 v[40:43], a[184:187], v[172:175], v[40:43]\n"
    "v_exp_f32 v219, v145\n"
    "v_pk_mul_f32 v[22:23], v[22:23], v[188:189] op_sel_hi:[1,0]\n"
    "v_cvt_pk_bf16_f32 v16, v16, v17\n"
    "v_cvt_pk_bf16_f32 v17, v18, v19\n"
    "v_mfma_f32_16x16x32_bf16 v[44:47], a[188:191], v[172:175], v[44:47]\n"
    "v_exp_f32 v220, v146\n"
    "v_exp_f32 v221, v147\n"
    "v_cvt_pk_bf16_f32 v18, v20, v21\n"
    "v_cvt_pk_bf16_f32 v19, v22, v23\n"
    "v_mfma_f32_16x16x32_bf16 v[48:51], a[192:195], v[172:175], v[48:51]\n"
    "v_cvt_pk_bf16_f32 v176, v218, v219\n"
    "v_exp_f32 v222, v148\n"
    "buffer_store_dwordx4 v[16:19], v234, s[52:55], s65 offen offset:2048 sc1\n"
    "ds_write_b128 v255, v[16:19]\n"
    "v_pk_mul_f32 v[24:25], v[24:25], v[188:189] op_sel_hi:[1,0]\n"
    "v_mfma_f32_16x16x32_bf16 v[52:55], a[196:199], v[172:175], v[52:55]\n"
    "v_exp_f32 v223, v149\n"
    "v_cvt_pk_bf16_f32 v177, v220, v221\n"
    "v_pk_mul_f32 v[26:27], v[26:27], v[188:189] op_sel_hi:[1,0]\n"
    "v_pk_mul_f32 v[28:29], v[28:29], v[188:189] op_sel_hi:[1,0]\n"
    "v_pk_mul_f32 v[30:31], v[30:31], v[188:189] op_sel_hi:[1,0]\n"
    "v_mfma_f32_16x16x32_bf16 v[56:59], a[200:203], v[172:175], v[56:59]\n"
    "v_exp_f32 v218, v150\n"
    "v_exp_f32 v219, v151\n"
    "v_cvt_pk_bf16_f32 v24, v24, v25\n"
    "v_cvt_pk_bf16_f32 v25, v26, v27\n"
    "v_mfma_f32_16x16x32_bf16 v[60:63], a[204:207], v[172:175], v[60:63]\n"
    "v_cvt_pk_bf16_f32 v178, v222, v223\n"
    "v_cvt_pk_bf16_f32 v179, v218, v219\n"
    "v_cvt_pk_bf16_f32 v26, v28, v29\n"
    "v_cvt_pk_bf16_f32 v27, v30, v31\n"
    "buffer_store_dwordx4 v[24:27], v234, s[52:55], s65 offen offset:3072 sc1\n"
    "v_mfma_f32_16x16x32_bf16 v[208:211], a[240:243], v[176:179], 0\n"
    "ds_write_b128 v255, v[24:27] offset:1024\n"
    "s_add_u32 s65, s65, 4096\n"
    "v_exp_f32 v218, v152\n"
    "v_rcp_f32 v192, v213\n"
    "s_nop 0\n"
    "v_pk_mul_f32 v[32:33], v[32:33], v[192:193] op_sel_hi:[1,0]\n"
    "v_pk_mul_f32 v[34:35], v[34:35], v[192:193] op_sel_hi:[1,0]\n"
    "v_pk_mul_f32 v[36:37], v[36:37], v[192:193] op_sel_hi:[1,0]\n"
    "v_pk_mul_f32 v[38:39], v[38:39], v[192:193] op_sel_hi:[1,0]\n"
    "v_cvt_pk_bf16_f32 v32, v32, v33\n"
    "v_cvt_pk_bf16_f32 v33, v34, v35\n"
    "v_cvt_pk_bf16_f32 v34, v36, v37\n"
    "v_cmp_nge_f32 vcc, s70, v208\n"
    "v_cvt_pk_bf16_f32 v35, v38, v39\n"
    "buffer_store_dwordx4 v[32:35], v234, s[52:55], s65 offen sc1\n"
    "ds_write_b128 v254, v[32:35] offset:2048\n"
    "v_pk_mul_f32 v[40:41], v[40:41], v[192:193] op_sel_hi:[1,0]\n"
    "v_pk_mul_f32 v[42:43], v[42:43], v[192:193] op_sel_hi:[1,0]\n"
    "v_pk_mul_f32 v[44:45], v[44:45], v[192:193] op_sel_hi:[1,0]\n"
    "s_cbranch_vccnz Lstub1_d_3\n"
    "Lback1_d_3:\n"
    "v_add_f32 v214, v214, v208\n"
    "v_pk_mul_f32 v[46:47], v[46:47], v[192:193] op_sel_hi:[1,0]\n"
    "v_cvt_pk_bf16_f32 v40, v40, v41\n"
    "v_cvt_pk_bf16_f32 v41, v42, v43\n"
    "v_cvt_pk_bf16_f32 v42, v44, v45\n"
    "v_cvt_pk_bf16_f32 v43, v46, v47\n"
    "v_mfma_f32_16x16x32_bf16 v[64:67], a[176:179], v[176:179], v[64:67]\n"
    "buffer_store_dwordx4 v[40:43], v234, s[52:55], s65 offen offset:1024 sc1\n"
    "ds_write_b128 v254, v[40:43] offset:3072\n"
    "v_mfma_f32_16x16x32_bf16 v[68:71], a[180:183], v[176:179], v[68:71]\n"
    "v_pk_mul_f32 v[48:49], v[48:49], v[192:193] op_sel_hi:[1,0]\n"
    "v_pk_mul_f32 v[50:51], v[50:51], v[192:193] op_sel_hi:[1,0]\n"
    "v_pk_mul_f32 v[52:53], v[52:53], v[192:193] op_sel_hi:[1,0]\n"
    "v_mfma_f32_16x16x32_bf16 v[72:75], a[184:187], v[176:179], v[72:75]\n"
    "v_exp_f32 v219, v153\n"
    "v_pk_mul_f32 v[54:55], v[54:55], v[192:193] op_sel_hi:[1,0]\n"
    "v_cvt_pk_bf16_f32 v48, v48, v49\n"
    "v_cvt_pk_bf16_f32 v49, v50, v51\n"
    "v_mfma_f32_16x16x32_bf16 v[76:79], a[188:191], v[176:179], v[76:79]\n"
    "v_exp_f32 v220, v154\n"
    "v_exp_f32 v221, v155\n"
    "v_cvt_pk_bf16_f32 v50, v52, v53\n"
    "v_cvt_pk_bf16_f32 v51, v54, v55\n"
    "v_mfma_f32_16x16x32_bf16 v[80:83], a[192:195], v[176:179], v[80:83]\n"
    "v_cvt_pk_bf16_f32 v180, v218, v219\n"
    "v_exp_f32 v222, v156\n"
    "buffer_store_dwordx4 v[48:51], v234, s[52:55], s65 offen offset:2048 sc1\n"
    "ds_write_b128 v255, v[48:51] offset:2048\n"
    "v_pk_mul_f32 v[56:57], v[56:57], v[192:193] op_sel_hi:[1,0]\n"
    "v_mfma_f32_16x16x32_bf16 v[84:87], a[196:199], v[176:179], v[84:87]\n"
    "v_exp_f32 v223, v157\n"
    "v_cvt_pk_bf16_f32 v181, v220, v221\n"
    "v_pk_mul_f32 v[58:59], v[58:59], v[192:193] op_sel_hi:[1,0]\n"
    "v_pk_mul_f32 v[60:61], v[60:61], v[192:193] op_sel_hi:[1,0]\n"
    "v_pk_mul_f32 v[62:63], v[62:63], v[192:193] op_sel_hi:[1,0]\n"
    "v_mfma_f32_16x16x32_bf16 v[88:91], a[200:203], v[176:179], v[88:91]\n"
    "v_exp_f32 v218, v158\n"
    "v_exp_f32 v219, v159\n"
    "v_cvt_pk_bf16_f32 v56, v56, v57\n"
    "v_cvt_pk_bf16_f32 v57, v58, v59\n"
    "v_mfma_f32_16x16x32_bf16 v[92:95], a[204:207], v[176:179], v[92:95]\n"
    "v_cvt_pk_bf16_f32 v182, v222, v223\n"
    "v_cvt_pk_bf16_f32 v183, v218, v219\n"
    "v_cvt_pk_bf16_f32 v58, v60, v61\n"
    "v_cvt_pk_bf16_f32 v59, v62, v63\n"
    "buffer_store_dwordx4 v[56:59], v234, s[52:55], s65 offen offset:3072 sc1\n"
    "v_mfma_f32_16x16x32_bf16 v[208:211], a[240:243], v[180:183], 0\n"
    "ds_write_b128 v255, v[56:59] offset:3072\n"
    "s_add_u32 s65, s65, 4096\n"
    "; ---- LEND\n"
    "v_rcp_f32 v196, v214\n"
    "s_nop 0\n"
    "v_pk_mul_f32 v[64:65], v[64:65], v[196:197] op_sel_hi:[1,0]\n"
    "v_pk_mul_f32 v[66:67], v[66:67], v[196:197] op_sel_hi:[1,0]\n"
    "v_pk_mul_f32 v[68:69], v[68:69], v[196:197] op_sel_hi:[1,0]\n"
    "v_pk_mul_f32 v[70:71], v[70:71], v[196:197] op_sel_hi:[1,0]\n"
    "v_cvt_pk_bf16_f32 v64, v64, v65\n"
    "v_cmp_nge_f32 vcc, s70, v208\n"
    "s_cbranch_vccnz Lstub1_e\n"
    "Lback1_e:\n"
    "v_add_f32 v215, v215, v208\n"
    "v_mfma_f32_16x16x32_bf16 v[96:99], a[176:179], v[180:183], v[96:99]\n"
    "v_cvt_pk_bf16_f32 v65, v66, v67\n"
    "v_cvt_pk_bf16_f32 v66, v68, v69\n"
    "v_cvt_pk_bf16_f32 v67, v70, v71\n"
    "buffer_store_dwordx4 v[64:67], v234, s[52:55], s65 offen sc1\n"
    "ds_write_b128 v254, v[64:67] offset:4096\n"
    "v_mfma_f32_16x16x32_bf16 v[100:103], a[180:183], v[180:183], v[100:103]\n"
    "v_pk_mul_f32 v[72:73], v[72:73], v[196:197] op_sel_hi:[1,0]\n"
    "v_pk_mul_f32 v[74:75], v[74:75], v[196:197] op_sel_hi:[1,0]\n"
    "v_pk_mul_f32 v[76:77], v[76:77], v[196:197] op_sel_hi:[1,0]\n"
    "v_pk_mul_f32 v[78:79], v[78:79], v[196:197] op_sel_hi:[1,0]\n"
    "v_mfma_f32_16x16x32_bf16 v[104:107], a[184:187], v[180:183], v[104:107]\n"
    "v_cvt_pk_bf16_f32 v72, v72, v73\n"
    "v_cvt_pk_bf16_f32 v73, v74, v75\n"
    "v_cvt_pk_bf16_f32 v74, v76, v77\n"
    "v_cvt_pk_bf16_f32 v75, v78, v79\n"
    "buffer_store_dwordx4 v[72:75], v234, s[52:55], s65 offen offset:1024 sc1\n"
    "v_mfma_f32_16x16x32_bf16 v[108:111], a[188:191], v[180:183], v[108:111]\n"
    "ds_write_b128 v254, v[72:75] offset:5120\n"
    "v_pk_mul_f32 v[80:81], v[80:81], v[196:197] op_sel_hi:[1,0]\n"
    "v_pk_mul_f32 v[82:83], v[82:83], v[196:197] op_sel_hi:[1,0]\n"
    "v_pk_mul_f32 v[84:85], v[84:85], v[196:197] op_sel_hi:[1,0]\n"
    "v_mfma_f32_16x16x32_bf16 v[112:115], a[192:195], v[180:183], v[112:115]\n"
    "v_pk_mul_f32 v[86:87], v[86:87], v[196:197] op_sel_hi:[1,0]\n"
    "v_cvt_pk_bf16_f32 v80, v80, v81\n"
    "v_cvt_pk_bf16_f32 v81, v82, v83\n"
    "v_cvt_pk_bf16_f32 v82, v84, v85\n"
    "v_cvt_pk_bf16_f32 v83, v86, v87\n"
    "v_mfma_f32_16x16x32_bf16 v[116:119], a[196:199], v[180:183], v[116:119]\n"
    "buffer_store_dwordx4 v[80:83], v234, s[52:55], s65 offen offset:2048 sc1\n"
    "ds_write_b128 v255, v[80:83] offset:4096\n"
    "v_pk_mul_f32 v[88:89], v[88:89], v[196:197] op_sel_hi:[1,0]\n"
    "v_pk_mul_f32 v[90:91], v[90:91], v[196:197] op_sel_hi:[1,0]\n"
    "v_mfma_f32_16x16x32_bf16 v[120:123], a[200:203], v[180:183], v[120:123]\n"
    "v_pk_mul_f32 v[92:93], v[92:93], v[196:197] op_sel_hi:[1,0]\n"
    "v_pk_mul_f32 v[94:95], v[94:95], v[196:197] op_sel_hi:[1,0]\n"
    "v_cvt_pk_bf16_f32 v88, v88, v89\n"
    "v_cvt_pk_bf16_f32 v89, v90, v91\n"
    "v_cvt_pk_bf16_f32 v90, v92, v93\n"
    "v_mfma_f32_16x16x32_bf16 v[124:127], a[204:207], v[180:183], v[124:127]\n"
    "v_cvt_pk_bf16_f32 v91, v94, v95\n"
    "buffer_store_dwordx4 v[88:91], v234, s[52:55], s65 offen offset:3072 sc1\n"
    "ds_write_b128 v255, v[88:91] offset:5120\n"
    "s_add_u32 s65, s65, 4096\n"
    "v_rcp_f32 v200, v215\n"
    "s_nop 0\n"
    "v_pk_mul_f32 v[96:97], v[96:97], v[200:201] op_sel_hi:[1,0]\n"
    "v_pk_mul_f32 v[98:99], v[98:99], v[200:201] op_sel_hi:[1,0]\n"
    "v_pk_mul_f32 v[100:101], v[100:101], v[200:201] op_sel_hi:[1,0]\n"
    "v_pk_mul_f32 v[102:103], v[102:103], v[200:201] op_sel_hi:[1,0]\n"
    "v_cvt_pk_bf16_f32 v96, v96, v97\n"
    "v_cvt_pk_bf16_f32 v97, v98, v99\n"
    "v_cvt_pk_bf16_f32 v98, v100, v101\n"
    "v_cvt_pk_bf16_f32 v99, v102, v103\n"
    "buffer_store_dwordx4 v[96:99], v234, s[52:55], s65 offen sc1\n"
    "ds_write_b128 v254, v[96:99] offset:6144\n"
    "v_pk_mul_f32 v[104:105], v[104:105], v[200:201] op_sel_hi:[1,0]\n"
    "v_pk_mul_f32 v[106:107], v[106:107], v[200:201] op_sel_hi:[1,0]\n"
    "v_pk_mul_f32 v[108:109], v[108:109], v[200:201] op_sel_hi:[1,0]\n"
    "v_pk_mul_f32 v[110:111], v[110:111], v[200:201] op_sel_hi:[1,0]\n"
    "v_cvt_pk_bf16_f32 v104, v104, v105\n"
    "v_cvt_pk_bf16_f32 v105, v106, v107\n"
    "v_cvt_pk_bf16_f32 v106, v108, v109\n"
    "v_cvt_pk_bf16_f32 v107, v110, v111\n"
    "buffer_store_dwordx4 v[104:107], v234, s[52:55], s65 offen offset:1024 sc1\n"
    "ds_write_b128 v254, v[104:107] offset:7168\n"
    "v_pk_mul_f32 v[112:113], v[112:113], v[200:201] op_sel_hi:[1,0]\n"
    "v_pk_mul_f32 v[114:115], v[114:115], v[200:201] op_sel_hi:[1,0]\n"
    "v_pk_mul_f32 v[116:117], v[116:117], v[200:201] op_sel_hi:[1,0]\n"
    "v_pk_mul_f32 v[118:119], v[118:119], v[200:201] op_sel_hi:[1,0]\n"
    "v_cvt_pk_bf16_f32 v112, v112, v113\n"
    "v_cvt_pk_bf16_f32 v113, v114, v115\n"
    "v_cvt_pk_bf16_f32 v114, v116, v117\n"
    "v_cvt_pk_bf16_f32 v115, v118, v119\n"
    "buffer_store_dwordx4 v[112:115], v234, s[52:55], s65 offen offset:2048 sc1\n"
    "ds_write_b128 v255, v[112:115] offset:6144\n"
    "v_pk_mul_f32 v[120:121], v[120:121], v[200:201] op_sel_hi:[1,0]\n"
    "v_pk_mul_f32 v[122:123], v[122:123], v[200:201] op_sel_hi:[1,0]\n"
    "v_pk_mul_f32 v[124:125], v[124:125], v[200:201] op_sel_hi:[1,0]\n"
    "v_pk_mul_f32 v[126:127], v[126:127], v[200:201] op_sel_hi:[1,0]\n"
    "v_cvt_pk_bf16_f32 v120, v120, v121\n"
    "v_cvt_pk_bf16_f32 v121, v122, v123\n"
    "v_cvt_pk_bf16_f32 v122, v124, v125\n"
    "v_cvt_pk_bf16_f32 v123, v126, v127\n"
    "buffer_store_dwordx4 v[120:123], v234, s[52:55], s65 offen offset:3072 sc1\n"
    "ds_write_b128 v255, v[120:123] offset:7168\n"
    "s_add_u32 s65, s65, 4096\n"
    "s_waitcnt vmcnt(37)\n"
    "s_waitcnt vmcnt(28)\n"
    "; ---- GPRE\n"
    "Lgemm:\n"
    "v_accvgpr_read_b32 v96, a244\n"
    "v_accvgpr_read_b32 v97, a245\n"
    "v_accvgpr_read_b32 v98, a246\n"
    "v_accvgpr_read_b32 v99, a247\n"
    "v_accvgpr_read_b32 v100, a248\n"
    "v_accvgpr_read_b32 v111, a249\n"
    "v_accvgpr_read_b32 v112, a250\n"
    "v_accvgpr_read_b32 v113, a251\n"
    "v_accvgpr_read_b32 v114, a252\n"
    "v_accvgpr_read_b32 v115, a253\n"
    "v_add_u32 v101, 65536, v96\n"
    "v_add_u32 v106, 131072, v96\n"
    "v_add_u32 v116, 65536, v111\n"
    "v_add_u32 v102, 65536, v97\n"
    "v_add_u32 v107, 131072, v97\n"
    "v_add_u32 v117, 65536, v112\n"
    "v_add_u32 v103, 65536, v98\n"
    "v_add_u32 v108, 131072, v98\n"
    "v_add_u32 v118, 65536, v113\n"
    "v_add_u32 v104, 65536, v99\n"
    "v_add_u32 v109, 131072, v99\n"
    "v_add_u32 v119, 65536, v114\n"
    "v_add_u32 v105, 65536, v100\n"
    "v_add_u32 v110, 131072, v100\n"
    "v_add_u32 v120, 65536, v115\n"
    "s_lshl_b32 s92, s84, 2\n"
    "s_add_u32 s92, s92, 4\n"
    "s_lshr_b32 s62, s31, 2\n"
    "s_mul_i32 s62, s62, 3\n"
    "s_lshr_b32 s62, s62, 1\n"
    "s_mov_b32 s91, s62\n"
    "s_add_u32 s58, s91, s92\n"
    "s_and_b32 s58, s58, 15\n"
    "s_mul_i32 s58, s58, 81920\n"
    "s_add_u32 s58, s58, s88\n"
    "s_add_u32 s90, s62, s92\n"
    "s_bfe_u32 s75, s90, 0x30001\n"
    "s_mul_i32 s89, s75, 36864\n"
    "s_and_b32 s75, s90, 1\n"
    "s_lshl_b32 s75, s75, 11\n"
    "s_add_u32 s89, s89, s75\n"
    "s_mov_b64 s[40:41], s[26:27]\n"
    "s_and_b32 s41, s41, 0xffff\n"
    "s_mov_b32 s42, 147456\n"
    "s_mov_b32 s43, 0x20000\n"
    "s_mov_b64 s[44:45], s[22:23]\n"
    "s_and_b32 s45, s45, 0xffff\n"
    "s_mov_b32 s46, 366400\n"
    "s_mov_b32 s47, 0x20000\n"
    "v_mbcnt_lo_u32_b32 v124, -1, 0\n"
    "v_mbcnt_hi_u32_b32 v124, -1, v124\n"
    "v_lshlrev_b32 v125, 4, v124\n"
    "v_lshlrev_b32 v130, 3, v124\n"
    "s_lshl_b32 s74, s82, 2\n"
    "s_add_u32 s74, s74, s56\n"
    "s_mul_i32 s60, s74, 9216\n"
    "s_add_u32 s64, s60, 2560\n"
    "s_add_u32 s65, s60, 5120\n"
    "s_add_u32 s59, s60, 7680\n"
    "s_waitcnt lgkmcnt(0)\n"
    "s_barrier\n"
    "ds_read_b128 a[40:43], v116 offset:59392\n"
    "ds_read_b128 a[44:47], v116 offset:60416\n"
    "ds_read_b128 a[48:51], v117 offset:59392\n"
    "ds_read_b128 a[52:55], v117 offset:60416\n"
    "ds_read_b128 a[56:59], v118 offset:59392\n"
    "ds_read_b128 a[60:63], v118 offset:60416\n"
    "ds_read_b128 a[64:67], v119 offset:59392\n"
    "ds_read_b128 a[68:71], v119 offset:60416\n"
    "ds_read_b128 a[72:75], v120 offset:59392\n"
    "ds_read_b128 a[76:79], v120 offset:60416\n"
    "ds_read_b64_tr_b16 a[0:1], v106 offset:12288\n"
    "ds_read_b64_tr_b16 a[2:3], v106 offset:17408\n"
    "ds_read_b64_tr_b16 a[4:5], v106 offset:22528\n"
    "ds_read_b64_tr_b16 a[6:7], v106 offset:27648\n"
    "; ---- C0\n"
    "s_waitcnt lgkmcnt(0)\n"
    "v_mfma_f32_16x16x32_bf16 v[0:3], a[0:3], a[40:43], 0\n"
    "ds_read_b64_tr_b16 a[8:9], v107 offset:12288\n"
    "v_mfma_f32_16x16x32_bf16 v[0:3], a[4:7], a[44:47], v[0:3]\n"
    "ds_read_b64_tr_b16 a[10:11], v107 offset:17408\n"
    "ds_read_b64_tr_b16 a[12:13], v107 offset:22528\n"
    "v_mfma_f32_16x16x32_bf16 v[4:7], a[0:3], a[48:51], 0\n"
    "ds_read_b64_tr_b16 a[14:15], v107 offset:27648\n"
    "ds_read_b64_tr_b16 a[16:17], v108 offset:12288\n"
    "v_mfma_f32_16x16x32_bf16 v[4:7], a[4:7], a[52:55], v[4:7]\n"
    "ds_read_b64_tr_b16 a[18:19], v108 offset:17408\n"
    "ds_read_b64_tr_b16 a[20:21], v108 offset:22528\n"
    "v_mfma_f32_16x16x32_bf16 v[8:11], a[0:3], a[56:59], 0\n"
    "ds_read_b64_tr_b16 a[22:23], v108 offset:27648\n"
    "ds_read_b64_tr_b16 a[24:25], v109 offset:12288\n"
    "v_mfma_f32_16x16x32_bf16 v[8:11], a[4:7], a[60:63], v[8:11]\n"
    "ds_read_b64_tr_b16 a[26:27], v109 offset:17408\n"
    "ds_read_b64_tr_b16 a[28:29], v109 offset:22528\n"
    "v_mfma_f32_16x16x32_bf16 v[12:15], a[0:3], a[64:67], 0\n"
    "ds_read_b64_tr_b16 a[30:31], v109 offset:27648\n"
    "ds_read_b64_tr_b16 a[32:33], v110 offset:12288\n"
    "v_mfma_f32_16x16x32_bf16 v[12:15], a[4:7], a[68:71], v[12:15]\n"
    "ds_read_b64_tr_b16 a[34:35], v110 offset:17408\n"
    "ds_read_b64_tr_b16 a[36:37], v110 offset:22528\n"
    "v_mfma_f32_16x16x32_bf16 v[80:83], a[0:3], a[72:75], 0\n"
    "ds_read_b64_tr_b16 a[38:39], v110 offset:27648\n"
    "ds_read_b64_tr_b16 a[120:121], v96 offset:8192\n"
    "v_mfma_f32_16x16x32_bf16 v[80:83], a[4:7], a[76:79], v[80:83]\n"
    "s_waitcnt lgkmcnt(13)\n"
    "ds_read_b64_tr_b16 a[122:123], v96 offset:13312\n"
    "v_mfma_f32_16x16x32_bf16 v[16:19], a[8:11], a[40:43], 0\n"
    "ds_read_b64_tr_b16 a[124:125], v96 offset:18432\n"
    "ds_read_b64_tr_b16 a[126:127], v96 offset:23552\n"
    "v_mfma_f32_16x16x32_bf16 v[16:19], a[12:15], a[44:47], v[16:19]\n"
    "ds_read_b64_tr_b16 a[128:129], v97 offset:8192\n"
    "ds_read_b64_tr_b16 a[130:131], v97 offset:13312\n"
    "v_mfma_f32_16x16x32_bf16 v[20:23], a[8:11], a[48:51], 0\n"
    "ds_read_b64_tr_b16 a[132:133], v97 offset:18432\n"
    "ds_read_b64_tr_b16 a[134:135], v97 offset:23552\n"
    "v_mfma_f32_16x16x32_bf16 v[20:23], a[12:15], a[52:55], v[20:23]\n"
    "ds_read_b64_tr_b16 a[136:137], v98 offset:8192\n"
    "v_mfma_f32_16x16x32_bf16 v[24:27], a[8:11], a[56:59], 0\n"
    "ds_read_b64_tr_b16 a[138:139], v98 offset:13312\n"
    "v_mfma_f32_16x16x32_bf16 v[24:27], a[12:15], a[60:63], v[24:27]\n"
    "ds_read_b64_tr_b16 a[140:141], v98 offset:18432\n"
    "ds_read_b64_tr_b16 a[142:143], v98 offset:23552\n"
    "v_mfma_f32_16x16x32_bf16 v[28:31], a[8:11], a[64:67], 0\n"
    "ds_read_b64_tr_b16 a[144:145], v99 offset:8192\n"
    "v_mfma_f32_16x16x32_bf16 v[28:31], a[12:15], a[68:71], v[28:31]\n"
    "ds_read_b64_tr_b16 a[146:147], v99 offset:13312\n"
    "ds_read_b64_tr_b16 a[148:149], v99 offset:18432\n"
    "v_mfma_f32_16x16x32_bf16 v[84:87], a[8:11], a[72:75], 0\n"
    "ds_read_b64_tr_b16 a[150:151], v99 offset:23552\n"
    "v_mfma_f32_16x16x32_bf16 v[84:87], a[12:15], a[76:79], v[84:87]\n"
    "s_waitcnt lgkmcnt(15)\n"
    "ds_read_b64_tr_b16 a[152:153], v100 offset:8192\n"
    "v_mfma_f32_16x16x32_bf16 v[32:35], a[16:19], a[40:43], 0\n"
    "ds_read_b64_tr_b16 a[154:155], v100 offset:13312\n"
    "ds_read_b64_tr_b16 a[156:157], v100 offset:18432\n"
    "v_mfma_f32_16x16x32_bf16 v[32:35], a[20:23], a[44:47], v[32:35]\n"
    "ds_read_b64_tr_b16 a[158:159], v100 offset:23552\n"
    "v_mfma_f32_16x16x32_bf16 v[36:39], a[16:19], a[48:51], 0\n"
    "v_mfma_f32_16x16x32_bf16 v[36:39], a[20:23], a[52:55], v[36:39]\n"
    "v_mfma_f32_16x16x32_bf16 v[40:43], a[16:19], a[56:59], 0\n"
    "v_mfma_f32_16x16x32_bf16 v[40:43], a[20:23], a[60:63], v[40:43]\n"
    "v_mfma_f32_16x16x32_bf16 v[44:47], a[16:19], a[64:67], 0\n"
    "v_mfma_f32_16x16x32_bf16 v[44:47], a[20:23], a[68:71], v[44:47]\n"
    "v_mfma_f32_16x16x32_bf16 v[88:91], a[16:19], a[72:75], 0\n"
    "s_waitcnt lgkmcnt(0)\n"
    "s_waitcnt vmcnt(0)\n"
    "s_barrier\n"
    "s_cmp_lg_u32 s56, 0\n"
    "s_cbranch_scc1 LhAs\n"
    "s_mov_b64 exec, 56\n"
    "global_store_dword v243, v230, s[80:81] offset:4 sc1\n"
    "s_mov_b64 exec, -1\n"
    "LhAs:\n"
    "v_mfma_f32_16x16x32_bf16 v[88:91], a[20:23], a[76:79], v[88:91]\n"
    "ds_read_b128 a[80:83], v111 offset:28672\n"
    "v_mfma_f32_16x16x32_bf16 v[48:51], a[24:27], a[40:43], 0\n"
    "s_add_u32 m0, s85, 143360\n"
    "ds_read_b128 a[84:87], v111 offset:29696\n"
    "v_mfma_f32_16x16x32_bf16 v[48:51], a[28:31], a[44:47], v[48:51]\n"
    "buffer_load_dwordx4 v236, s[36:39], s58 offen lds\n"
    "v_mfma_f32_16x16x32_bf16 v[52:55], a[24:27], a[48:51], 0\n"
    "ds_read_b128 a[88:91], v112 offset:28672\n"
    "ds_read_b128 a[92:95], v112 offset:29696\n"
    "v_mfma_f32_16x16x32_bf16 v[52:55], a[28:31], a[52:55], v[52:55]\n"
    "ds_read_b128 a[96:99], v113 offset:28672\n"
    "ds_read_b64_tr_b16 a[160:161], v101\n"
    "v_mfma_f32_16x16x32_bf16 v[56:59], a[24:27], a[56:59], 0\n"
    "ds_read_b128 a[100:103], v113 offset:29696\n"
    "ds_read_b64_tr_b16 a[162:163], v101 offset:5120\n"
    "v_mfma_f32_16x16x32_bf16 v[56:59], a[28:31], a[60:63], v[56:59]\n"
    "buffer_load_dwordx4 v237, s[36:39], s58 offen offset:1024 lds\n"
    "v_mfma_f32_16x16x32_bf16 v[60:63], a[24:27], a[64:67], 0\n"
    "ds_read_b128 a[104:107], v114 offset:28672\n"
    "ds_read_b128 a[108:111], v114 offset:29696\n"
    "v_mfma_f32_16x16x32_bf16 v[60:63], a[28:31], a[68:71], v[60:63]\n"
    "ds_read_b64_tr_b16 a[164:165], v101 offset:10240\n"
    "v_mfma_f32_16x16x32_bf16 v[64:67], a[32:35], a[40:43], 0\n"
    "ds_read_b128 a[112:115], v115 offset:28672\n"
    "ds_read_b64_tr_b16 a[166:167], v101 offset:15360\n"
    "v_mfma_f32_16x16x32_bf16 v[64:67], a[36:39], a[44:47], v[64:67]\n"
    "ds_read_b128 a[116:119], v115 offset:29696\n"
    "ds_read_b64_tr_b16 a[168:169], v102\n"
    "v_mfma_f32_16x16x32_bf16 v[68:71], a[32:35], a[48:51], 0\n"
    "buffer_load_dwordx4 v238, s[36:39], s58 offen offset:2048 lds\n"
    "v_mfma_f32_16x16x32_bf16 v[68:71], a[36:39], a[52:55], v[68:71]\n"
    "ds_read_b64_tr_b16 a[170:171], v102 offset:5120\n"
    "v_mfma_f32_16x16x32_bf16 v[72:75], a[32:35], a[56:59], 0\n"
    "ds_read_b64_tr_b16 a[172:173], v102 offset:10240\n"
    "v_mfma_f32_16x16x32_bf16 v[72:75], a[36:39], a[60:63], v[72:75]\n"
    "buffer_load_dwordx4 v239, s[36:39], s58 offen offset:3072 lds\n"
    "v_mfma_f32_16x16x32_bf16 v[76:79], a[32:35], a[64:67], 0\n"
    "s_add_u32 m0, s85, 147456\n"
    "ds_read_b64_tr_b16 a[174:175], v102 offset:15360\n"
    "v_mfma_f32_16x16x32_bf16 v[76:79], a[36:39], a[68:71], v[76:79]\n"
    "s_waitcnt lgkmcnt(4)\n"
    "; ---- C1\n"
    "v_mfma_f32_16x16x32_bf16 v[0:3], a[120:123], a[80:83], v[0:3]\n"
    "ds_read_b64_tr_b16 a[176:177], v103\n"
    "v_mfma_f32_16x16x32_bf16 v[0:3], a[124:127], a[84:87], v[0:3]\n"
    "buffer_load_dwordx4 v240, s[36:39], s58 offen lds\n"
    "v_mfma_f32_16x16x32_bf16 v[4:7], a[120:123], a[88:91], v[4:7]\n"
    "s_add_u32 s91, s91, 1\n"
    "ds_read_b64_tr_b16 a[178:179], v103 offset:5120\n"
    "v_mfma_f32_16x16x32_bf16 v[4:7], a[124:127], a[92:95], v[4:7]\n"
    "s_cmp_eq_u32 s91, 12\n"
    "s_cselect_b32 s91, 0, s91\n"
    "v_mfma_f32_16x16x32_bf16 v[8:11], a[120:123], a[96:99], v[8:11]\n"
    "s_add_u32 s58, s91, s92\n"
    "s_and_b32 s58, s58, 15\n"
    "v_mfma_f32_16x16x32_bf16 v[8:11], a[124:127], a[100:103], v[8:11]\n"
    "s_mul_i32 s58, s58, 81920\n"
    "s_add_u32 s58, s58, s88\n"
    "v_mfma_f32_16x16x32_bf16 v[12:15], a[120:123], a[104:107], v[12:15]\n"
    "ds_read_b64_tr_b16 a[180:181], v103 offset:10240\n"
    "s_add_u32 m0, s85, 8192\n"
    "v_mfma_f32_16x16x32_bf16 v[12:15], a[124:127], a[108:111], v[12:15]\n"
    "ds_read_b64_tr_b16 a[182:183], v103 offset:15360\n"
    "ds_read_b64_tr_b16 a[184:185], v104\n"
    "v_mfma_f32_16x16x32_bf16 v[80:83], a[120:123], a[112:115], v[80:83]\n"
    "buffer_load_dwordx4 v236, s[36:39], s58 offen lds\n"
    "v_mfma_f32_16x16x32_bf16 v[80:83], a[124:127], a[116:119], v[80:83]\n"
    "ds_read_b64_tr_b16 a[186:187], v104 offset:5120\n"
    "v_mfma_f32_16x16x32_bf16 v[16:19], a[128:131], a[80:83], v[16:19]\n"
    "buffer_load_dwordx4 v237, s[36:39], s58 offen offset:1024 lds\n"
    "v_mfma_f32_16x16x32_bf16 v[16:19], a[132:135], a[84:87], v[16:19]\n"
    "ds_read_b64_tr_b16 a[188:189], v104 offset:10240\n"
    "ds_read_b64_tr_b16 a[190:191], v104 offset:15360\n"
    "v_mfma_f32_16x16x32_bf16 v[20:23], a[128:131], a[88:91], v[20:23]\n"
    "v_mfma_f32_16x16x32_bf16 v[20:23], a[132:135], a[92:95], v[20:23]\n"
    "ds_read_b64_tr_b16 a[192:193], v105\n"
    "v_mfma_f32_16x16x32_bf16 v[24:27], a[128:131], a[96:99], v[24:27]\n"
    "buffer_load_dwordx4 v238, s[36:39], s58 offen offset:2048 lds\n"
    "v_mfma_f32_16x16x32_bf16 v[24:27], a[132:135], a[100:103], v[24:27]\n"
    "ds_read_b64_tr_b16 a[194:195], v105 offset:5120\n"
    "v_mfma_f32_16x16x32_bf16 v[28:31], a[128:131], a[104:107], v[28:31]\n"
    "ds_read_b64_tr_b16 a[196:197], v105 offset:10240\n"
    "v_mfma_f32_16x16x32_bf16 v[28:31], a[132:135], a[108:111], v[28:31]\n"
    "v_mfma_f32_16x16x32_bf16 v[84:87], a[128:131], a[112:115], v[84:87]\n"
    "ds_read_b64_tr_b16 a[198:199], v105 offset:15360\n"
    "v_mfma_f32_16x16x32_bf16 v[84:87], a[132:135], a[116:119], v[84:87]\n"
    "v_mfma_f32_16x16x32_bf16 v[32:35], a[136:139], a[80:83], v[32:35]\n"
    "buffer_load_dwordx4 v239, s[36:39], s58 offen offset:3072 lds\n"
    "v_mfma_f32_16x16x32_bf16 v[32:35], a[140:143], a[84:87], v[32:35]\n"
    "s_add_u32 m0, s85, 12288\n"
    "v_mfma_f32_16x16x32_bf16 v[36:39], a[136:139], a[88:91], v[36:39]\n"
    "buffer_load_dwordx4 v240, s[36:39], s58 offen lds\n"
    "v_mfma_f32_16x16x32_bf16 v[36:39], a[140:143], a[92:95], v[36:39]\n"
    "s_add_u32 s91, s91, 1\n"
    "v_mfma_f32_16x16x32_bf16 v[40:43], a[136:139], a[96:99], v[40:43]\n"
    "s_cmp_eq_u32 s91, 12\n"
    "s_cselect_b32 s91, 0, s91\n"
    "v_mfma_f32_16x16x32_bf16 v[40:43], a[140:143], a[100:103], v[40:43]\n"
    "s_add_u32 s58, s91, s92\n"
    "s_and_b32 s58, s58, 15\n"
    "v_mfma_f32_16x16x32_bf16 v[44:47], a[136:139], a[104:107], v[44:47]\n"
    "s_mul_i32 s58, s58, 81920\n"
    "s_add_u32 s58, s58, s88\n"
    "v_mfma_f32_16x16x32_bf16 v[44:47], a[140:143], a[108:111], v[44:47]\n"
    "v_mfma_f32_16x16x32_bf16 v[88:91], a[136:139], a[112:115], v[88:91]\n"
    "s_waitcnt lgkmcnt(0)\n"
    "s_barrier\n"
    "s_cmp_lg_u32 s56, 0\n"
    "s_cbranch_scc1 LhAe\n"
    "s_mov_b64 exec, 56\n"
    "global_load_dwordx2 v[138:139], v244, s[80:81] sc1\n"
    "s_mov_b64 exec, -1\n"
    "LhAe:\n"
    "v_mfma_f32_16x16x32_bf16 v[88:91], a[140:143], a[116:119], v[88:91]\n"
    "ds_read_b128 a[40:43], v111 offset:47104\n"
    "v_mfma_f32_16x16x32_bf16 v[48:51], a[144:147], a[80:83], v[48:51]\n"
    "s_add_u32 m0, s85, 65536\n"
    "ds_read_b128 a[44:47], v111 offset:48128\n"
    "v_mfma_f32_16x16x32_bf16 v[48:51], a[148:151], a[84:87], v[48:51]\n"
    "buffer_load_dwordx4 v236, s[36:39], s58 offen lds\n"
    "v_mfma_f32_16x16x32_bf16 v[52:55], a[144:147], a[88:91], v[52:55]\n"
    "ds_read_b128 a[48:51], v112 offset:47104\n"
    "ds_read_b128 a[52:55], v112 offset:48128\n"
    "v_mfma_f32_16x16x32_bf16 v[52:55], a[148:151], a[92:95], v[52:55]\n"
    "ds_read_b128 a[56:59], v113 offset:47104\n"
    "ds_read_b128 a[60:63], v113 offset:48128\n"
    "v_mfma_f32_16x16x32_bf16 v[56:59], a[144:147], a[96:99], v[56:59]\n"
    "ds_read_b128 a[64:67], v114 offset:47104\n"
    "v_mfma_f32_16x16x32_bf16 v[56:59], a[148:151], a[100:103], v[56:59]\n"
    "ds_read_b128 a[68:71], v114 offset:48128\n"
    "v_mfma_f32_16x16x32_bf16 v[60:63], a[144:147], a[104:107], v[60:63]\n"
    "ds_read_b128 a[72:75], v115 offset:47104\n"
    "v_mfma_f32_16x16x32_bf16 v[60:63], a[148:151], a[108:111], v[60:63]\n"
    "ds_read_b128 a[76:79], v115 offset:48128\n"
    "v_mfma_f32_16x16x32_bf16 v[64:67], a[152:155], a[80:83], v[64:67]\n"
    "buffer_load_dwordx4 v237, s[36:39], s58 offen offset:1024 lds\n"
    "v_mfma_f32_16x16x32_bf16 v[64:67], a[156:159], a[84:87], v[64:67]\n"
    "v_mfma_f32_16x16x32_bf16 v[68:71], a[152:155], a[88:91], v[68:71]\n"
    "v_mfma_f32_16x16x32_bf16 v[68:71], a[156:159], a[92:95], v[68:71]\n"
    "v_mfma_f32_16x16x32_bf16 v[72:75], a[152:155], a[96:99], v[72:75]\n"
    "v_mfma_f32_16x16x32_bf16 v[72:75], a[156:159], a[100:103], v[72:75]\n"
    "v_mfma_f32_16x16x32_bf16 v[76:79], a[152:155], a[104:107], v[76:79]\n"
    "v_mfma_f32_16x16x32_bf16 v[76:79], a[156:159], a[108:111], v[76:79]\n"
    "s_waitcnt lgkmcnt(0)\n"
    "; ---- C2\n"
    "v_mfma_f32_16x16x32_bf16 v[0:3], a[160:163], a[40:43], v[0:3]\n"
    "buffer_load_dwordx4 v238, s[36:39], s58 offen offset:2048 lds\n"
    "v_mfma_f32_16x16x32_bf16 v[0:3], a[164:167], a[44:47], v[0:3]\n"
    "v_mfma_f32_16x16x32_bf16 v[4:7], a[160:163], a[48:51], v[4:7]\n"
    "v_mfma_f32_16x16x32_bf16 v[4:7], a[164:167], a[52:55], v[4:7]\n"
    "v_mfma_f32_16x16x32_bf16 v[8:11], a[160:163], a[56:59], v[8:11]\n"
    "v_mfma_f32_16x16x32_bf16 v[8:11], a[164:167], a[60:63], v[8:11]\n"
    "v_mfma_f32_16x16x32_bf16 v[12:15], a[160:163], a[64:67], v[12:15]\n"
    "v_mfma_f32_16x16x32_bf16 v[12:15], a[164:167], a[68:71], v[12:15]\n"
    "v_mfma_f32_16x16x32_bf16 v[80:83], a[160:163], a[72:75], v[80:83]\n"
    "v_mfma_f32_16x16x32_bf16 v[80:83], a[164:167], a[76:79], v[80:83]\n"
    "v_mfma_f32_16x16x32_bf16 v[16:19], a[168:171], a[40:43], v[16:19]\n"
    "buffer_load_dwordx4 v239, s[36:39], s58 offen offset:3072 lds\n"
    "v_mfma_f32_16x16x32_bf16 v[16:19], a[172:175], a[44:47], v[16:19]\n"
    "s_add_u32 m0, s85, 69632\n"
    "v_mfma_f32_16x16x32_bf16 v[20:23], a[168:171], a[48:51], v[20:23]\n"
    "v_mfma_f32_16x16x32_bf16 v[20:23], a[172:175], a[52:55], v[20:23]\n"
    "v_mfma_f32_16x16x32_bf16 v[24:27], a[168:171], a[56:59], v[24:27]\n"
    "v_mfma_f32_16x16x32_bf16 v[24:27], a[172:175], a[60:63], v[24:27]\n"
    "v_mfma_f32_16x16x32_bf16 v[28:31], a[168:171], a[64:67], v[28:31]\n"
    "v_mfma_f32_16x16x32_bf16 v[28:31], a[172:175], a[68:71], v[28:31]\n"
    "v_mfma_f32_16x16x32_bf16 v[84:87], a[168:171], a[72:75], v[84:87]\n"
    "buffer_load_dwordx4 v240, s[36:39], s58 offen lds\n"
    "v_mfma_f32_16x16x32_bf16 v[84:87], a[172:175], a[76:79], v[84:87]\n"
    "s_add_u32 s91, s91, 1\n"
    "v_mfma_f32_16x16x32_bf16 v[32:35], a[176:179], a[40:43], v[32:35]\n"
    "s_cmp_eq_u32 s91, 12\n"
    "s_cselect_b32 s91, 0, s91\n"
    "v_mfma_f32_16x16x32_bf16 v[32:35], a[180:183], a[44:47], v[32:35]\n"
    "s_add_u32 s58, s91, s92\n"
    "s_and_b32 s58, s58, 15\n"
    "v_mfma_f32_16x16x32_bf16 v[36:39], a[176:179], a[48:51], v[36:39]\n"
    "s_mul_i32 s58, s58, 81920\n"
    "s_add_u32 s58, s58, s88\n"
    "v_mfma_f32_16x16x32_bf16 v[36:39], a[180:183], a[52:55], v[36:39]\n"
    "v_mfma_f32_16x16x32_bf16 v[40:43], a[176:179], a[56:59], v[40:43]\n"
    "v_mfma_f32_16x16x32_bf16 v[40:43], a[180:183], a[60:63], v[40:43]\n"
    "v_mfma_f32_16x16x32_bf16 v[44:47], a[176:179], a[64:67], v[44:47]\n"
    "v_mfma_f32_16x16x32_bf16 v[44:47], a[180:183], a[68:71], v[44:47]\n"
    "v_mfma_f32_16x16x32_bf16 v[88:91], a[176:179], a[72:75], v[88:91]\n"
    "s_cmp_lg_u32 s56, 0\n"
    "s_cbranch_scc1 LhAj\n"
    "s_mov_b64 exec, 56\n"
    "s_mov_b32 s74, 1048576\n"
    "s_waitcnt vmcnt(5)\n"
    "s_branch LhAq\n"
    "LhAp:\n"
    "global_load_dwordx2 v[138:139], v244, s[80:81] sc1\n"
    "s_waitcnt vmcnt(0)\n"
    "LhAq:\n"
    "v_cmp_ne_u32 vcc, v138, v230\n"
    "s_cbranch_vccz LhAc\n"
    "v_mov_b32 v230, v138\n"
    "global_store_dword v243, v230, s[80:81] offset:4 sc1\n"
    "LhAc:\n"
    "v_cmp_ne_u32 vcc, v139, v245\n"
    "s_cbranch_vccz LhAd\n"
    "s_sub_u32 s74, s74, 1\n"
    "s_cbranch_scc0 LhAp\n"
    "LhAd:\n"
    "s_mov_b64 exec, -1\n"
    "LhAj:\n"
    "s_barrier\n"
    "v_mfma_f32_16x16x32_bf16 v[88:91], a[180:183], a[76:79], v[88:91]\n"
    "ds_read_b128 a[80:83], v116 offset:40960\n"
    "v_mfma_f32_16x16x32_bf16 v[48:51], a[184:187], a[40:43], v[48:51]\n"
    "s_add_u32 m0, s86, 124928\n"
    "ds_read_b128 a[84:87], v116 offset:41984\n"
    "v_mfma_f32_16x16x32_bf16 v[48:51], a[188:191], a[44:47], v[48:51]\n"
    "buffer_load_dwordx4 v241, s[52:55], s89 offen sc1 lds\n"
    "v_mfma_f32_16x16x32_bf16 v[52:55], a[184:187], a[48:51], v[52:55]\n"
    "ds_read_b128 a[88:91], v117 offset:40960\n"
    "ds_read_b128 a[92:95], v117 offset:41984\n"
    "v_mfma_f32_16x16x32_bf16 v[52:55], a[188:191], a[52:55], v[52:55]\n"
    "ds_read_b128 a[96:99], v118 offset:40960\n"
    "ds_read_b128 a[100:103], v118 offset:41984\n"
    "v_mfma_f32_16x16x32_bf16 v[56:59], a[184:187], a[56:59], v[56:59]\n"
    "buffer_load_dwordx4 v241, s[52:55], s89 offen offset:1024 sc1 lds\n"
    "v_mfma_f32_16x16x32_bf16 v[56:59], a[188:191], a[60:63], v[56:59]\n"
    "s_add_u32 s90, s89, 2048\n"
    "ds_read_b128 a[104:107], v119 offset:40960\n"
    "v_mfma_f32_16x16x32_bf16 v[60:63], a[184:187], a[64:67], v[60:63]\n"
    "ds_read_b64_tr_b16 a[0:1], v101 offset:20480\n"
    "ds_read_b64_tr_b16 a[2:3], v101 offset:25600\n"
    "v_mfma_f32_16x16x32_bf16 v[60:63], a[188:191], a[68:71], v[60:63]\n"
    "ds_read_b128 a[108:111], v119 offset:41984\n"
    "v_mfma_f32_16x16x32_bf16 v[64:67], a[192:195], a[40:43], v[64:67]\n"
    "ds_read_b64_tr_b16 a[4:5], v101 offset:30720\n"
    "ds_read_b64_tr_b16 a[6:7], v101 offset:35840\n"
    "v_mfma_f32_16x16x32_bf16 v[64:67], a[196:199], a[44:47], v[64:67]\n"
    "ds_read_b128 a[112:115], v120 offset:40960\n"
    "ds_read_b128 a[116:119], v120 offset:41984\n"
    "v_mfma_f32_16x16x32_bf16 v[68:71], a[192:195], a[48:51], v[68:71]\n"
    "buffer_load_dwordx4 v241, s[52:55], s90 offen offset:2048 sc1 lds\n"
    "v_mfma_f32_16x16x32_bf16 v[68:71], a[196:199], a[52:55], v[68:71]\n"
    "buffer_load_dwordx4 v241, s[52:55], s90 offen offset:3072 sc1 lds\n"
    "v_mfma_f32_16x16x32_bf16 v[72:75], a[192:195], a[56:59], v[72:75]\n"
    "s_add_u32 m0, s87, 124928\n"
    "v_mfma_f32_16x16x32_bf16 v[72:75], a[196:199], a[60:63], v[72:75]\n"
    "buffer_load_dword v242, s[52:55], s89 offen sc1 lds\n"
    "v_mfma_f32_16x16x32_bf16 v[76:79], a[192:195], a[64:67], v[76:79]\n"
    "v_mfma_f32_16x16x32_bf16 v[76:79], a[196:199], a[68:71], v[76:79]\n"
    "s_waitcnt lgkmcnt(0)\n"
    "ds_read_b64_tr_b16 a[8:9], v102 offset:20480\n"
    "; ---- C3\n"
    "v_mfma_f32_16x16x32_bf16 v[0:3], a[0:3], a[80:83], v[0:3]\n"
    "ds_read_b64_tr_b16 a[10:11], v102 offset:25600\n"
    "ds_read_b64_tr_b16 a[12:13], v102 offset:30720\n"
    "v_mfma_f32_16x16x32_bf16 v[0:3], a[4:7], a[84:87], v[0:3]\n"
    "ds_read_b64_tr_b16 a[14:15], v102 offset:35840\n"
    "v_mfma_f32_16x16x32_bf16 v[4:7], a[0:3], a[88:91], v[4:7]\n"
    "buffer_load_dword v242, s[52:55], s89 offen offset:256 sc1 lds\n"
    "v_mfma_f32_16x16x32_bf16 v[4:7], a[4:7], a[92:95], v[4:7]\n"
    "s_add_u32 s62, s62, 1\n"
    "v_mfma_f32_16x16x32_bf16 v[8:11], a[0:3], a[96:99], v[8:11]\n"
    "s_cmp_eq_u32 s62, 12\n"
    "s_cselect_b32 s62, 0, s62\n"
    "v_mfma_f32_16x16x32_bf16 v[8:11], a[4:7], a[100:103], v[8:11]\n"
    "s_add_u32 s90, s62, s92\n"
    "s_bfe_u32 s75, s90, 0x30001\n"
    "v_mfma_f32_16x16x32_bf16 v[12:15], a[0:3], a[104:107], v[12:15]\n"
    "s_mul_i32 s89, s75, 36864\n"
    "s_and_b32 s75, s90, 1\n"
    "v_mfma_f32_16x16x32_bf16 v[12:15], a[4:7], a[108:111], v[12:15]\n"
    "s_lshl_b32 s75, s75, 11\n"
    "s_add_u32 s89, s89, s75\n"
    "v_mfma_f32_16x16x32_bf16 v[80:83], a[0:3], a[112:115], v[80:83]\n"
    "s_add_u32 m0, s86, 28672\n"
    "v_mfma_f32_16x16x32_bf16 v[80:83], a[4:7], a[116:119], v[80:83]\n"
    "s_waitcnt lgkmcnt(0)\n"
    "buffer_load_dwordx4 v241, s[52:55], s89 offen sc1 lds\n"
    "v_mfma_f32_16x16x32_bf16 v[16:19], a[8:11], a[80:83], v[16:19]\n"
    "buffer_load_dwordx4 v241, s[52:55], s89 offen offset:1024 sc1 lds\n"
    "v_mfma_f32_16x16x32_bf16 v[16:19], a[12:15], a[84:87], v[16:19]\n"
    "s_add_u32 s90, s89, 2048\n"
    "ds_read_b64_tr_b16 a[16:17], v103 offset:20480\n"
    "v_mfma_f32_16x16x32_bf16 v[20:23], a[8:11], a[88:91], v[20:23]\n"
    "ds_read_b64_tr_b16 a[18:19], v103 offset:25600\n"
    "ds_read_b64_tr_b16 a[20:21], v103 offset:30720\n"
    "v_mfma_f32_16x16x32_bf16 v[20:23], a[12:15], a[92:95], v[20:23]\n"
    "ds_read_b64_tr_b16 a[22:23], v103 offset:35840\n"
    "v_mfma_f32_16x16x32_bf16 v[24:27], a[8:11], a[96:99], v[24:27]\n"
    "buffer_load_dwordx4 v241, s[52:55], s90 offen offset:2048 sc1 lds\n"
    "v_mfma_f32_16x16x32_bf16 v[24:27], a[12:15], a[100:103], v[24:27]\n"
    "v_mfma_f32_16x16x32_bf16 v[28:31], a[8:11], a[104:107], v[28:31]\n"
    "buffer_load_dwordx4 v241, s[52:55], s90 offen offset:3072 sc1 lds\n"
    "v_mfma_f32_16x16x32_bf16 v[28:31], a[12:15], a[108:111], v[28:31]\n"
    "s_add_u32 m0, s87, 28672\n"
    "v_mfma_f32_16x16x32_bf16 v[84:87], a[8:11], a[112:115], v[84:87]\n"
    "v_mfma_f32_16x16x32_bf16 v[84:87], a[12:15], a[116:119], v[84:87]\n"
    "s_waitcnt lgkmcnt(0)\n"
    "ds_read_b64_tr_b16 a[24:25], v104 offset:20480\n"
    "v_mfma_f32_16x16x32_bf16 v[32:35], a[16:19], a[80:83], v[32:35]\n"
    "ds_read_b64_tr_b16 a[26:27], v104 offset:25600\n"
    "ds_read_b64_tr_b16 a[28:29], v104 offset:30720\n"
    "v_mfma_f32_16x16x32_bf16 v[32:35], a[20:23], a[84:87], v[32:35]\n"
    "buffer_load_dword v242, s[52:55], s89 offen sc1 lds\n"
    "v_mfma_f32_16x16x32_bf16 v[36:39], a[16:19], a[88:91], v[36:39]\n"
    "ds_read_b64_tr_b16 a[30:31], v104 offset:35840\n"
    "v_mfma_f32_16x16x32_bf16 v[36:39], a[20:23], a[92:95], v[36:39]\n"
    "buffer_load_dword v242, s[52:55], s89 offen offset:256 sc1 lds\n"
    "v_mfma_f32_16x16x32_bf16 v[40:43], a[16:19], a[96:99], v[40:43]\n"
    "s_add_u32 s62, s62, 1\n"
    "v_mfma_f32_16x16x32_bf16 v[40:43], a[20:23], a[100:103], v[40:43]\n"
    "s_cmp_eq_u32 s62, 12\n"
    "s_cselect_b32 s62, 0, s62\n"
    "v_mfma_f32_16x16x32_bf16 v[44:47], a[16:19], a[104:107], v[44:47]\n"
    "s_add_u32 s90, s62, s92\n"
    "s_bfe_u32 s75, s90, 0x30001\n"
    "v_mfma_f32_16x16x32_bf16 v[44:47], a[20:23], a[108:111], v[44:47]\n"
    "s_mul_i32 s89, s75, 36864\n"
    "s_and_b32 s75, s90, 1\n"
    "s_lshl_b32 s75, s75, 11\n"
    "s_add_u32 s89, s89, s75\n"
    "v_mfma_f32_16x16x32_bf16 v[88:91], a[16:19], a[112:115], v[88:91]\n"
    "s_waitcnt vmcnt(6)\n"
    "s_barrier\n"
    "v_mfma_f32_16x16x32_bf16 v[88:91], a[20:23], a[116:119], v[88:91]\n"
    "s_waitcnt lgkmcnt(0)\n"
    "ds_read_b64_tr_b16 a[32:33], v105 offset:20480\n"
    "v_mfma_f32_16x16x32_bf16 v[48:51], a[24:27], a[80:83], v[48:51]\n"
    "ds_read_b64_tr_b16 a[34:35], v105 offset:25600\n"
    "ds_read_b64_tr_b16 a[36:37], v105 offset:30720\n"
    "v_mfma_f32_16x16x32_bf16 v[48:51], a[28:31], a[84:87], v[48:51]\n"
    "ds_read_b64_tr_b16 a[38:39], v105 offset:35840\n"
    "ds_read_b128 a[40:43], v116 offset:59392\n"
    "v_mfma_f32_16x16x32_bf16 v[52:55], a[24:27], a[88:91], v[52:55]\n"
    "s_add_u32 m0, s86, 47104\n"
    "ds_read_b128 a[44:47], v116 offset:60416\n"
    "v_mfma_f32_16x16x32_bf16 v[52:55], a[28:31], a[92:95], v[52:55]\n"
    "buffer_load_dwordx4 v241, s[52:55], s89 offen sc1 lds\n"
    "v_mfma_f32_16x16x32_bf16 v[56:59], a[24:27], a[96:99], v[56:59]\n"
    "ds_read_b128 a[48:51], v117 offset:59392\n"
    "ds_read_b128 a[52:55], v117 offset:60416\n"
    "v_mfma_f32_16x16x32_bf16 v[56:59], a[28:31], a[100:103], v[56:59]\n"
    "ds_read_b128 a[56:59], v118 offset:59392\n"
    "ds_read_b128 a[60:63], v118 offset:60416\n"
    "v_mfma_f32_16x16x32_bf16 v[60:63], a[24:27], a[104:107], v[60:63]\n"
    "ds_read_b128 a[64:67], v119 offset:59392\n"
    "ds_read_b64_tr_b16 a[0:1], v106 offset:12288\n"
    "v_mfma_f32_16x16x32_bf16 v[60:63], a[28:31], a[108:111], v[60:63]\n"
    "s_waitcnt lgkmcnt(8)\n"
    "ds_read_b64_tr_b16 a[2:3], v106 offset:17408\n"
    "v_mfma_f32_16x16x32_bf16 v[64:67], a[32:35], a[80:83], v[64:67]\n"
    "ds_read_b128 a[68:71], v119 offset:60416\n"
    "ds_read_b64_tr_b16 a[4:5], v106 offset:22528\n"
    "v_mfma_f32_16x16x32_bf16 v[64:67], a[36:39], a[84:87], v[64:67]\n"
    "ds_read_b64_tr_b16 a[6:7], v106 offset:27648\n"
    "ds_read_b128 a[72:75], v120 offset:59392\n"
    "v_mfma_f32_16x16x32_bf16 v[68:71], a[32:35], a[88:91], v[68:71]\n"
    "buffer_load_dwordx4 v241, s[52:55], s89 offen offset:1024 sc1 lds\n"
    "v_mfma_f32_16x16x32_bf16 v[68:71], a[36:39], a[92:95], v[68:71]\n"
    "s_add_u32 s90, s89, 2048\n"
    "ds_read_b128 a[76:79], v120 offset:60416\n"
    "v_mfma_f32_16x16x32_bf16 v[72:75], a[32:35], a[96:99], v[72:75]\n"
    "v_mfma_f32_16x16x32_bf16 v[72:75], a[36:39], a[100:103], v[72:75]\n"
    "buffer_load_dwordx4 v241, s[52:55], s90 offen offset:2048 sc1 lds\n"
    "v_mfma_f32_16x16x32_bf16 v[76:79], a[32:35], a[104:107], v[76:79]\n"
    "v_mfma_f32_16x16x32_bf16 v[76:79], a[36:39], a[108:111], v[76:79]\n"
    "s_waitcnt lgkmcnt(0)\n"
    "ds_read_b64_tr_b16 a[8:9], v107 offset:12288\n"
    "; ---- C4\n"
    "v_mfma_f32_16x16x32_bf16 v[0:3], a[0:3], a[40:43], v[0:3]\n"
    "ds_read_b64_tr_b16 a[10:11], v107 offset:17408\n"
    "ds_read_b64_tr_b16 a[12:13], v107 offset:22528\n"
    "v_mfma_f32_16x16x32_bf16 v[0:3], a[4:7], a[44:47], v[0:3]\n"
    "ds_read_b64_tr_b16 a[14:15], v107 offset:27648\n"
    "v_mfma_f32_16x16x32_bf16 v[4:7], a[0:3], a[48:51], v[4:7]\n"
    "v_mfma_f32_16x16x32_bf16 v[4:7], a[4:7], a[52:55], v[4:7]\n"
    "v_mfma_f32_16x16x32_bf16 v[8:11], a[0:3], a[56:59], v[8:11]\n"
    "v_mfma_f32_16x16x32_bf16 v[8:11], a[4:7], a[60:63], v[8:11]\n"
    "buffer_load_dwordx4 v241, s[52:55], s90 offen offset:3072 sc1 lds\n"
    "v_mfma_f32_16x16x32_bf16 v[12:15], a[0:3], a[64:67], v[12:15]\n"
    "s_add_u32 m0, s87, 47104\n"
    "v_mfma_f32_16x16x32_bf16 v[12:15], a[4:7], a[68:71], v[12:15]\n"
    "v_mfma_f32_16x16x32_bf16 v[80:83], a[0:3], a[72:75], v[80:83]\n"
    "v_mfma_f32_16x16x32_bf16 v[80:83], a[4:7], a[76:79], v[80:83]\n"
    "s_waitcnt lgkmcnt(0)\n"
    "ds_read_b64_tr_b16 a[16:17], v108 offset:12288\n"
    "v_mfma_f32_16x16x32_bf16 v[16:19], a[8:11], a[40:43], v[16:19]\n"
    "ds_read_b64_tr_b16 a[18:19], v108 offset:17408\n"
    "ds_read_b64_tr_b16 a[20:21], v108 offset:22528\n"
    "v_mfma_f32_16x16x32_bf16 v[16:19], a[12:15], a[44:47], v[16:19]\n"
    "ds_read_b64_tr_b16 a[22:23], v108 offset:27648\n"
    "v_mfma_f32_16x16x32_bf16 v[20:23], a[8:11], a[48:51], v[20:23]\n"
    "buffer_load_dword v242, s[52:55], s89 offen sc1 lds\n"
    "v_mfma_f32_16x16x32_bf16 v[20:23], a[12:15], a[52:55], v[20:23]\n"
    "v_mfma_f32_16x16x32_bf16 v[24:27], a[8:11], a[56:59], v[24:27]\n"
    "v_mfma_f32_16x16x32_bf16 v[24:27], a[12:15], a[60:63], v[24:27]\n"
    "v_mfma_f32_16x16x32_bf16 v[28:31], a[8:11], a[64:67], v[28:31]\n"
    "v_mfma_f32_16x16x32_bf16 v[28:31], a[12:15], a[68:71], v[28:31]\n"
    "v_mfma_f32_16x16x32_bf16 v[84:87], a[8:11], a[72:75], v[84:87]\n"
    "v_mfma_f32_16x16x32_bf16 v[84:87], a[12:15], a[76:79], v[84:87]\n"
    "s_waitcnt lgkmcnt(0)\n"
    "ds_read_b64_tr_b16 a[24:25], v109 offset:12288\n"
    "v_mfma_f32_16x16x32_bf16 v[32:35], a[16:19], a[40:43], v[32:35]\n"
    "ds_read_b64_tr_b16 a[26:27], v109 offset:17408\n"
    "ds_read_b64_tr_b16 a[28:29], v109 offset:22528\n"
    "v_mfma_f32_16x16x32_bf16 v[32:35], a[20:23], a[44:47], v[32:35]\n"
    "buffer_load_dword v242, s[52:55], s89 offen offset:256 sc1 lds\n"
    "v_mfma_f32_16x16x32_bf16 v[36:39], a[16:19], a[48:51], v[36:39]\n"
    "s_add_u32 s62, s62, 1\n"
    "ds_read_b64_tr_b16 a[30:31], v109 offset:27648\n"
    "v_mfma_f32_16x16x32_bf16 v[36:39], a[20:23], a[52:55], v[36:39]\n"
    "s_cmp_eq_u32 s62, 12\n"
    "s_cselect_b32 s62, 0, s62\n"
    "v_mfma_f32_16x16x32_bf16 v[40:43], a[16:19], a[56:59], v[40:43]\n"
    "s_add_u32 s90, s62, s92\n"
    "s_bfe_u32 s75, s90, 0x30001\n"
    "v_mfma_f32_16x16x32_bf16 v[40:43], a[20:23], a[60:63], v[40:43]\n"
    "s_mul_i32 s89, s75, 36864\n"
    "s_and_b32 s75, s90, 1\n"
    "v_mfma_f32_16x16x32_bf16 v[44:47], a[16:19], a[64:67], v[44:47]\n"
    "s_lshl_b32 s75, s75, 11\n"
    "s_add_u32 s89, s89, s75\n"
    "v_mfma_f32_16x16x32_bf16 v[44:47], a[20:23], a[68:71], v[44:47]\n"
    "ds_read_b64_tr_b16 a[32:33], v110 offset:12288\n"
    "ds_read_b64_tr_b16 a[34:35], v110 offset:17408\n"
    "v_mfma_f32_16x16x32_bf16 v[88:91], a[16:19], a[72:75], v[88:91]\n"
    "s_waitcnt vmcnt(6)\n"
    "s_barrier\n"
    "v_mfma_f32_16x16x32_bf16 v[88:91], a[20:23], a[76:79], v[88:91]\n"
    "s_waitcnt lgkmcnt(2)\n"
    "ds_read_b64_tr_b16 a[36:37], v110 offset:22528\n"
    "v_mfma_f32_16x16x32_bf16 v[48:51], a[24:27], a[40:43], v[48:51]\n"
    "ds_read_b64_tr_b16 a[38:39], v110 offset:27648\n"
    "ds_read_b128 a[80:83], v111 offset:28672\n"
    "v_mfma_f32_16x16x32_bf16 v[48:51], a[28:31], a[44:47], v[48:51]\n"
    "s_add_u32 m0, s86, 106496\n"
    "ds_read_b128 a[84:87], v111 offset:29696\n"
    "v_mfma_f32_16x16x32_bf16 v[52:55], a[24:27], a[48:51], v[52:55]\n"
    "buffer_load_dwordx4 v241, s[52:55], s89 offen sc1 lds\n"
    "v_mfma_f32_16x16x32_bf16 v[52:55], a[28:31], a[52:55], v[52:55]\n"
    "ds_read_b128 a[88:91], v112 offset:28672\n"
    "ds_read_b128 a[92:95], v112 offset:29696\n"
    "v_mfma_f32_16x16x32_bf16 v[56:59], a[24:27], a[56:59], v[56:59]\n"
    "ds_read_b128 a[96:99], v113 offset:28672\n"
    "ds_read_b128 a[100:103], v113 offset:29696\n"
    "v_mfma_f32_16x16x32_bf16 v[56:59], a[28:31], a[60:63], v[56:59]\n"
    "buffer_load_dwordx4 v241, s[52:55], s89 offen offset:1024 sc1 lds\n"
    "v_mfma_f32_16x16x32_bf16 v[60:63], a[24:27], a[64:67], v[60:63]\n"
    "s_add_u32 s90, s89, 2048\n"
    "ds_read_b128 a[104:107], v114 offset:28672\n"
    "v_mfma_f32_16x16x32_bf16 v[60:63], a[28:31], a[68:71], v[60:63]\n"
    "s_waitcnt lgkmcnt(7)\n"
    "ds_read_b64_tr_b16 a[0:1], v96 offset:8192\n"
    "v_mfma_f32_16x16x32_bf16 v[64:67], a[32:35], a[40:43], v[64:67]\n"
    "ds_read_b64_tr_b16 a[2:3], v96 offset:13312\n"
    "ds_read_b128 a[108:111], v114 offset:29696\n"
    "v_mfma_f32_16x16x32_bf16 v[64:67], a[36:39], a[44:47], v[64:67]\n"
    "ds_read_b64_tr_b16 a[4:5], v96 offset:18432\n"
    "ds_read_b64_tr_b16 a[6:7], v96 offset:23552\n"
    "v_mfma_f32_16x16x32_bf16 v[68:71], a[32:35], a[48:51], v[68:71]\n"
    "ds_read_b128 a[112:115], v115 offset:28672\n"
    "ds_read_b128 a[116:119], v115 offset:29696\n"
    "v_mfma_f32_16x16x32_bf16 v[68:71], a[36:39], a[52:55], v[68:71]\n"
    "buffer_load_dwordx4 v241, s[52:55], s90 offen offset:2048 sc1 lds\n"
    "v_mfma_f32_16x16x32_bf16 v[72:75], a[32:35], a[56:59], v[72:75]\n"
    "buffer_load_dwordx4 v241, s[52:55], s90 offen offset:3072 sc1 lds\n"
    "v_mfma_f32_16x16x32_bf16 v[72:75], a[36:39], a[60:63], v[72:75]\n"
    "s_add_u32 m0, s87, 106496\n"
    "v_mfma_f32_16x16x32_bf16 v[76:79], a[32:35], a[64:67], v[76:79]\n"
    "v_mfma_f32_16x16x32_bf16 v[76:79], a[36:39], a[68:71], v[76:79]\n"
    "s_waitcnt lgkmcnt(0)\n"
    "ds_read_b64_tr_b16 a[8:9], v97 offset:8192\n"
    "s_mov_b32 s63, 2\n"
    "Lgloop:\n"
    "; ---- C5\n"
    "v_mfma_f32_16x16x32_bf16 v[0:3], a[0:3], a[80:83], v[0:3]\n"
    "ds_read_b64_tr_b16 a[10:11], v97 offset:13312\n"
    "ds_read_b64_tr_b16 a[12:13], v97 offset:18432\n"
    "v_mfma_f32_16x16x32_bf16 v[0:3], a[4:7], a[84:87], v[0:3]\n"
    "buffer_load_dword v242, s[52:55], s89 offen sc1 lds\n"
    "v_mfma_f32_16x16x32_bf16 v[4:7], a[0:3], a[88:91], v[4:7]\n"
    "ds_read_b64_tr_b16 a[14:15], v97 offset:23552\n"
    "v_mfma_f32_16x16x32_bf16 v[4:7], a[4:7], a[92:95], v[4:7]\n"
    "buffer_load_dword v242, s[52:55], s89 offen offset:256 sc1 lds\n"
    "v_mfma_f32_16x16x32_bf16 v[8:11], a[0:3], a[96:99], v[8:11]\n"
    "s_add_u32 s62, s62, 1\n"
    "v_mfma_f32_16x16x32_bf16 v[8:11], a[4:7], a[100:103], v[8:11]\n"
    "s_cmp_eq_u32 s62, 12\n"
    "s_cselect_b32 s62, 0, s62\n"
    "v_mfma_f32_16x16x32_bf16 v[12:15], a[0:3], a[104:107], v[12:15]\n"
    "s_add_u32 s90, s62, s92\n"
    "s_bfe_u32 s75, s90, 0x30001\n"
    "v_mfma_f32_16x16x32_bf16 v[12:15], a[4:7], a[108:111], v[12:15]\n"
    "s_mul_i32 s89, s75, 36864\n"
    "s_and_b32 s75, s90, 1\n"
    "v_mfma_f32_16x16x32_bf16 v[80:83], a[0:3], a[112:115], v[80:83]\n"
    "s_lshl_b32 s75, s75, 11\n"
    "s_add_u32 s89, s89, s75\n"
    "v_mfma_f32_16x16x32_bf16 v[80:83], a[4:7], a[116:119], v[80:83]\n"
    "s_waitcnt lgkmcnt(0)\n"
    "s_add_u32 m0, s85, 86016\n"
    "v_mfma_f32_16x16x32_bf16 v[16:19], a[8:11], a[80:83], v[16:19]\n"
    "buffer_load_dwordx4 v236, s[36:39], s58 offen lds\n"
    "v_mfma_f32_16x16x32_bf16 v[16:19], a[12:15], a[84:87], v[16:19]\n"
    "ds_read_b64_tr_b16 a[16:17], v98 offset:8192\n"
    "ds_read_b64_tr_b16 a[18:19], v98 offset:13312\n"
    "v_mfma_f32_16x16x32_bf16 v[20:23], a[8:11], a[88:91], v[20:23]\n"
    "ds_read_b64_tr_b16 a[20:21], v98 offset:18432\n"
    "ds_read_b64_tr_b16 a[22:23], v98 offset:23552\n"
    "v_mfma_f32_16x16x32_bf16 v[20:23], a[12:15], a[92:95], v[20:23]\n"
    "buffer_load_dwordx4 v237, s[36:39], s58 offen offset:1024 lds\n"
    "v_mfma_f32_16x16x32_bf16 v[24:27], a[8:11], a[96:99], v[24:27]\n"
    "v_mfma_f32_16x16x32_bf16 v[24:27], a[12:15], a[100:103], v[24:27]\n"
    "buffer_load_dwordx4 v238, s[36:39], s58 offen offset:2048 lds\n"
    "v_mfma_f32_16x16x32_bf16 v[28:31], a[8:11], a[104:107], v[28:31]\n"
    "v_mfma_f32_16x16x32_bf16 v[28:31], a[12:15], a[108:111], v[28:31]\n"
    "v_mfma_f32_16x16x32_bf16 v[84:87], a[8:11], a[112:115], v[84:87]\n"
    "v_mfma_f32_16x16x32_bf16 v[84:87], a[12:15], a[116:119], v[84:87]\n"
    "s_waitcnt lgkmcnt(0)\n"
    "ds_read_b64_tr_b16 a[24:25], v99 offset:8192\n"
    "v_mfma_f32_16x16x32_bf16 v[32:35], a[16:19], a[80:83], v[32:35]\n"
    "ds_read_b64_tr_b16 a[26:27], v99 offset:13312\n"
    "ds_read_b64_tr_b16 a[28:29], v99 offset:18432\n"
    "v_mfma_f32_16x16x32_bf16 v[32:35], a[20:23], a[84:87], v[32:35]\n"
    "buffer_load_dwordx4 v239, s[36:39], s58 offen offset:3072 lds\n"
    "v_mfma_f32_16x16x32_bf16 v[36:39], a[16:19], a[88:91], v[36:39]\n"
    "s_add_u32 m0, s85, 90112\n"
    "ds_read_b64_tr_b16 a[30:31], v99 offset:23552\n"
    "v_mfma_f32_16x16x32_bf16 v[36:39], a[20:23], a[92:95], v[36:39]\n"
    "buffer_load_dwordx4 v240, s[36:39], s58 offen lds\n"
    "v_mfma_f32_16x16x32_bf16 v[40:43], a[16:19], a[96:99], v[40:43]\n"
    "s_add_u32 s91, s91, 1\n"
    "v_mfma_f32_16x16x32_bf16 v[40:43], a[20:23], a[100:103], v[40:43]\n"
    "s_cmp_eq_u32 s91, 12\n"
    "s_cselect_b32 s91, 0, s91\n"
    "v_mfma_f32_16x16x32_bf16 v[44:47], a[16:19], a[104:107], v[44:47]\n"
    "s_add_u32 s58, s91, s92\n"
    "s_and_b32 s58, s58, 15\n"
    "v_mfma_f32_16x16x32_bf16 v[44:47], a[20:23], a[108:111], v[44:47]\n"
    "s_mul_i32 s58, s58, 81920\n"
    "s_add_u32 s58, s58, s88\n"
    "v_mfma_f32_16x16x32_bf16 v[88:91], a[16:19], a[112:115], v[88:91]\n"
    "s_waitcnt vmcnt(11)\n"
    "s_barrier\n"
    "v_mfma_f32_16x16x32_bf16 v[88:91], a[20:23], a[116:119], v[88:91]\n"
    "s_waitcnt lgkmcnt(0)\n"
    "ds_read_b64_tr_b16 a[32:33], v100 offset:8192\n"
    "v_mfma_f32_16x16x32_bf16 v[48:51], a[24:27], a[80:83], v[48:51]\n"
    "ds_read_b64_tr_b16 a[34:35], v100 offset:13312\n"
    "ds_read_b64_tr_b16 a[36:37], v100 offset:18432\n"
    "v_mfma_f32_16x16x32_bf16 v[48:51], a[28:31], a[84:87], v[48:51]\n"
    "ds_read_b64_tr_b16 a[38:39], v100 offset:23552\n"
    "ds_read_b128 a[40:43], v111 offset:47104\n"
    "v_mfma_f32_16x16x32_bf16 v[52:55], a[24:27], a[88:91], v[52:55]\n"
    "s_add_u32 m0, s86, 124928\n"
    "ds_read_b128 a[44:47], v111 offset:48128\n"
    "v_mfma_f32_16x16x32_bf16 v[52:55], a[28:31], a[92:95], v[52:55]\n"
    "buffer_load_dwordx4 v241, s[52:55], s89 offen sc1 lds\n"
    "v_mfma_f32_16x16x32_bf16 v[56:59], a[24:27], a[96:99], v[56:59]\n"
    "ds_read_b128 a[48:51], v112 offset:47104\n"
    "ds_read_b128 a[52:55], v112 offset:48128\n"
    "v_mfma_f32_16x16x32_bf16 v[56:59], a[28:31], a[100:103], v[56:59]\n"
    "ds_read_b128 a[56:59], v113 offset:47104\n"
    "ds_read_b128 a[60:63], v113 offset:48128\n"
    "v_mfma_f32_16x16x32_bf16 v[60:63], a[24:27], a[104:107], v[60:63]\n"
    "buffer_load_dwordx4 v241, s[52:55], s89 offen offset:1024 sc1 lds\n"
    "v_mfma_f32_16x16x32_bf16 v[60:63], a[28:31], a[108:111], v[60:63]\n"
    "s_waitcnt lgkmcnt(6)\n"
    "s_add_u32 s90, s89, 2048\n"
    "v_mfma_f32_16x16x32_bf16 v[64:67], a[32:35], a[80:83], v[64:67]\n"
    "ds_read_b128 a[64:67], v114 offset:47104\n"
    "ds_read_b64_tr_b16 a[0:1], v101\n"
    "v_mfma_f32_16x16x32_bf16 v[64:67], a[36:39], a[84:87], v[64:67]\n"
    "ds_read_b64_tr_b16 a[2:3], v101 offset:5120\n"
    "ds_read_b128 a[68:71], v114 offset:48128\n"
    "v_mfma_f32_16x16x32_bf16 v[68:71], a[32:35], a[88:91], v[68:71]\n"
    "ds_read_b64_tr_b16 a[4:5], v101 offset:10240\n"
    "ds_read_b64_tr_b16 a[6:7], v101 offset:15360\n"
    "v_mfma_f32_16x16x32_bf16 v[68:71], a[36:39], a[92:95], v[68:71]\n"
    "buffer_load_dwordx4 v241, s[52:55], s90 offen offset:2048 sc1 lds\n"
    "v_mfma_f32_16x16x32_bf16 v[72:75], a[32:35], a[96:99], v[72:75]\n"
    "ds_read_b128 a[72:75], v115 offset:47104\n"
    "ds_read_b128 a[76:79], v115 offset:48128\n"
    "v_mfma_f32_16x16x32_bf16 v[72:75], a[36:39], a[100:103], v[72:75]\n"
    "buffer_load_dwordx4 v241, s[52:55], s90 offen offset:3072 sc1 lds\n"
    "v_mfma_f32_16x16x32_bf16 v[76:79], a[32:35], a[104:107], v[76:79]\n"
    "s_add_u32 m0, s87, 124928\n"
    "v_mfma_f32_16x16x32_bf16 v[76:79], a[36:39], a[108:111], v[76:79]\n"
    "s_waitcnt lgkmcnt(0)\n"
    "ds_read_b64_tr_b16 a[8:9], v102\n"
    "; ---- C6\n"
    "v_mfma_f32_16x16x32_bf16 v[0:3], a[0:3], a[40:43], v[0:3]\n"
    "ds_read_b64_tr_b16 a[10:11], v102 offset:5120\n"
    "ds_read_b64_tr_b16 a[12:13], v102 offset:10240\n"
    "v_mfma_f32_16x16x32_bf16 v[0:3], a[4:7], a[44:47], v[0:3]\n"
    "buffer_load_dword v242, s[52:55], s89 offen sc1 lds\n"
    "v_mfma_f32_16x16x32_bf16 v[4:7], a[0:3], a[48:51], v[4:7]\n"
    "ds_read_b64_tr_b16 a[14:15], v102 offset:15360\n"
    "v_mfma_f32_16x16x32_bf16 v[4:7], a[4:7], a[52:55], v[4:7]\n"
    "buffer_load_dword v242, s[52:55], s89 offen offset:256 sc1 lds\n"
    "v_mfma_f32_16x16x32_bf16 v[8:11], a[0:3], a[56:59], v[8:11]\n"
    "s_add_u32 s62, s62, 1\n"
    "v_mfma_f32_16x16x32_bf16 v[8:11], a[4:7], a[60:63], v[8:11]\n"
    "s_cmp_eq_u32 s62, 12\n"
    "s_cselect_b32 s62, 0, s62\n"
    "v_mfma_f32_16x16x32_bf16 v[12:15], a[0:3], a[64:67], v[12:15]\n"
    "s_add_u32 s90, s62, s92\n"
    "s_bfe_u32 s75, s90, 0x30001\n"
    "v_mfma_f32_16x16x32_bf16 v[12:15], a[4:7], a[68:71], v[12:15]\n"
    "s_mul_i32 s89, s75, 36864\n"
    "s_and_b32 s75, s90, 1\n"
    "v_mfma_f32_16x16x32_bf16 v[80:83], a[0:3], a[72:75], v[80:83]\n"
    "s_lshl_b32 s75, s75, 11\n"
    "s_add_u32 s89, s89, s75\n"
    "v_mfma_f32_16x16x32_bf16 v[80:83], a[4:7], a[76:79], v[80:83]\n"
    "s_waitcnt lgkmcnt(0)\n"
    "s_add_u32 m0, s85, 143360\n"
    "v_mfma_f32_16x16x32_bf16 v[16:19], a[8:11], a[40:43], v[16:19]\n"
    "buffer_load_dwordx4 v236, s[36:39], s58 offen lds\n"
    "v_mfma_f32_16x16x32_bf16 v[16:19], a[12:15], a[44:47], v[16:19]\n"
    "ds_read_b64_tr_b16 a[16:17], v103\n"
    "ds_read_b64_tr_b16 a[18:19], v103 offset:5120\n"
    "v_mfma_f32_16x16x32_bf16 v[20:23], a[8:11], a[48:51], v[20:23]\n"
    "ds_read_b64_tr_b16 a[20:21], v103 offset:10240\n"
    "ds_read_b64_tr_b16 a[22:23], v103 offset:15360\n"
    "v_mfma_f32_16x16x32_bf16 v[20:23], a[12:15], a[52:55], v[20:23]\n"
    "buffer_load_dwordx4 v237, s[36:39], s58 offen offset:1024 lds\n"
    "v_mfma_f32_16x16x32_bf16 v[24:27], a[8:11], a[56:59], v[24:27]\n"
    "v_mfma_f32_16x16x32_bf16 v[24:27], a[12:15], a[60:63], v[24:27]\n"
    "buffer_load_dwordx4 v238, s[36:39], s58 offen offset:2048 lds\n"
    "v_mfma_f32_16x16x32_bf16 v[28:31], a[8:11], a[64:67], v[28:31]\n"
    "v_mfma_f32_16x16x32_bf16 v[28:31], a[12:15], a[68:71], v[28:31]\n"
    "v_mfma_f32_16x16x32_bf16 v[84:87], a[8:11], a[72:75], v[84:87]\n"
    "v_mfma_f32_16x16x32_bf16 v[84:87], a[12:15], a[76:79], v[84:87]\n"
    "s_waitcnt lgkmcnt(0)\n"
    "ds_read_b64_tr_b16 a[24:25], v104\n"
    "v_mfma_f32_16x16x32_bf16 v[32:35], a[16:19], a[40:43], v[32:35]\n"
    "ds_read_b64_tr_b16 a[26:27], v104 offset:5120\n"
    "ds_read_b64_tr_b16 a[28:29], v104 offset:10240\n"
    "v_mfma_f32_16x16x32_bf16 v[32:35], a[20:23], a[44:47], v[32:35]\n"
    "buffer_load_dwordx4 v239, s[36:39], s58 offen offset:3072 lds\n"
    "v_mfma_f32_16x16x32_bf16 v[36:39], a[16:19], a[48:51], v[36:39]\n"
    "s_add_u32 m0, s85, 147456\n"
    "ds_read_b64_tr_b16 a[30:31], v104 offset:15360\n"
    "v_mfma_f32_16x16x32_bf16 v[36:39], a[20:23], a[52:55], v[36:39]\n"
    "buffer_load_dwordx4 v240, s[36:39], s58 offen lds\n"
    "v_mfma_f32_16x16x32_bf16 v[40:43], a[16:19], a[56:59], v[40:43]\n"
    "s_add_u32 s91, s91, 1\n"
    "v_mfma_f32_16x16x32_bf16 v[40:43], a[20:23], a[60:63], v[40:43]\n"
    "s_cmp_eq_u32 s91, 12\n"
    "s_cselect_b32 s91, 0, s91\n"
    "v_mfma_f32_16x16x32_bf16 v[44:47], a[16:19], a[64:67], v[44:47]\n"
    "s_add_u32 s58, s91, s92\n"
    "s_and_b32 s58, s58, 15\n"
    "v_mfma_f32_16x16x32_bf16 v[44:47], a[20:23], a[68:71], v[44:47]\n"
    "s_mul_i32 s58, s58, 81920\n"
    "s_add_u32 s58, s58, s88\n"
    "v_mfma_f32_16x16x32_bf16 v[88:91], a[16:19], a[72:75], v[88:91]\n"
    "s_waitcnt vmcnt(11)\n"
    "s_barrier\n"
    "v_mfma_f32_16x16x32_bf16 v[88:91], a[20:23], a[76:79], v[88:91]\n"
    "s_waitcnt lgkmcnt(0)\n"
    "ds_read_b64_tr_b16 a[32:33], v105\n"
    "v_mfma_f32_16x16x32_bf16 v[48:51], a[24:27], a[40:43], v[48:51]\n"
    "ds_read_b64_tr_b16 a[34:35], v105 offset:5120\n"
    "ds_read_b64_tr_b16 a[36:37], v105 offset:10240\n"
    "v_mfma_f32_16x16x32_bf16 v[48:51], a[28:31], a[44:47], v[48:51]\n"
    "ds_read_b64_tr_b16 a[38:39], v105 offset:15360\n"
    "ds_read_b128 a[80:83], v116 offset:40960\n"
    "v_mfma_f32_16x16x32_bf16 v[52:55], a[24:27], a[48:51], v[52:55]\n"
    "s_add_u32 m0, s86, 28672\n"
    "ds_read_b128 a[84:87], v116 offset:41984\n"
    "v_mfma_f32_16x16x32_bf16 v[52:55], a[28:31], a[52:55], v[52:55]\n"
    "buffer_load_dwordx4 v241, s[52:55], s89 offen sc1 lds\n"
    "v_mfma_f32_16x16x32_bf16 v[56:59], a[24:27], a[56:59], v[56:59]\n"
    "ds_read_b128 a[88:91], v117 offset:40960\n"
    "ds_read_b128 a[92:95], v117 offset:41984\n"
    "v_mfma_f32_16x16x32_bf16 v[56:59], a[28:31], a[60:63], v[56:59]\n"
    "ds_read_b128 a[96:99], v118 offset:40960\n"
    "ds_read_b128 a[100:103], v118 offset:41984\n"
    "v_mfma_f32_16x16x32_bf16 v[60:63], a[24:27], a[64:67], v[60:63]\n"
    "buffer_load_dwordx4 v241, s[52:55], s89 offen offset:1024 sc1 lds\n"
    "v_mfma_f32_16x16x32_bf16 v[60:63], a[28:31], a[68:71], v[60:63]\n"
    "s_waitcnt lgkmcnt(6)\n"
    "s_add_u32 s90, s89, 2048\n"
    "v_mfma_f32_16x16x32_bf16 v[64:67], a[32:35], a[40:43], v[64:67]\n"
    "ds_read_b128 a[104:107], v119 offset:40960\n"
    "ds_read_b64_tr_b16 a[0:1], v101 offset:20480\n"
    "v_mfma_f32_16x16x32_bf16 v[64:67], a[36:39], a[44:47], v[64:67]\n"
    "ds_read_b64_tr_b16 a[2:3], v101 offset:25600\n"
    "ds_read_b128 a[108:111], v119 offset:41984\n"
    "v_mfma_f32_16x16x32_bf16 v[68:71], a[32:35], a[48:51], v[68:71]\n"
    "ds_read_b64_tr_b16 a[4:5], v101 offset:30720\n"
    "ds_read_b64_tr_b16 a[6:7], v101 offset:35840\n"
    "v_mfma_f32_16x16x32_bf16 v[68:71], a[36:39], a[52:55], v[68:71]\n"
    "buffer_load_dwordx4 v241, s[52:55], s90 offen offset:2048 sc1 lds\n"
    "v_mfma_f32_16x16x32_bf16 v[72:75], a[32:35], a[56:59], v[72:75]\n"
    "ds_read_b128 a[112:115], v120 offset:40960\n"
    "ds_read_b128 a[116:119], v120 offset:41984\n"
    "v_mfma_f32_16x16x32_bf16 v[72:75], a[36:39], a[60:63], v[72:75]\n"
    "buffer_load_dwordx4 v241, s[52:55], s90 offen offset:3072 sc1 lds\n"
    "v_mfma_f32_16x16x32_bf16 v[76:79], a[32:35], a[64:67], v[76:79]\n"
    "s_add_u32 m0, s87, 28672\n"
    "v_mfma_f32_16x16x32_bf16 v[76:79], a[36:39], a[68:71], v[76:79]\n"
    "s_waitcnt lgkmcnt(0)\n"
    "ds_read_b64_tr_b16 a[8:9], v102 offset:20480\n"
    "; ---- C7\n"
    "v_mfma_f32_16x16x32_bf16 v[0:3], a[0:3], a[80:83], v[0:3]\n"
    "ds_read_b64_tr_b16 a[10:11], v102 offset:25600\n"
    "ds_read_b64_tr_b16 a[12:13], v102 offset:30720\n"
    "v_mfma_f32_16x16x32_bf16 v[0:3], a[4:7], a[84:87], v[0:3]\n"
    "buffer_load_dword v242, s[52:55], s89 offen sc1 lds\n"
    "v_mfma_f32_16x16x32_bf16 v[4:7], a[0:3], a[88:91], v[4:7]\n"
    "ds_read_b64_tr_b16 a[14:15], v102 offset:35840\n"
    "v_mfma_f32_16x16x32_bf16 v[4:7], a[4:7], a[92:95], v[4:7]\n"
    "buffer_load_dword v242, s[52:55], s89 offen offset:256 sc1 lds\n"
    "v_mfma_f32_16x16x32_bf16 v[8:11], a[0:3], a[96:99], v[8:11]\n"
    "s_add_u32 s62, s62, 1\n"
    "v_mfma_f32_16x16x32_bf16 v[8:11], a[4:7], a[100:103], v[8:11]\n"
    "s_cmp_eq_u32 s62, 12\n"
    "s_cselect_b32 s62, 0, s62\n"
    "v_mfma_f32_16x16x32_bf16 v[12:15], a[0:3], a[104:107], v[12:15]\n"
    "s_add_u32 s90, s62, s92\n"
    "s_bfe_u32 s75, s90, 0x30001\n"
    "v_mfma_f32_16x16x32_bf16 v[12:15], a[4:7], a[108:111], v[12:15]\n"
    "s_mul_i32 s89, s75, 36864\n"
    "s_and_b32 s75, s90, 1\n"
    "v_mfma_f32_16x16x32_bf16 v[80:83], a[0:3], a[112:115], v[80:83]\n"
    "s_lshl_b32 s75, s75, 11\n"
    "s_add_u32 s89, s89, s75\n"
    "v_mfma_f32_16x16x32_bf16 v[80:83], a[4:7], a[116:119], v[80:83]\n"
    "s_waitcnt lgkmcnt(0)\n"
    "s_add_u32 m0, s85, 8192\n"
    "v_mfma_f32_16x16x32_bf16 v[16:19], a[8:11], a[80:83], v[16:19]\n"
    "buffer_load_dwordx4 v236, s[36:39], s58 offen lds\n"
    "v_mfma_f32_16x16x32_bf16 v[16:19], a[12:15], a[84:87], v[16:19]\n"
    "ds_read_b64_tr_b16 a[16:17], v103 offset:20480\n"
    "ds_read_b64_tr_b16 a[18:19], v103 offset:25600\n"
    "v_mfma_f32_16x16x32_bf16 v[20:23], a[8:11], a[88:91], v[20:23]\n"
    "ds_read_b64_tr_b16 a[20:21], v103 offset:30720\n"
    "ds_read_b64_tr_b16 a[22:23], v103 offset:35840\n"
    "v_mfma_f32_16x16x32_bf16 v[20:23], a[12:15], a[92:95], v[20:23]\n"
    "buffer_load_dwordx4 v237, s[36:39], s58 offen offset:1024 lds\n"
    "v_mfma_f32_16x16x32_bf16 v[24:27], a[8:11], a[96:99], v[24:27]\n"
    "v_mfma_f32_16x16x32_bf16 v[24:27], a[12:15], a[100:103], v[24:27]\n"
    "buffer_load_dwordx4 v238, s[36:39], s58 offen offset:2048 lds\n"
    "v_mfma_f32_16x16x32_bf16 v[28:31], a[8:11], a[104:107], v[28:31]\n"
    "v_mfma_f32_16x16x32_bf16 v[28:31], a[12:15], a[108:111], v[28:31]\n"
    "v_mfma_f32_16x16x32_bf16 v[84:87], a[8:11], a[112:115], v[84:87]\n"
    "v_mfma_f32_16x16x32_bf16 v[84:87], a[12:15], a[116:119], v[84:87]\n"
    "s_waitcnt lgkmcnt(0)\n"
    "ds_read_b64_tr_b16 a[24:25], v104 offset:20480\n"
    "v_mfma_f32_16x16x32_bf16 v[32:35], a[16:19], a[80:83], v[32:35]\n"
    "ds_read_b64_tr_b16 a[26:27], v104 offset:25600\n"
    "ds_read_b64_tr_b16 a[28:29], v104 offset:30720\n"
    "v_mfma_f32_16x16x32_bf16 v[32:35], a[20:23], a[84:87], v[32:35]\n"
    "buffer_load_dwordx4 v239, s[36:39], s58 offen offset:3072 lds\n"
    "v_mfma_f32_16x16x32_bf16 v[36:39], a[16:19], a[88:91], v[36:39]\n"
    "s_add_u32 m0, s85, 12288\n"
    "ds_read_b64_tr_b16 a[30:31], v104 offset:35840\n"
    "v_mfma_f32_16x16x32_bf16 v[36:39], a[20:23], a[92:95], v[36:39]\n"
    "buffer_load_dwordx4 v240, s[36:39], s58 offen lds\n"
    "v_mfma_f32_16x16x32_bf16 v[40:43], a[16:19], a[96:99], v[40:43]\n"
    "s_add_u32 s91, s91, 1\n"
    "v_mfma_f32_16x16x32_bf16 v[40:43], a[20:23], a[100:103], v[40:43]\n"
    "s_cmp_eq_u32 s91, 12\n"
    "s_cselect_b32 s91, 0, s91\n"
    "v_mfma_f32_16x16x32_bf16 v[44:47], a[16:19], a[104:107], v[44:47]\n"
    "s_add_u32 s58, s91, s92\n"
    "s_and_b32 s58, s58, 15\n"
    "v_mfma_f32_16x16x32_bf16 v[44:47], a[20:23], a[108:111], v[44:47]\n"
    "s_mul_i32 s58, s58, 81920\n"
    "s_add_u32 s58, s58, s88\n"
    "v_mfma_f32_16x16x32_bf16 v[88:91], a[16:19], a[112:115], v[88:91]\n"
    "s_waitcnt vmcnt(11)\n"
    "s_barrier\n"
    "v_mfma_f32_16x16x32_bf16 v[88:91], a[20:23], a[116:119], v[88:91]\n"
    "s_waitcnt lgkmcnt(0)\n"
    "ds_read_b64_tr_b16 a[32:33], v105 offset:20480\n"
    "v_mfma_f32_16x16x32_bf16 v[48:51], a[24:27], a[80:83], v[48:51]\n"
    "ds_read_b64_tr_b16 a[34:35], v105 offset:25600\n"
    "ds_read_b64_tr_b16 a[36:37], v105 offset:30720\n"
    "v_mfma_f32_16x16x32_bf16 v[48:51], a[28:31], a[84:87], v[48:51]\n"
    "ds_read_b64_tr_b16 a[38:39], v105 offset:35840\n"
    "ds_read_b128 a[40:43], v116 offset:59392\n"
    "v_mfma_f32_16x16x32_bf16 v[52:55], a[24:27], a[88:91], v[52:55]\n"
    "s_add_u32 m0, s86, 47104\n"
    "ds_read_b128 a[44:47], v116 offset:60416\n"
    "v_mfma_f32_16x16x32_bf16 v[52:55], a[28:31], a[92:95], v[52:55]\n"
    "buffer_load_dwordx4 v241, s[52:55], s89 offen sc1 lds\n"
    "v_mfma_f32_16x16x32_bf16 v[56:59], a[24:27], a[96:99], v[56:59]\n"
    "ds_read_b128 a[48:51], v117 offset:59392\n"
    "ds_read_b128 a[52:55], v117 offset:60416\n"
    "v_mfma_f32_16x16x32_bf16 v[56:59], a[28:31], a[100:103], v[56:59]\n"
    "ds_read_b128 a[56:59], v118 offset:59392\n"
    "ds_read_b128 a[60:63], v118 offset:60416\n"
    "v_mfma_f32_16x16x32_bf16 v[60:63], a[24:27], a[104:107], v[60:63]\n"
    "buffer_load_dwordx4 v241, s[52:55], s89 offen offset:1024 sc1 lds\n"
    "v_mfma_f32_16x16x32_bf16 v[60:63], a[28:31], a[108:111], v[60:63]\n"
    "s_waitcnt lgkmcnt(6)\n"
    "s_add_u32 s90, s89, 2048\n"
    "v_mfma_f32_16x16x32_bf16 v[64:67], a[32:35], a[80:83], v[64:67]\n"
    "ds_read_b128 a[64:67], v119 offset:59392\n"
    "ds_read_b64_tr_b16 a[0:1], v106 offset:12288\n"
    "v_mfma_f32_16x16x32_bf16 v[64:67], a[36:39], a[84:87], v[64:67]\n"
    "ds_read_b64_tr_b16 a[2:3], v106 offset:17408\n"
    "ds_read_b128 a[68:71], v119 offset:60416\n"
    "v_mfma_f32_16x16x32_bf16 v[68:71], a[32:35], a[88:91], v[68:71]\n"
    "ds_read_b64_tr_b16 a[4:5], v106 offset:22528\n"
    "ds_read_b64_tr_b16 a[6:7], v106 offset:27648\n"
    "v_mfma_f32_16x16x32_bf16 v[68:71], a[36:39], a[92:95], v[68:71]\n"
    "buffer_load_dwordx4 v241, s[52:55], s90 offen offset:2048 sc1 lds\n"
    "v_mfma_f32_16x16x32_bf16 v[72:75], a[32:35], a[96:99], v[72:75]\n"
    "ds_read_b128 a[72:75], v120 offset:59392\n"
    "ds_read_b128 a[76:79], v120 offset:60416\n"
    "v_mfma_f32_16x16x32_bf16 v[72:75], a[36:39], a[100:103], v[72:75]\n"
    "buffer_load_dwordx4 v241, s[52:55], s90 offen offset:3072 sc1 lds\n"
    "v_mfma_f32_16x16x32_bf16 v[76:79], a[32:35], a[104:107], v[76:79]\n"
    "s_add_u32 m0, s87, 47104\n"
    "v_mfma_f32_16x16x32_bf16 v[76:79], a[36:39], a[108:111], v[76:79]\n"
    "s_waitcnt lgkmcnt(0)\n"
    "ds_read_b64_tr_b16 a[8:9], v107 offset:12288\n"
    "; ---- C8\n"
    "v_mfma_f32_16x16x32_bf16 v[0:3], a[0:3], a[40:43], v[0:3]\n"
    "ds_read_b64_tr_b16 a[10:11], v107 offset:17408\n"
    "ds_read_b64_tr_b16 a[12:13], v107 offset:22528\n"
    "v_mfma_f32_16x16x32_bf16 v[0:3], a[4:7], a[44:47], v[0:3]\n"
    "buffer_load_dword v242, s[52:55], s89 offen sc1 lds\n"
    "v_mfma_f32_16x16x32_bf16 v[4:7], a[0:3], a[48:51], v[4:7]\n"
    "ds_read_b64_tr_b16 a[14:15], v107 offset:27648\n"
    "v_mfma_f32_16x16x32_bf16 v[4:7], a[4:7], a[52:55], v[4:7]\n"
    "buffer_load_dword v242, s[52:55], s89 offen offset:256 sc1 lds\n"
    "v_mfma_f32_16x16x32_bf16 v[8:11], a[0:3], a[56:59], v[8:11]\n"
    "s_add_u32 s62, s62, 1\n"
    "v_mfma_f32_16x16x32_bf16 v[8:11], a[4:7], a[60:63], v[8:11]\n"
    "s_cmp_eq_u32 s62, 12\n"
    "s_cselect_b32 s62, 0, s62\n"
    "v_mfma_f32_16x16x32_bf16 v[12:15], a[0:3], a[64:67], v[12:15]\n"
    "s_add_u32 s90, s62, s92\n"
    "s_bfe_u32 s75, s90, 0x30001\n"
    "v_mfma_f32_16x16x32_bf16 v[12:15], a[4:7], a[68:71], v[12:15]\n"
    "s_mul_i32 s89, s75, 36864\n"
    "s_and_b32 s75, s90, 1\n"
    "v_mfma_f32_16x16x32_bf16 v[80:83], a[0:3], a[72:75], v[80:83]\n"
    "s_lshl_b32 s75, s75, 11\n"
    "s_add_u32 s89, s89, s75\n"
    "v_mfma_f32_16x16x32_bf16 v[80:83], a[4:7], a[76:79], v[80:83]\n"
    "s_waitcnt lgkmcnt(0)\n"
    "s_add_u32 m0, s85, 65536\n"
    "v_mfma_f32_16x16x32_bf16 v[16:19], a[8:11], a[40:43], v[16:19]\n"
    "buffer_load_dwordx4 v236, s[36:39], s58 offen lds\n"
    "v_mfma_f32_16x16x32_bf16 v[16:19], a[12:15], a[44:47], v[16:19]\n"
    "ds_read_b64_tr_b16 a[16:17], v108 offset:12288\n"
    "ds_read_b64_tr_b16 a[18:19], v108 offset:17408\n"
    "v_mfma_f32_16x16x32_bf16 v[20:23], a[8:11], a[48:51], v[20:23]\n"
    "ds_read_b64_tr_b16 a[20:21], v108 offset:22528\n"
    "ds_read_b64_tr_b16 a[22:23], v108 offset:27648\n"
    "v_mfma_f32_16x16x32_bf16 v[20:23], a[12:15], a[52:55], v[20:23]\n"
    "buffer_load_dwordx4 v237, s[36:39], s58 offen offset:1024 lds\n"
    "v_mfma_f32_16x16x32_bf16 v[24:27], a[8:11], a[56:59], v[24:27]\n"
    "v_mfma_f32_16x16x32_bf16 v[24:27], a[12:15], a[60:63], v[24:27]\n"
    "buffer_load_dwordx4 v238, s[36:39], s58 offen offset:2048 lds\n"
    "v_mfma_f32_16x16x32_bf16 v[28:31], a[8:11], a[64:67], v[28:31]\n"
    "v_mfma_f32_16x16x32_bf16 v[28:31], a[12:15], a[68:71], v[28:31]\n"
    "v_mfma_f32_16x16x32_bf16 v[84:87], a[8:11], a[72:75], v[84:87]\n"
    "v_mfma_f32_16x16x32_bf16 v[84:87], a[12:15], a[76:79], v[84:87]\n"
    "s_waitcnt lgkmcnt(0)\n"
    "ds_read_b64_tr_b16 a[24:25], v109 offset:12288\n"
    "v_mfma_f32_16x16x32_bf16 v[32:35], a[16:19], a[40:43], v[32:35]\n"
    "ds_read_b64_tr_b16 a[26:27], v109 offset:17408\n"
    "ds_read_b64_tr_b16 a[28:29], v109 offset:22528\n"
    "v_mfma_f32_16x16x32_bf16 v[32:35], a[20:23], a[44:47], v[32:35]\n"
    "buffer_load_dwordx4 v239, s[36:39], s58 offen offset:3072 lds\n"
    "v_mfma_f32_16x16x32_bf16 v[36:39], a[16:19], a[48:51], v[36:39]\n"
    "s_add_u32 m0, s85, 69632\n"
    "ds_read_b64_tr_b16 a[30:31], v109 offset:27648\n"
    "v_mfma_f32_16x16x32_bf16 v[36:39], a[20:23], a[52:55], v[36:39]\n"
    "buffer_load_dwordx4 v240, s[36:39], s58 offen lds\n"
    "v_mfma_f32_16x16x32_bf16 v[40:43], a[16:19], a[56:59], v[40:43]\n"
    "s_add_u32 s91, s91, 1\n"
    "v_mfma_f32_16x16x32_bf16 v[40:43], a[20:23], a[60:63], v[40:43]\n"
    "s_cmp_eq_u32 s91, 12\n"
    "s_cselect_b32 s91, 0, s91\n"
    "v_mfma_f32_16x16x32_bf16 v[44:47], a[16:19], a[64:67], v[44:47]\n"
    "s_add_u32 s58, s91, s92\n"
    "s_and_b32 s58, s58, 15\n"
    "v_mfma_f32_16x16x32_bf16 v[44:47], a[20:23], a[68:71], v[44:47]\n"
    "s_mul_i32 s58, s58, 81920\n"
    "s_add_u32 s58, s58, s88\n"
    "v_mfma_f32_16x16x32_bf16 v[88:91], a[16:19], a[72:75], v[88:91]\n"
    "s_waitcnt vmcnt(11)\n"
    "s_barrier\n"
    "v_mfma_f32_16x16x32_bf16 v[88:91], a[20:23], a[76:79], v[88:91]\n"
    "s_waitcnt lgkmcnt(0)\n"
    "ds_read_b64_tr_b16 a[32:33], v110 offset:12288\n"
    "v_mfma_f32_16x16x32_bf16 v[48:51], a[24:27], a[40:43], v[48:51]\n"
    "ds_read_b64_tr_b16 a[34:35], v110 offset:17408\n"
    "ds_read_b64_tr_b16 a[36:37], v110 offset:22528\n"
    "v_mfma_f32_16x16x32_bf16 v[48:51], a[28:31], a[44:47], v[48:51]\n"
    "ds_read_b64_tr_b16 a[38:39], v110 offset:27648\n"
    "ds_read_b128 a[80:83], v111 offset:28672\n"
    "v_mfma_f32_16x16x32_bf16 v[52:55], a[24:27], a[48:51], v[52:55]\n"
    "s_add_u32 m0, s86, 106496\n"
    "ds_read_b128 a[84:87], v111 offset:29696\n"
    "v_mfma_f32_16x16x32_bf16 v[52:55], a[28:31], a[52:55], v[52:55]\n"
    "buffer_load_dwordx4 v241, s[52:55], s89 offen sc1 lds\n"
    "v_mfma_f32_16x16x32_bf16 v[56:59], a[24:27], a[56:59], v[56:59]\n"
    "ds_read_b128 a[88:91], v112 offset:28672\n"
    "ds_read_b128 a[92:95], v112 offset:29696\n"
    "v_mfma_f32_16x16x32_bf16 v[56:59], a[28:31], a[60:63], v[56:59]\n"
    "ds_read_b128 a[96:99], v113 offset:28672\n"
    "ds_read_b128 a[100:103], v113 offset:29696\n"
    "v_mfma_f32_16x16x32_bf16 v[60:63], a[24:27], a[64:67], v[60:63]\n"
    "buffer_load_dwordx4 v241, s[52:55], s89 offen offset:1024 sc1 lds\n"
    "v_mfma_f32_16x16x32_bf16 v[60:63], a[28:31], a[68:71], v[60:63]\n"
    "s_waitcnt lgkmcnt(6)\n"
    "s_add_u32 s90, s89, 2048\n"
    "v_mfma_f32_16x16x32_bf16 v[64:67], a[32:35], a[40:43], v[64:67]\n"
    "ds_read_b128 a[104:107], v114 offset:28672\n"
    "ds_read_b64_tr_b16 a[0:1], v96 offset:8192\n"
    "v_mfma_f32_16x16x32_bf16 v[64:67], a[36:39], a[44:47], v[64:67]\n"
    "ds_read_b64_tr_b16 a[2:3], v96 offset:13312\n"
    "ds_read_b128 a[108:111], v114 offset:29696\n"
    "v_mfma_f32_16x16x32_bf16 v[68:71], a[32:35], a[48:51], v[68:71]\n"
    "ds_read_b64_tr_b16 a[4:5], v96 offset:18432\n"
    "ds_read_b64_tr_b16 a[6:7], v96 offset:23552\n"
    "v_mfma_f32_16x16x32_bf16 v[68:71], a[36:39], a[52:55], v[68:71]\n"
    "buffer_load_dwordx4 v241, s[52:55], s90 offen offset:2048 sc1 lds\n"
    "v_mfma_f32_16x16x32_bf16 v[72:75], a[32:35], a[56:59], v[72:75]\n"
    "ds_read_b128 a[112:115], v115 offset:28672\n"
    "ds_read_b128 a[116:119], v115 offset:29696\n"
    "v_mfma_f32_16x16x32_bf16 v[72:75], a[36:39], a[60:63], v[72:75]\n"
    "buffer_load_dwordx4 v241, s[52:55], s90 offen offset:3072 sc1 lds\n"
    "v_mfma_f32_16x16x32_bf16 v[76:79], a[32:35], a[64:67], v[76:79]\n"
    "s_add_u32 m0, s87, 106496\n"
    "v_mfma_f32_16x16x32_bf16 v[76:79], a[36:39], a[68:71], v[76:79]\n"
    "s_waitcnt lgkmcnt(0)\n"
    "ds_read_b64_tr_b16 a[8:9], v97 offset:8192\n"
    "s_sub_u32 s63, s63, 1\n"
    "s_cmp_lg_u32 s63, 0\n"
    "s_cbranch_scc1 Lgloop\n"
    "; ---- C13\n"
    "v_mfma_f32_16x16x32_bf16 v[0:3], a[0:3], a[80:83], v[0:3]\n"
    "ds_read_b64_tr_b16 a[10:11], v97 offset:13312\n"
    "ds_read_b64_tr_b16 a[12:13], v97 offset:18432\n"
    "v_mfma_f32_16x16x32_bf16 v[0:3], a[4:7], a[84:87], v[0:3]\n"
    "buffer_load_dword v242, s[52:55], s89 offen sc1 lds\n"
    "v_mfma_f32_16x16x32_bf16 v[4:7], a[0:3], a[88:91], v[4:7]\n"
    "ds_read_b64_tr_b16 a[14:15], v97 offset:23552\n"
    "v_mfma_f32_16x16x32_bf16 v[4:7], a[4:7], a[92:95], v[4:7]\n"
    "buffer_load_dword v242, s[52:55], s89 offen offset:256 sc1 lds\n"
    "v_mfma_f32_16x16x32_bf16 v[8:11], a[0:3], a[96:99], v[8:11]\n"
    "s_add_u32 s62, s62, 1\n"
    "v_mfma_f32_16x16x32_bf16 v[8:11], a[4:7], a[100:103], v[8:11]\n"
    "s_cmp_eq_u32 s62, 12\n"
    "s_cselect_b32 s62, 0, s62\n"
    "v_mfma_f32_16x16x32_bf16 v[12:15], a[0:3], a[104:107], v[12:15]\n"
    "s_add_u32 s90, s62, s92\n"
    "s_bfe_u32 s75, s90, 0x30001\n"
    "v_mfma_f32_16x16x32_bf16 v[12:15], a[4:7], a[108:111], v[12:15]\n"
    "s_mul_i32 s89, s75, 36864\n"
    "s_and_b32 s75, s90, 1\n"
    "v_mfma_f32_16x16x32_bf16 v[80:83], a[0:3], a[112:115], v[80:83]\n"
    "s_lshl_b32 s75, s75, 11\n"
    "s_add_u32 s89, s89, s75\n"
    "v_mfma_f32_16x16x32_bf16 v[80:83], a[4:7], a[116:119], v[80:83]\n"
    "s_waitcnt lgkmcnt(0)\n"
    "s_add_u32 m0, s85, 86016\n"
    "v_mfma_f32_16x16x32_bf16 v[16:19], a[8:11], a[80:83], v[16:19]\n"
    "buffer_load_dwordx4 v236, s[36:39], s58 offen lds\n"
    "v_mfma_f32_16x16x32_bf16 v[16:19], a[12:15], a[84:87], v[16:19]\n"
    "ds_read_b64_tr_b16 a[16:17], v98 offset:8192\n"
    "ds_read_b64_tr_b16 a[18:19], v98 offset:13312\n"
    "v_mfma_f32_16x16x32_bf16 v[20:23], a[8:11], a[88:91], v[20:23]\n"
    "ds_read_b64_tr_b16 a[20:21], v98 offset:18432\n"
    "ds_read_b64_tr_b16 a[22:23], v98 offset:23552\n"
    "v_mfma_f32_16x16x32_bf16 v[20:23], a[12:15], a[92:95], v[20:23]\n"
    "buffer_load_dwordx4 v237, s[36:39], s58 offen offset:1024 lds\n"
    "v_mfma_f32_16x16x32_bf16 v[24:27], a[8:11], a[96:99], v[24:27]\n"
    "v_mfma_f32_16x16x32_bf16 v[24:27], a[12:15], a[100:103], v[24:27]\n"
    "buffer_load_dwordx4 v238, s[36:39], s58 offen offset:2048 lds\n"
    "v_mfma_f32_16x16x32_bf16 v[28:31], a[8:11], a[104:107], v[28:31]\n"
    "v_mfma_f32_16x16x32_bf16 v[28:31], a[12:15], a[108:111], v[28:31]\n"
    "v_mfma_f32_16x16x32_bf16 v[84:87], a[8:11], a[112:115], v[84:87]\n"
    "v_mfma_f32_16x16x32_bf16 v[84:87], a[12:15], a[116:119], v[84:87]\n"
    "s_waitcnt lgkmcnt(0)\n"
    "ds_read_b64_tr_b16 a[24:25], v99 offset:8192\n"
    "v_mfma_f32_16x16x32_bf16 v[32:35], a[16:19], a[80:83], v[32:35]\n"
    "ds_read_b64_tr_b16 a[26:27], v99 offset:13312\n"
    "ds_read_b64_tr_b16 a[28:29], v99 offset:18432\n"
    "v_mfma_f32_16x16x32_bf16 v[32:35], a[20:23], a[84:87], v[32:35]\n"
    "buffer_load_dwordx4 v239, s[36:39], s58 offen offset:3072 lds\n"
    "v_mfma_f32_16x16x32_bf16 v[36:39], a[16:19], a[88:91], v[36:39]\n"
    "s_add_u32 m0, s85, 90112\n"
    "ds_read_b64_tr_b16 a[30:31], v99 offset:23552\n"
    "v_mfma_f32_16x16x32_bf16 v[36:39], a[20:23], a[92:95], v[36:39]\n"
    "buffer_load_dwordx4 v240, s[36:39], s58 offen lds\n"
    "v_mfma_f32_16x16x32_bf16 v[40:43], a[16:19], a[96:99], v[40:43]\n"
    "s_add_u32 s91, s91, 1\n"
    "v_mfma_f32_16x16x32_bf16 v[40:43], a[20:23], a[100:103], v[40:43]\n"
    "s_cmp_eq_u32 s91, 12\n"
    "s_cselect_b32 s91, 0, s91\n"
    "v_mfma_f32_16x16x32_bf16 v[44:47], a[16:19], a[104:107], v[44:47]\n"
    "s_add_u32 s58, s91, s92\n"
    "s_and_b32 s58, s58, 15\n"
    "v_mfma_f32_16x16x32_bf16 v[44:47], a[20:23], a[108:111], v[44:47]\n"
    "s_mul_i32 s58, s58, 81920\n"
    "s_add_u32 s58, s58, s88\n"
    "v_mfma_f32_16x16x32_bf16 v[88:91], a[16:19], a[112:115], v[88:91]\n"
    "s_waitcnt vmcnt(11)\n"
    "s_barrier\n"
    "v_mfma_f32_16x16x32_bf16 v[88:91], a[20:23], a[116:119], v[88:91]\n"
    "s_waitcnt lgkmcnt(0)\n"
    "ds_read_b64_tr_b16 a[32:33], v100 offset:8192\n"
    "v_mfma_f32_16x16x32_bf16 v[48:51], a[24:27], a[80:83], v[48:51]\n"
    "ds_read_b64_tr_b16 a[34:35], v100 offset:13312\n"
    "ds_read_b64_tr_b16 a[36:37], v100 offset:18432\n"
    "v_mfma_f32_16x16x32_bf16 v[48:51], a[28:31], a[84:87], v[48:51]\n"
    "ds_read_b64_tr_b16 a[38:39], v100 offset:23552\n"
    "ds_read_b128 a[40:43], v111 offset:47104\n"
    "v_mfma_f32_16x16x32_bf16 v[52:55], a[24:27], a[88:91], v[52:55]\n"
    "ds_read_b128 a[44:47], v111 offset:48128\n"
    "ds_read_b128 a[48:51], v112 offset:47104\n"
    "v_mfma_f32_16x16x32_bf16 v[52:55], a[28:31], a[92:95], v[52:55]\n"
    "ds_read_b128 a[52:55], v112 offset:48128\n"
    "ds_read_b128 a[56:59], v113 offset:47104\n"
    "v_mfma_f32_16x16x32_bf16 v[56:59], a[24:27], a[96:99], v[56:59]\n"
    "ds_read_b128 a[60:63], v113 offset:48128\n"
    "ds_read_b128 a[64:67], v114 offset:47104\n"
    "v_mfma_f32_16x16x32_bf16 v[56:59], a[28:31], a[100:103], v[56:59]\n"
    "ds_read_b64_tr_b16 a[120:121], v101\n"
    "ds_read_b64_tr_b16 a[122:123], v101 offset:5120\n"
    "v_mfma_f32_16x16x32_bf16 v[60:63], a[24:27], a[104:107], v[60:63]\n"
    "ds_read_b128 a[68:71], v114 offset:48128\n"
    "ds_read_b64_tr_b16 a[124:125], v101 offset:10240\n"
    "v_mfma_f32_16x16x32_bf16 v[60:63], a[28:31], a[108:111], v[60:63]\n"
    "s_waitcnt lgkmcnt(11)\n"
    "ds_read_b64_tr_b16 a[126:127], v101 offset:15360\n"
    "v_mfma_f32_16x16x32_bf16 v[64:67], a[32:35], a[80:83], v[64:67]\n"
    "ds_read_b128 a[72:75], v115 offset:47104\n"
    "ds_read_b128 a[76:79], v115 offset:48128\n"
    "v_mfma_f32_16x16x32_bf16 v[64:67], a[36:39], a[84:87], v[64:67]\n"
    "v_mfma_f32_16x16x32_bf16 v[68:71], a[32:35], a[88:91], v[68:71]\n"
    "v_mfma_f32_16x16x32_bf16 v[68:71], a[36:39], a[92:95], v[68:71]\n"
    "v_mfma_f32_16x16x32_bf16 v[72:75], a[32:35], a[96:99], v[72:75]\n"
    "v_mfma_f32_16x16x32_bf16 v[72:75], a[36:39], a[100:103], v[72:75]\n"
    "ds_read_b64_tr_b16 a[128:129], v102\n"
    "ds_read_b64_tr_b16 a[130:131], v102 offset:5120\n"
    "v_mfma_f32_16x16x32_bf16 v[76:79], a[32:35], a[104:107], v[76:79]\n"
    "ds_read_b64_tr_b16 a[132:133], v102 offset:10240\n"
    "ds_read_b64_tr_b16 a[134:135], v102 offset:15360\n"
    "v_mfma_f32_16x16x32_bf16 v[76:79], a[36:39], a[108:111], v[76:79]\n"
    "s_waitcnt lgkmcnt(4)\n"
    "; ---- C14\n"
    "v_mfma_f32_16x16x32_bf16 v[4:7], a[120:123], a[48:51], v[4:7]\n"
    "v_mfma_f32_16x16x32_bf16 v[4:7], a[124:127], a[52:55], v[4:7]\n"
    "v_mfma_f32_16x16x32_bf16 v[8:11], a[120:123], a[56:59], v[8:11]\n"
    "v_mfma_f32_16x16x32_bf16 v[8:11], a[124:127], a[60:63], v[8:11]\n"
    "v_mfma_f32_16x16x32_bf16 v[12:15], a[120:123], a[64:67], v[12:15]\n"
    "v_mfma_f32_16x16x32_bf16 v[12:15], a[124:127], a[68:71], v[12:15]\n"
    "ds_read_b64_tr_b16 a[136:137], v103\n"
    "ds_read_b64_tr_b16 a[138:139], v103 offset:5120\n"
    "v_mfma_f32_16x16x32_bf16 v[80:83], a[120:123], a[72:75], v[80:83]\n"
    "ds_read_b64_tr_b16 a[140:141], v103 offset:10240\n"
    "ds_read_b64_tr_b16 a[142:143], v103 offset:15360\n"
    "v_mfma_f32_16x16x32_bf16 v[80:83], a[124:127], a[76:79], v[80:83]\n"
    "s_waitcnt lgkmcnt(4)\n"
    "v_mfma_f32_16x16x32_bf16 v[20:23], a[128:131], a[48:51], v[20:23]\n"
    "v_mfma_f32_16x16x32_bf16 v[20:23], a[132:135], a[52:55], v[20:23]\n"
    "v_mfma_f32_16x16x32_bf16 v[24:27], a[128:131], a[56:59], v[24:27]\n"
    "v_mfma_f32_16x16x32_bf16 v[24:27], a[132:135], a[60:63], v[24:27]\n"
    "v_mfma_f32_16x16x32_bf16 v[28:31], a[128:131], a[64:67], v[28:31]\n"
    "v_mfma_f32_16x16x32_bf16 v[28:31], a[132:135], a[68:71], v[28:31]\n"
    "ds_read_b64_tr_b16 a[144:145], v104\n"
    "ds_read_b64_tr_b16 a[146:147], v104 offset:5120\n"
    "v_mfma_f32_16x16x32_bf16 v[84:87], a[128:131], a[72:75], v[84:87]\n"
    "ds_read_b64_tr_b16 a[148:149], v104 offset:10240\n"
    "ds_read_b64_tr_b16 a[150:151], v104 offset:15360\n"
    "v_mfma_f32_16x16x32_bf16 v[84:87], a[132:135], a[76:79], v[84:87]\n"
    "s_waitcnt lgkmcnt(4)\n"
    "v_mfma_f32_16x16x32_bf16 v[36:39], a[136:139], a[48:51], v[36:39]\n"
    "v_mfma_f32_16x16x32_bf16 v[36:39], a[140:143], a[52:55], v[36:39]\n"
    "v_mfma_f32_16x16x32_bf16 v[40:43], a[136:139], a[56:59], v[40:43]\n"
    "s_waitcnt vmcnt(0)\n"
    "s_barrier\n"
    "v_mfma_f32_16x16x32_bf16 v[40:43], a[140:143], a[60:63], v[40:43]\n"
    "ds_read_b128 a[80:83], v116 offset:40960\n"
    "ds_read_b64_tr_b16 a[152:153], v105\n"
    "v_mfma_f32_16x16x32_bf16 v[44:47], a[136:139], a[64:67], v[44:47]\n"
    "ds_read_b64_tr_b16 a[154:155], v105 offset:5120\n"
    "ds_read_b128 a[84:87], v116 offset:41984\n"
    "v_mfma_f32_16x16x32_bf16 v[44:47], a[140:143], a[68:71], v[44:47]\n"
    "ds_read_b64_tr_b16 a[156:157], v105 offset:10240\n"
    "ds_read_b64_tr_b16 a[158:159], v105 offset:15360\n"
    "v_mfma_f32_16x16x32_bf16 v[88:91], a[136:139], a[72:75], v[88:91]\n"
    "ds_read_b128 a[88:91], v117 offset:40960\n"
    "ds_read_b128 a[92:95], v117 offset:41984\n"
    "v_mfma_f32_16x16x32_bf16 v[88:91], a[140:143], a[76:79], v[88:91]\n"
    "s_waitcnt lgkmcnt(8)\n"
    "ds_read_b128 a[96:99], v118 offset:40960\n"
    "v_mfma_f32_16x16x32_bf16 v[52:55], a[144:147], a[48:51], v[52:55]\n"
    "ds_read_b128 a[100:103], v118 offset:41984\n"
    "ds_read_b128 a[104:107], v119 offset:40960\n"
    "v_mfma_f32_16x16x32_bf16 v[52:55], a[148:151], a[52:55], v[52:55]\n"
    "ds_read_b64_tr_b16 a[0:1], v101 offset:20480\n"
    "ds_read_b64_tr_b16 a[2:3], v101 offset:25600\n"
    "v_mfma_f32_16x16x32_bf16 v[56:59], a[144:147], a[56:59], v[56:59]\n"
    "ds_read_b128 a[108:111], v119 offset:41984\n"
    "ds_read_b64_tr_b16 a[4:5], v101 offset:30720\n"
    "v_mfma_f32_16x16x32_bf16 v[56:59], a[148:151], a[60:63], v[56:59]\n"
    "ds_read_b64_tr_b16 a[6:7], v101 offset:35840\n"
    "ds_read_b128 a[112:115], v120 offset:40960\n"
    "v_mfma_f32_16x16x32_bf16 v[60:63], a[144:147], a[64:67], v[60:63]\n"
    "ds_read_b128 a[116:119], v120 offset:41984\n"
    "v_mfma_f32_16x16x32_bf16 v[60:63], a[148:151], a[68:71], v[60:63]\n"
    "s_waitcnt lgkmcnt(12)\n"
    "v_mfma_f32_16x16x32_bf16 v[68:71], a[152:155], a[48:51], v[68:71]\n"
    "v_mfma_f32_16x16x32_bf16 v[68:71], a[156:159], a[52:55], v[68:71]\n"
    "v_mfma_f32_16x16x32_bf16 v[72:75], a[152:155], a[56:59], v[72:75]\n"
    "v_mfma_f32_16x16x32_bf16 v[72:75], a[156:159], a[60:63], v[72:75]\n"
    "ds_read_b64_tr_b16 a[8:9], v102 offset:20480\n"
    "ds_read_b64_tr_b16 a[10:11], v102 offset:25600\n"
    "v_mfma_f32_16x16x32_bf16 v[76:79], a[152:155], a[64:67], v[76:79]\n"
    "ds_read_b64_tr_b16 a[12:13], v102 offset:30720\n"
    "ds_read_b64_tr_b16 a[14:15], v102 offset:35840\n"
    "v_mfma_f32_16x16x32_bf16 v[76:79], a[156:159], a[68:71], v[76:79]\n"
    "s_waitcnt lgkmcnt(4)\n"
    "; ---- C15\n"
    "v_mfma_f32_16x16x32_bf16 v[4:7], a[0:3], a[88:91], v[4:7]\n"
    "v_mfma_f32_16x16x32_bf16 v[4:7], a[4:7], a[92:95], v[4:7]\n"
    "v_mfma_f32_16x16x32_bf16 v[8:11], a[0:3], a[96:99], v[8:11]\n"
    "v_mfma_f32_16x16x32_bf16 v[8:11], a[4:7], a[100:103], v[8:11]\n"
    "v_mfma_f32_16x16x32_bf16 v[12:15], a[0:3], a[104:107], v[12:15]\n"
    "v_mfma_f32_16x16x32_bf16 v[12:15], a[4:7], a[108:111], v[12:15]\n"
    "ds_read_b64_tr_b16 a[16:17], v103 offset:20480\n"
    "ds_read_b64_tr_b16 a[18:19], v103 offset:25600\n"
    "v_mfma_f32_16x16x32_bf16 v[80:83], a[0:3], a[112:115], v[80:83]\n"
    "ds_read_b64_tr_b16 a[20:21], v103 offset:30720\n"
    "ds_read_b64_tr_b16 a[22:23], v103 offset:35840\n"
    "v_mfma_f32_16x16x32_bf16 v[80:83], a[4:7], a[116:119], v[80:83]\n"
    "s_waitcnt lgkmcnt(4)\n"
    "v_mfma_f32_16x16x32_bf16 v[20:23], a[8:11], a[88:91], v[20:23]\n"
    "v_mfma_f32_16x16x32_bf16 v[20:23], a[12:15], a[92:95], v[20:23]\n"
    "v_mfma_f32_16x16x32_bf16 v[24:27], a[8:11], a[96:99], v[24:27]\n"
    "v_mfma_f32_16x16x32_bf16 v[24:27], a[12:15], a[100:103], v[24:27]\n"
    "v_mfma_f32_16x16x32_bf16 v[28:31], a[8:11], a[104:107], v[28:31]\n"
    "v_mfma_f32_16x16x32_bf16 v[28:31], a[12:15], a[108:111], v[28:31]\n"
    "ds_read_b64_tr_b16 a[24:25], v104 offset:20480\n"
    "ds_read_b64_tr_b16 a[26:27], v104 offset:25600\n"
    "v_mfma_f32_16x16x32_bf16 v[84:87], a[8:11], a[112:115], v[84:87]\n"
    "ds_read_b64_tr_b16 a[28:29], v104 offset:30720\n"
    "ds_read_b64_tr_b16 a[30:31], v104 offset:35840\n"
    "v_mfma_f32_16x16x32_bf16 v[84:87], a[12:15], a[116:119], v[84:87]\n"
    "s_waitcnt lgkmcnt(4)\n"
    "v_mfma_f32_16x16x32_bf16 v[36:39], a[16:19], a[88:91], v[36:39]\n"
    "v_mfma_f32_16x16x32_bf16 v[36:39], a[20:23], a[92:95], v[36:39]\n"
    "v_mfma_f32_16x16x32_bf16 v[40:43], a[16:19], a[96:99], v[40:43]\n"
    "v_cvt_pk_bf16_f32 v4, v4, v5\n"
    "v_cvt_pk_bf16_f32 v5, v6, v7\n"
    "v_mfma_f32_16x16x32_bf16 v[40:43], a[20:23], a[100:103], v[40:43]\n"
    "v_cvt_pk_bf16_f32 v6, v20, v21\n"
    "ds_read_b64_tr_b16 a[32:33], v105 offset:20480\n"
    "v_mfma_f32_16x16x32_bf16 v[44:47], a[16:19], a[104:107], v[44:47]\n"
    "ds_read_b64_tr_b16 a[34:35], v105 offset:25600\n"
    "v_cvt_pk_bf16_f32 v7, v22, v23\n"
    "v_mfma_f32_16x16x32_bf16 v[44:47], a[20:23], a[108:111], v[44:47]\n"
    "buffer_store_dwordx4 v[4:7], v125, s[40:43], s60 offen sc1\n"
    "ds_read_b64_tr_b16 a[36:37], v105 offset:30720\n"
    "v_mfma_f32_16x16x32_bf16 v[88:91], a[16:19], a[112:115], v[88:91]\n"
    "v_cvt_pk_bf16_f32 v8, v8, v9\n"
    "ds_read_b64_tr_b16 a[38:39], v105 offset:35840\n"
    "v_mfma_f32_16x16x32_bf16 v[88:91], a[20:23], a[116:119], v[88:91]\n"
    "s_waitcnt lgkmcnt(4)\n"
    "v_cvt_pk_bf16_f32 v9, v10, v11\n"
    "v_mfma_f32_16x16x32_bf16 v[52:55], a[24:27], a[88:91], v[52:55]\n"
    "v_cvt_pk_bf16_f32 v10, v24, v25\n"
    "v_cvt_pk_bf16_f32 v11, v26, v27\n"
    "v_mfma_f32_16x16x32_bf16 v[52:55], a[28:31], a[92:95], v[52:55]\n"
    "buffer_store_dwordx4 v[8:11], v125, s[40:43], s64 offen sc1\n"
    "v_cvt_pk_bf16_f32 v12, v12, v13\n"
    "v_mfma_f32_16x16x32_bf16 v[56:59], a[24:27], a[96:99], v[56:59]\n"
    "v_cvt_pk_bf16_f32 v13, v14, v15\n"
    "v_cvt_pk_bf16_f32 v14, v28, v29\n"
    "v_mfma_f32_16x16x32_bf16 v[56:59], a[28:31], a[100:103], v[56:59]\n"
    "v_cvt_pk_bf16_f32 v15, v30, v31\n"
    "buffer_store_dwordx4 v[12:15], v125, s[40:43], s65 offen sc1\n"
    "v_mfma_f32_16x16x32_bf16 v[60:63], a[24:27], a[104:107], v[60:63]\n"
    "v_cvt_pk_bf16_f32 v80, v80, v81\n"
    "v_cvt_pk_bf16_f32 v81, v82, v83\n"
    "v_mfma_f32_16x16x32_bf16 v[60:63], a[28:31], a[108:111], v[60:63]\n"
    "s_waitcnt lgkmcnt(0)\n"
    "buffer_store_dwordx2 v[80:81], v130, s[40:43], s59 offen sc1\n"
    "v_mfma_f32_16x16x32_bf16 v[68:71], a[32:35], a[88:91], v[68:71]\n"
    "v_cvt_pk_bf16_f32 v84, v84, v85\n"
    "v_cvt_pk_bf16_f32 v85, v86, v87\n"
    "v_mfma_f32_16x16x32_bf16 v[68:71], a[36:39], a[92:95], v[68:71]\n"
    "buffer_store_dwordx2 v[84:85], v130, s[40:43], s59 offen offset:512 sc1\n"
    "v_mfma_f32_16x16x32_bf16 v[72:75], a[32:35], a[96:99], v[72:75]\n"
    "v_cvt_pk_bf16_f32 v36, v36, v37\n"
    "v_cvt_pk_bf16_f32 v37, v38, v39\n"
    "v_mfma_f32_16x16x32_bf16 v[72:75], a[36:39], a[100:103], v[72:75]\n"
    "v_cvt_pk_bf16_f32 v38, v52, v53\n"
    "v_cvt_pk_bf16_f32 v39, v54, v55\n"
    "buffer_store_dwordx4 v[36:39], v125, s[40:43], s60 offen offset:1024 sc1\n"
    "v_mfma_f32_16x16x32_bf16 v[76:79], a[32:35], a[104:107], v[76:79]\n"
    "v_cvt_pk_bf16_f32 v40, v40, v41\n"
    "v_cvt_pk_bf16_f32 v41, v42, v43\n"
    "v_cvt_pk_bf16_f32 v42, v56, v57\n"
    "v_cvt_pk_bf16_f32 v43, v58, v59\n"
    "v_mfma_f32_16x16x32_bf16 v[76:79], a[36:39], a[108:111], v[76:79]\n"
    "buffer_store_dwordx4 v[40:43], v125, s[40:43], s64 offen offset:1024 sc1\n"
    "v_cvt_pk_bf16_f32 v44, v44, v45\n"
    "v_cvt_pk_bf16_f32 v45, v46, v47\n"
    "v_cvt_pk_bf16_f32 v46, v60, v61\n"
    "v_cvt_pk_bf16_f32 v47, v62, v63\n"
    "buffer_store_dwordx4 v[44:47], v125, s[40:43], s65 offen offset:1024 sc1\n"
    "v_cvt_pk_bf16_f32 v88, v88, v89\n"
    "v_cvt_pk_bf16_f32 v89, v90, v91\n"
    "buffer_store_dwordx2 v[88:89], v130, s[40:43], s59 offen offset:1024 sc1\n"
    "; ---- RED\n"
    "v_cvt_pk_bf16_f32 v68, v68, v69\n"
    "v_cvt_pk_bf16_f32 v69, v70, v71\n"
    "buffer_store_dwordx2 v[68:69], v130, s[40:43], s60 offen offset:2048 sc1\n"
    "v_cvt_pk_bf16_f32 v72, v72, v73\n"
    "v_cvt_pk_bf16_f32 v73, v74, v75\n"
    "buffer_store_dwordx2 v[72:73], v130, s[40:43], s64 offen offset:2048 sc1\n"
    "v_cvt_pk_bf16_f32 v76, v76, v77\n"
    "v_cvt_pk_bf16_f32 v77, v78, v79\n"
    "buffer_store_dwordx2 v[76:77], v130, s[40:43], s65 offen offset:2048 sc1\n"
    "v_mfma_f32_16x16x32_bf16 v[0:3], a[120:123], a[40:43], v[0:3]\n"
    "v_mfma_f32_16x16x32_bf16 v[0:3], a[124:127], a[44:47], v[0:3]\n"
    "v_mfma_f32_16x16x32_bf16 v[16:19], a[128:131], a[40:43], v[16:19]\n"
    "v_mfma_f32_16x16x32_bf16 v[16:19], a[132:135], a[44:47], v[16:19]\n"
    "v_mfma_f32_16x16x32_bf16 v[32:35], a[136:139], a[40:43], v[32:35]\n"
    "v_mfma_f32_16x16x32_bf16 v[32:35], a[140:143], a[44:47], v[32:35]\n"
    "v_mfma_f32_16x16x32_bf16 v[48:51], a[144:147], a[40:43], v[48:51]\n"
    "v_mfma_f32_16x16x32_bf16 v[48:51], a[148:151], a[44:47], v[48:51]\n"
    "v_mfma_f32_16x16x32_bf16 v[64:67], a[152:155], a[40:43], v[64:67]\n"
    "v_mfma_f32_16x16x32_bf16 v[64:67], a[156:159], a[44:47], v[64:67]\n"
    "v_mfma_f32_16x16x32_bf16 v[0:3], a[0:3], a[80:83], v[0:3]\n"
    "v_mfma_f32_16x16x32_bf16 v[0:3], a[4:7], a[84:87], v[0:3]\n"
    "v_mfma_f32_16x16x32_bf16 v[16:19], a[8:11], a[80:83], v[16:19]\n"
    "v_mfma_f32_16x16x32_bf16 v[16:19], a[12:15], a[84:87], v[16:19]\n"
    "v_mfma_f32_16x16x32_bf16 v[32:35], a[16:19], a[80:83], v[32:35]\n"
    "v_mfma_f32_16x16x32_bf16 v[32:35], a[20:23], a[84:87], v[32:35]\n"
    "v_mfma_f32_16x16x32_bf16 v[48:51], a[24:27], a[80:83], v[48:51]\n"
    "v_mfma_f32_16x16x32_bf16 v[48:51], a[28:31], a[84:87], v[48:51]\n"
    "v_mfma_f32_16x16x32_bf16 v[64:67], a[32:35], a[80:83], v[64:67]\n"
    "v_mfma_f32_16x16x32_bf16 v[64:67], a[36:39], a[84:87], v[64:67]\n"
    "s_waitcnt vmcnt(0)\n"
    "s_mov_b64 exec, 7\n"
    "global_store_dword v243, v230, s[80:81] offset:4 sc1\n"
    "s_mov_b64 exec, -1\n"
    "LhBs:\n"
    "s_sub_u32 s74, s82, 1\n"
    "s_and_b32 s74, s74, 3\n"
    "s_lshl_b32 s74, s74, 2\n"
    "s_add_u32 s74, s74, s56\n"
    "s_mul_i32 s74, s74, 9216\n"
    "s_add_u32 s70, s74, 0\n"
    "s_sub_u32 s74, s82, 2\n"
    "s_and_b32 s74, s74, 3\n"
    "s_lshl_b32 s74, s74, 2\n"
    "s_add_u32 s74, s74, s56\n"
    "s_mul_i32 s74, s74, 9216\n"
    "s_add_u32 s71, s74, 2560\n"
    "s_sub_u32 s74, s82, 3\n"
    "s_and_b32 s74, s74, 3\n"
    "s_lshl_b32 s74, s74, 2\n"
    "s_add_u32 s74, s74, s56\n"
    "s_mul_i32 s74, s74, 9216\n"
    "s_add_u32 s72, s74, 5120\n"
    "s_min_u32 s75, s82, 2\n"
    "s_lshl_b32 s75, s75, 9\n"
    "s_mul_i32 s66, s56, 9216\n"
    "s_add_u32 s66, s66, s75\n"
    "s_add_u32 s66, s66, 7680\n"
    "s_lshl_b32 s74, s77, 2\n"
    "s_add_u32 s74, s74, s82\n"
    "s_mul_i32 s90, s74, 40960\n"
    "s_mul_i32 s74, s77, 3\n"
    "s_add_u32 s74, s74, 0\n"
    "s_sub_u32 s75, s74, 5\n"
    "s_cmp_ge_u32 s74, 5\n"
    "s_cselect_b32 s74, s75, s74\n"
    "s_mul_i32 s75, s83, 5\n"
    "s_add_u32 s74, s74, s75\n"
    "s_lshl_b32 s36, s74, 5\n"
    "s_mul_i32 s74, s77, 3\n"
    "s_add_u32 s74, s74, 1\n"
    "s_sub_u32 s75, s74, 5\n"
    "s_cmp_ge_u32 s74, 5\n"
    "s_cselect_b32 s74, s75, s74\n"
    "s_mul_i32 s75, s83, 5\n"
    "s_add_u32 s74, s74, s75\n"
    "s_lshl_b32 s37, s74, 5\n"
    "s_mul_i32 s74, s77, 3\n"
    "s_add_u32 s74, s74, 2\n"
    "s_sub_u32 s75, s74, 5\n"
    "s_cmp_ge_u32 s74, 5\n"
    "s_cselect_b32 s74, s75, s74\n"
    "s_mul_i32 s75, s83, 5\n"
    "s_add_u32 s74, s74, s75\n"
    "s_lshl_b32 s38, s74, 5\n"
    "s_mul_i32 s74, s77, 3\n"
    "s_add_u32 s74, s74, 3\n"
    "s_sub_u32 s75, s74, 5\n"
    "s_cmp_ge_u32 s74, 5\n"
    "s_cselect_b32 s74, s75, s74\n"
    "s_mul_i32 s75, s83, 5\n"
    "s_add_u32 s74, s74, s75\n"
    "s_lshl_b32 s39, s74, 5\n"
    "s_mul_i32 s74, s77, 3\n"
    "s_add_u32 s74, s74, 4\n"
    "s_sub_u32 s75, s74, 5\n"
    "s_cmp_ge_u32 s74, 5\n"
    "s_cselect_b32 s74, s75, s74\n"
    "s_mul_i32 s75, s83, 5\n"
    "s_add_u32 s74, s74, s75\n"
    "s_lshl_b32 s58, s74, 5\n"
    "s_cmp_eq_u32 s82, 1\n"
    "s_cselect_b32 s74, s37, s36\n"
    "s_cmp_eq_u32 s82, 2\n"
    "s_cselect_b32 s74, s38, s74\n"
    "s_add_u32 s74, s74, 327680\n"
    "s_add_u32 s75, s82, s77\n"
    "s_cmp_ge_u32 s75, 3\n"
    "s_cselect_b32 s57, 0x40000000, s74\n"
    "s_add_u32 s36, s36, s90\n"
    "s_add_u32 s37, s37, s90\n"
    "s_add_u32 s38, s38, s90\n"
    "s_add_u32 s39, s39, s90\n"
    "s_add_u32 s58, s58, s90\n"
    "v_and_b32 v127, 15, v124\n"
    "v_lshrrev_b32 v128, 5, v124\n"
    "v_mul_u32_u24 v129, 2560, v127\n"
    "v_lshl_add_u32 v129, v128, 4, v129\n"
    "v_and_b32 v128, 16, v124\n"
    "v_cmp_ne_u32 vcc, 0, v128\n"
    "v_mov_b32 v127, s36\n"
    "v_mov_b32 v128, s37\n"
    "v_cndmask_b32 v127, v127, v128, vcc\n"
    "v_add_u32 v126, v127, v129\n"
    "v_mov_b32 v127, s38\n"
    "v_mov_b32 v128, s39\n"
    "v_cndmask_b32 v127, v127, v128, vcc\n"
    "v_add_u32 v131, v127, v129\n"
    "v_mov_b32 v127, s58\n"
    "v_mov_b32 v128, s57\n"
    "v_cndmask_b32 v127, v127, v128, vcc\n"
    "v_add_u32 v137, v127, v129\n"
    "s_mov_b64 exec, 7\n"
    "s_mov_b32 s74, 1048576\n"
    "LhBp:\n"
    "global_load_dwordx2 v[138:139], v244, s[80:81] sc1\n"
    "s_waitcnt vmcnt(0)\n"
    "LhBq:\n"
    "v_cmp_ne_u32 vcc, v138, v230\n"
    "s_cbranch_vccz LhBc\n"
    "v_mov_b32 v230, v138\n"
    "global_store_dword v243, v230, s[80:81] offset:4 sc1\n"
    "LhBc:\n"
    "v_cmp_ne_u32 vcc, v139, v245\n"
    "s_cbranch_vccz LhBd\n"
    "s_sub_u32 s74, s74, 1\n"
    "s_cbranch_scc0 LhBp\n"
    "LhBd:\n"
    "s_mov_b64 exec, -1\n"
    "LhBj:\n"
    "buffer_load_dwordx4 v[140:143], v125, s[40:43], s70 offen sc1\n"
    "buffer_load_dwordx4 v[144:147], v125, s[40:43], s70 offen offset:1024 sc1\n"
    "buffer_load_dwordx2 v[148:149], v130, s[40:43], s70 offen offset:2048 sc1\n"
    "buffer_load_dwordx4 v[152:155], v125, s[40:43], s71 offen sc1\n"
    "buffer_load_dwordx4 v[156:159], v125, s[40:43], s71 offen offset:1024 sc1\n"
    "buffer_load_dwordx2 v[160:161], v130, s[40:43], s71 offen offset:2048 sc1\n"
    "buffer_load_dwordx4 v[164:167], v125, s[40:43], s72 offen sc1\n"
    "buffer_load_dwordx4 v[168:171], v125, s[40:43], s72 offen offset:1024 sc1\n"
    "buffer_load_dwordx2 v[172:173], v130, s[40:43], s72 offen offset:2048 sc1\n"
    "buffer_load_dwordx2 v[176:177], v130, s[40:43], s66 offen sc1\n"
    "s_add_u32 s66, s66, 36864\n"
    "buffer_load_dwordx2 v[178:179], v130, s[40:43], s66 offen sc1\n"
    "s_add_u32 s66, s66, 36864\n"
    "buffer_load_dwordx2 v[180:181], v130, s[40:43], s66 offen sc1\n"
    "s_add_u32 s66, s66, 36864\n"
    "buffer_load_dwordx2 v[182:183], v130, s[40:43], s66 offen sc1\n"
    "s_add_u32 s66, s66, 36864\n"
    "s_waitcnt vmcnt(12)\n"
    "v_lshlrev_b32 v190, 16, v140\n"
    "v_and_b32 v191, 0xffff0000, v140\n"
    "v_lshlrev_b32 v192, 16, v141\n"
    "v_and_b32 v193, 0xffff0000, v141\n"
    "v_pk_add_f32 v[0:1], v[0:1], v[190:191]\n"
    "v_pk_add_f32 v[2:3], v[2:3], v[192:193]\n"
    "v_lshlrev_b32 v194, 16, v142\n"
    "v_and_b32 v195, 0xffff0000, v142\n"
    "v_lshlrev_b32 v196, 16, v143\n"
    "v_and_b32 v197, 0xffff0000, v143\n"
    "v_pk_add_f32 v[16:17], v[16:17], v[194:195]\n"
    "v_pk_add_f32 v[18:19], v[18:19], v[196:197]\n"
    "s_waitcnt vmcnt(11)\n"
    "v_lshlrev_b32 v190, 16, v144\n"
    "v_and_b32 v191, 0xffff0000, v144\n"
    "v_lshlrev_b32 v192, 16, v145\n"
    "v_and_b32 v193, 0xffff0000, v145\n"
    "v_pk_add_f32 v[32:33], v[32:33], v[190:191]\n"
    "v_pk_add_f32 v[34:35], v[34:35], v[192:193]\n"
    "v_lshlrev_b32 v194, 16, v146\n"
    "v_and_b32 v195, 0xffff0000, v146\n"
    "v_lshlrev_b32 v196, 16, v147\n"
    "v_and_b32 v197, 0xffff0000, v147\n"
    "v_pk_add_f32 v[48:49], v[48:49], v[194:195]\n"
    "v_pk_add_f32 v[50:51], v[50:51], v[196:197]\n"
    "s_waitcnt vmcnt(10)\n"
    "v_lshlrev_b32 v190, 16, v148\n"
    "v_and_b32 v191, 0xffff0000, v148\n"
    "v_lshlrev_b32 v192, 16, v149\n"
    "v_and_b32 v193, 0xffff0000, v149\n"
    "v_pk_add_f32 v[64:65], v[64:65], v[190:191]\n"
    "v_pk_add_f32 v[66:67], v[66:67], v[192:193]\n"
    "s_waitcnt vmcnt(9)\n"
    "v_lshlrev_b32 v190, 16, v152\n"
    "v_and_b32 v191, 0xffff0000, v152\n"
    "v_lshlrev_b32 v192, 16, v153\n"
    "v_and_b32 v193, 0xffff0000, v153\n"
    "v_pk_add_f32 v[0:1], v[0:1], v[190:191]\n"
    "v_pk_add_f32 v[2:3], v[2:3], v[192:193]\n"
    "v_lshlrev_b32 v194, 16, v154\n"
    "v_and_b32 v195, 0xffff0000, v154\n"
    "v_lshlrev_b32 v196, 16, v155\n"
    "v_and_b32 v197, 0xffff0000, v155\n"
    "v_pk_add_f32 v[16:17], v[16:17], v[194:195]\n"
    "v_pk_add_f32 v[18:19], v[18:19], v[196:197]\n"
    "s_waitcnt vmcnt(8)\n"
    "v_lshlrev_b32 v190, 16, v156\n"
    "v_and_b32 v191, 0xffff0000, v156\n"
    "v_lshlrev_b32 v192, 16, v157\n"
    "v_and_b32 v193, 0xffff0000, v157\n"
    "v_pk_add_f32 v[32:33], v[32:33], v[190:191]\n"
    "v_pk_add_f32 v[34:35], v[34:35], v[192:193]\n"
    "v_lshlrev_b32 v194, 16, v158\n"
    "v_and_b32 v195, 0xffff0000, v158\n"
    "v_lshlrev_b32 v196, 16, v159\n"
    "v_and_b32 v197, 0xffff0000, v159\n"
    "v_pk_add_f32 v[48:49], v[48:49], v[194:195]\n"
    "v_pk_add_f32 v[50:51], v[50:51], v[196:197]\n"
    "s_waitcnt vmcnt(7)\n"
    "v_lshlrev_b32 v190, 16, v160\n"
    "v_and_b32 v191, 0xffff0000, v160\n"
    "v_lshlrev_b32 v192, 16, v161\n"
    "v_and_b32 v193, 0xffff0000, v161\n"
    "v_pk_add_f32 v[64:65], v[64:65], v[190:191]\n"
    "v_pk_add_f32 v[66:67], v[66:67], v[192:193]\n"
    "s_waitcnt vmcnt(6)\n"
    "v_lshlrev_b32 v190, 16, v164\n"
    "v_and_b32 v191, 0xffff0000, v164\n"
    "v_lshlrev_b32 v192, 16, v165\n"
    "v_and_b32 v193, 0xffff0000, v165\n"
    "v_pk_add_f32 v[0:1], v[0:1], v[190:191]\n"
    "v_pk_add_f32 v[2:3], v[2:3], v[192:193]\n"
    "v_lshlrev_b32 v194, 16, v166\n"
    "v_and_b32 v195, 0xffff0000, v166\n"
    "v_lshlrev_b32 v196, 16, v167\n"
    "v_and_b32 v197, 0xffff0000, v167\n"
    "v_pk_add_f32 v[16:17], v[16:17], v[194:195]\n"
    "v_pk_add_f32 v[18:19], v[18:19], v[196:197]\n"
    "v_cvt_pk_bf16_f32 v0, v0, v1\n"
    "v_cvt_pk_bf16_f32 v1, v2, v3\n"
    "v_cvt_pk_bf16_f32 v2, v16, v17\n"
    "v_cvt_pk_bf16_f32 v3, v18, v19\n"
    "s_nop 0\n"
    "v_permlane16_swap_b32 v0, v2\n"
    "v_permlane16_swap_b32 v1, v3\n"
    "buffer_store_dwordx4 v[0:3], v126, s[44:47], 0 offen nt\n"
    "s_waitcnt vmcnt(6)\n"
    "v_lshlrev_b32 v190, 16, v168\n"
    "v_and_b32 v191, 0xffff0000, v168\n"
    "v_lshlrev_b32 v192, 16, v169\n"
    "v_and_b32 v193, 0xffff0000, v169\n"
    "v_pk_add_f32 v[32:33], v[32:33], v[190:191]\n"
    "v_pk_add_f32 v[34:35], v[34:35], v[192:193]\n"
    "v_lshlrev_b32 v194, 16, v170\n"
    "v_and_b32 v195, 0xffff0000, v170\n"
    "v_lshlrev_b32 v196, 16, v171\n"
    "v_and_b32 v197, 0xffff0000, v171\n"
    "v_pk_add_f32 v[48:49], v[48:49], v[194:195]\n"
    "v_pk_add_f32 v[50:51], v[50:51], v[196:197]\n"
    "v_cvt_pk_bf16_f32 v32, v32, v33\n"
    "v_cvt_pk_bf16_f32 v33, v34, v35\n"
    "v_cvt_pk_bf16_f32 v34, v48, v49\n"
    "v_cvt_pk_bf16_f32 v35, v50, v51\n"
    "s_nop 0\n"
    "v_permlane16_swap_b32 v32, v34\n"
    "v_permlane16_swap_b32 v33, v35\n"
    "buffer_store_dwordx4 v[32:35], v131, s[44:47], 0 offen nt\n"
    "s_waitcnt vmcnt(6)\n"
    "v_lshlrev_b32 v190, 16, v172\n"
    "v_and_b32 v191, 0xffff0000, v172\n"
    "v_lshlrev_b32 v192, 16, v173\n"
    "v_and_b32 v193, 0xffff0000, v173\n"
    "v_pk_add_f32 v[64:65], v[64:65], v[190:191]\n"
    "v_pk_add_f32 v[66:67], v[66:67], v[192:193]\n"
    "s_waitcnt vmcnt(5)\n"
    "v_lshlrev_b32 v80, 16, v176\n"
    "v_and_b32 v81, 0xffff0000, v176\n"
    "v_lshlrev_b32 v82, 16, v177\n"
    "v_and_b32 v83, 0xffff0000, v177\n"
    "s_waitcnt vmcnt(4)\n"
    "v_lshlrev_b32 v194, 16, v178\n"
    "v_and_b32 v195, 0xffff0000, v178\n"
    "v_lshlrev_b32 v196, 16, v179\n"
    "v_and_b32 v197, 0xffff0000, v179\n"
    "v_pk_add_f32 v[80:81], v[80:81], v[194:195]\n"
    "v_pk_add_f32 v[82:83], v[82:83], v[196:197]\n"
    "s_waitcnt vmcnt(3)\n"
    "v_lshlrev_b32 v190, 16, v180\n"
    "v_and_b32 v191, 0xffff0000, v180\n"
    "v_lshlrev_b32 v192, 16, v181\n"
    "v_and_b32 v193, 0xffff0000, v181\n"
    "v_pk_add_f32 v[80:81], v[80:81], v[190:191]\n"
    "v_pk_add_f32 v[82:83], v[82:83], v[192:193]\n"
    "s_waitcnt vmcnt(2)\n"
    "v_lshlrev_b32 v194, 16, v182\n"
    "v_and_b32 v195, 0xffff0000, v182\n"
    "v_lshlrev_b32 v196, 16, v183\n"
    "v_and_b32 v197, 0xffff0000, v183\n"
    "v_pk_add_f32 v[80:81], v[80:81], v[194:195]\n"
    "v_pk_add_f32 v[82:83], v[82:83], v[196:197]\n"
    "v_cvt_pk_bf16_f32 v64, v64, v65\n"
    "v_cvt_pk_bf16_f32 v65, v66, v67\n"
    "v_cvt_pk_bf16_f32 v66, v80, v81\n"
    "v_cvt_pk_bf16_f32 v67, v82, v83\n"
    "s_nop 0\n"
    "v_permlane16_swap_b32 v64, v66\n"
    "v_permlane16_swap_b32 v65, v67\n"
    "buffer_store_dwordx4 v[64:67], v137, s[44:47], 0 offen nt\n"
    "Lfin:\n"
    "s_waitcnt vmcnt(0) lgkmcnt(0)\n"
    "s_branch Lend\n"
    "Lstub0_b_1:\n"
    "s_getpc_b64 s[68:69]\n"
    "s_branch Lslow0_0\n"
    "s_branch Lback0_b_1\n"
    "Lstub0_b_2:\n"
    "s_getpc_b64 s[68:69]\n"
    "s_branch Lslow0_1\n"
    "s_branch Lback0_b_2\n"
    "Lstub0_b_3:\n"
    "s_getpc_b64 s[68:69]\n"
    "s_branch Lslow0_2\n"
    "s_branch Lback0_b_3\n"
    "Lstub0_b_4:\n"
    "s_getpc_b64 s[68:69]\n"
    "s_branch Lslow0_3\n"
    "s_branch Lback0_b_4\n"
    "Lstub0_2_0:\n"
    "s_getpc_b64 s[68:69]\n"
    "s_branch Lslow0_4\n"
    "s_branch Lback0_2_0\n"
    "Lstub0_2_1:\n"
    "s_getpc_b64 s[68:69]\n"
    "s_branch Lslow0_0\n"
    "s_branch Lback0_2_1\n"
    "Lstub0_2_2:\n"
    "s_getpc_b64 s[68:69]\n"
    "s_branch Lslow0_1\n"
    "s_branch Lback0_2_2\n"
    "Lstub0_2_3:\n"
    "s_getpc_b64 s[68:69]\n"
    "s_branch Lslow0_2\n"
    "s_branch Lback0_2_3\n"
    "Lstub0_2_4:\n"
    "s_getpc_b64 s[68:69]\n"
    "s_branch Lslow0_3\n"
    "s_branch Lback0_2_4\n"
    "Lstub0_3_0:\n"
    "s_getpc_b64 s[68:69]\n"
    "s_branch Lslow0_4\n"
    "s_branch Lback0_3_0\n"
    "Lstub0_3_1:\n"
    "s_getpc_b64 s[68:69]\n"
    "s_branch Lslow0_0\n"
    "s_branch Lback0_3_1\n"
    "Lstub0_3_2:\n"
    "s_getpc_b64 s[68:69]\n"
    "s_branch Lslow0_1\n"
    "s_branch Lback0_3_2\n"
    "Lstub0_3_3:\n"
    "s_getpc_b64 s[68:69]\n"
    "s_branch Lslow0_2\n"
    "s_branch Lback0_3_3\n"
    "Lstub0_3_4:\n"
    "s_getpc_b64 s[68:69]\n"
    "s_branch Lslow0_3\n"
    "s_branch Lback0_3_4\n"
    "Lstub0_0_0:\n"
    "s_getpc_b64 s[68:69]\n"
    "s_branch Lslow0_4\n"
    "s_branch Lback0_0_0\n"
    "Lstub0_0_1:\n"
    "s_getpc_b64 s[68:69]\n"
    "s_branch Lslow0_0\n"
    "s_branch Lback0_0_1\n"
    "Lstub0_0_2:\n"
    "s_getpc_b64 s[68:69]\n"
    "s_branch Lslow0_1\n"
    "s_branch Lback0_0_2\n"
    "Lstub0_0_3:\n"
    "s_getpc_b64 s[68:69]\n"
    "s_branch Lslow0_2\n"
    "s_branch Lback0_0_3\n"
    "Lstub0_0_4:\n"
    "s_getpc_b64 s[68:69]\n"
    "s_branch Lslow0_3\n"
    "s_branch Lback0_0_4\n"
    "Lstub0_1_0:\n"
    "s_getpc_b64 s[68:69]\n"
    "s_branch Lslow0_4\n"
    "s_branch Lback0_1_0\n"
    "Lstub0_1_1:\n"
    "s_getpc_b64 s[68:69]\n"
    "s_branch Lslow0_0\n"
    "s_branch Lback0_1_1\n"
    "Lstub0_1_2:\n"
    "s_getpc_b64 s[68:69]\n"
    "s_branch Lslow0_1\n"
    "s_branch Lback0_1_2\n"
    "Lstub0_1_3:\n"
    "s_getpc_b64 s[68:69]\n"
    "s_branch Lslow0_2\n"
    "s_branch Lback0_1_3\n"
    "Lstub0_1_4:\n"
    "s_getpc_b64 s[68:69]\n"
    "s_branch Lslow0_3\n"
    "s_branch Lback0_1_4\n"
    "Lstub0_c_0:\n"
    "s_getpc_b64 s[68:69]\n"
    "s_branch Lslow0_4\n"
    "s_branch Lback0_c_0\n"
    "Lstub0_c_1:\n"
    "s_getpc_b64 s[68:69]\n"
    "s_branch Lslow0_0\n"
    "s_branch Lback0_c_1\n"
    "Lstub0_c_2:\n"
    "s_getpc_b64 s[68:69]\n"
    "s_branch Lslow0_1\n"
    "s_branch Lback0_c_2\n"
    "Lstub0_c_3:\n"
    "s_getpc_b64 s[68:69]\n"
    "s_branch Lslow0_2\n"
    "s_branch Lback0_c_3\n"
    "Lstub0_c_4:\n"
    "s_getpc_b64 s[68:69]\n"
    "s_branch Lslow0_3\n"
    "s_branch Lback0_c_4\n"
    "Lstub0_d_0:\n"
    "s_getpc_b64 s[68:69]\n"
    "s_branch Lslow0_4\n"
    "s_branch Lback0_d_0\n"
    "Lstub0_d_1:\n"
    "s_getpc_b64 s[68:69]\n"
    "s_branch Lslow0_0\n"
    "s_branch Lback0_d_1\n"
    "Lstub0_d_2:\n"
    "s_getpc_b64 s[68:69]\n"
    "s_branch Lslow0_1\n"
    "s_branch Lback0_d_2\n"
    "Lstub0_d_3:\n"
    "s_getpc_b64 s[68:69]\n"
    "s_branch Lslow0_2\n"
    "s_branch Lback0_d_3\n"
    "Lstub0_d_4:\n"
    "s_getpc_b64 s[68:69]\n"
    "s_branch Lslow0_3\n"
    "s_branch Lback0_d_4\n"
    "Lstub0_e:\n"
    "s_getpc_b64 s[68:69]\n"
    "s_branch Lslow0_4\n"
    "s_branch Lback0_e\n"
    "Lslow0_0:\n"
    "s_nop 7\n"
    "v_max3_f32 v224, v128, v129, v130\n"
    "v_max3_f32 v225, v131, v132, v133\n"
    "v_max3_f32 v224, v224, v134, v135\n"
    "v_max_f32 v224, v224, v225\n"
    "v_mov_b32 v225, v224\n"
    "s_nop 1\n"
    "v_permlane16_swap_b32 v224, v225\n"
    "v_max_f32 v224, v224, v225\n"
    "v_mov_b32 v225, v224\n"
    "s_nop 1\n"
    "v_permlane32_swap_b32 v224, v225\n"
    "v_max_f32 v224, v224, v225\n"
    "v_ceil_f32 v224, v224\n"
    "v_max_f32 v224, 0, v224\n"
    "v_sub_f32 v128, v128, v224\n"
    "v_sub_f32 v129, v129, v224\n"
    "v_sub_f32 v130, v130, v224\n"
    "v_sub_f32 v131, v131, v224\n"
    "v_sub_f32 v132, v132, v224\n"
    "v_sub_f32 v133, v133, v224\n"
    "v_sub_f32 v134, v134, v224\n"
    "v_sub_f32 v135, v135, v224\n"
    "v_sub_f32 v188, v188, v224\n"
    "v_sub_f32 v189, v189, v224\n"
    "v_sub_f32 v190, v190, v224\n"
    "v_sub_f32 v191, v191, v224\n"
    "v_sub_f32 v225, 0, v224\n"
    "v_exp_f32 v226, v225\n"
    "s_nop 0\n"
    "v_mul_f32 v212, v212, v226\n"
    "v_mul_f32 v0, v0, v226\n"
    "v_mul_f32 v1, v1, v226\n"
    "v_mul_f32 v2, v2, v226\n"
    "v_mul_f32 v3, v3, v226\n"
    "v_mul_f32 v4, v4, v226\n"
    "v_mul_f32 v5, v5, v226\n"
    "v_mul_f32 v6, v6, v226\n"
    "v_mul_f32 v7, v7, v226\n"
    "v_mul_f32 v8, v8, v226\n"
    "v_mul_f32 v9, v9, v226\n"
    "v_mul_f32 v10, v10, v226\n"
    "v_mul_f32 v11, v11, v226\n"
    "v_mul_f32 v12, v12, v226\n"
    "v_mul_f32 v13, v13, v226\n"
    "v_mul_f32 v14, v14, v226\n"
    "v_mul_f32 v15, v15, v226\n"
    "v_mul_f32 v16, v16, v226\n"
    "v_mul_f32 v17, v17, v226\n"
    "v_mul_f32 v18, v18, v226\n"
    "v_mul_f32 v19, v19, v226\n"
    "v_mul_f32 v20, v20, v226\n"
    "v_mul_f32 v21, v21, v226\n"
    "v_mul_f32 v22, v22, v226\n"
    "v_mul_f32 v23, v23, v226\n"
    "v_mul_f32 v24, v24, v226\n"
    "v_mul_f32 v25, v25, v226\n"
    "v_mul_f32 v26, v26, v226\n"
    "v_mul_f32 v27, v27, v226\n"
    "v_mul_f32 v28, v28, v226\n"
    "v_mul_f32 v29, v29, v226\n"
    "v_mul_f32 v30, v30, v226\n"
    "v_mul_f32 v31, v31, v226\n"
    "v_exp_f32 v226, v128\n"
    "v_exp_f32 v227, v129\n"
    "v_exp_f32 v228, v130\n"
    "v_exp_f32 v229, v131\n"
    "v_cvt_pk_bf16_f32 v168, v226, v227\n"
    "v_exp_f32 v226, v132\n"
    "v_exp_f32 v227, v133\n"
    "v_cvt_pk_bf16_f32 v169, v228, v229\n"
    "v_exp_f32 v228, v134\n"
    "v_exp_f32 v229, v135\n"
    "v_cvt_pk_bf16_f32 v170, v226, v227\n"
    "v_cvt_pk_bf16_f32 v171, v228, v229\n"
    "s_nop 1\n"
    "v_mfma_f32_16x16x32_bf16 v[208:211], a[240:243], v[168:171], 0\n"
    "s_add_u32 s68, s68, 4\n"
    "s_addc_u32 s69, s69, 0\n"
    "s_nop 7\n"
    "s_setpc_b64 s[68:69]\n"
    "Lslow0_1:\n"
    "s_nop 7\n"
    "v_max3_f32 v224, v136, v137, v138\n"
    "v_max3_f32 v225, v139, v140, v141\n"
    "v_max3_f32 v224, v224, v142, v143\n"
    "v_max_f32 v224, v224, v225\n"
    "v_mov_b32 v225, v224\n"
    "s_nop 1\n"
    "v_permlane16_swap_b32 v224, v225\n"
    "v_max_f32 v224, v224, v225\n"
    "v_mov_b32 v225, v224\n"
    "s_nop 1\n"
    "v_permlane32_swap_b32 v224, v225\n"
    "v_max_f32 v224, v224, v225\n"
    "v_ceil_f32 v224, v224\n"
    "v_max_f32 v224, 0, v224\n"
    "v_sub_f32 v136, v136, v224\n"
    "v_sub_f32 v137, v137, v224\n"
    "v_sub_f32 v138, v138, v224\n"
    "v_sub_f32 v139, v139, v224\n"
    "v_sub_f32 v140, v140, v224\n"
    "v_sub_f32 v141, v141, v224\n"
    "v_sub_f32 v142, v142, v224\n"
    "v_sub_f32 v143, v143, v224\n"
    "v_sub_f32 v192, v192, v224\n"
    "v_sub_f32 v193, v193, v224\n"
    "v_sub_f32 v194, v194, v224\n"
    "v_sub_f32 v195, v195, v224\n"
    "v_sub_f32 v225, 0, v224\n"
    "v_exp_f32 v226, v225\n"
    "s_nop 0\n"
    "v_mul_f32 v213, v213, v226\n"
    "v_mul_f32 v32, v32, v226\n"
    "v_mul_f32 v33, v33, v226\n"
    "v_mul_f32 v34, v34, v226\n"
    "v_mul_f32 v35, v35, v226\n"
    "v_mul_f32 v36, v36, v226\n"
    "v_mul_f32 v37, v37, v226\n"
    "v_mul_f32 v38, v38, v226\n"
    "v_mul_f32 v39, v39, v226\n"
    "v_mul_f32 v40, v40, v226\n"
    "v_mul_f32 v41, v41, v226\n"
    "v_mul_f32 v42, v42, v226\n"
    "v_mul_f32 v43, v43, v226\n"
    "v_mul_f32 v44, v44, v226\n"
    "v_mul_f32 v45, v45, v226\n"
    "v_mul_f32 v46, v46, v226\n"
    "v_mul_f32 v47, v47, v226\n"
    "v_mul_f32 v48, v48, v226\n"
    "v_mul_f32 v49, v49, v226\n"
    "v_mul_f32 v50, v50, v226\n"
    "v_mul_f32 v51, v51, v226\n"
    "v_mul_f32 v52, v52, v226\n"
    "v_mul_f32 v53, v53, v226\n"
    "v_mul_f32 v54, v54, v226\n"
    "v_mul_f32 v55, v55, v226\n"
    "v_mul_f32 v56, v56, v226\n"
    "v_mul_f32 v57, v57, v226\n"
    "v_mul_f32 v58, v58, v226\n"
    "v_mul_f32 v59, v59, v226\n"
    "v_mul_f32 v60, v60, v226\n"
    "v_mul_f32 v61, v61, v226\n"
    "v_mul_f32 v62, v62, v226\n"
    "v_mul_f32 v63, v63, v226\n"
    "v_exp_f32 v226, v136\n"
    "v_exp_f32 v227, v137\n"
    "v_exp_f32 v228, v138\n"
    "v_exp_f32 v229, v139\n"
    "v_cvt_pk_bf16_f32 v172, v226, v227\n"
    "v_exp_f32 v226, v140\n"
    "v_exp_f32 v227, v141\n"
    "v_cvt_pk_bf16_f32 v173, v228, v229\n"
    "v_exp_f32 v228, v142\n"
    "v_exp_f32 v229, v143\n"
    "v_cvt_pk_bf16_f32 v174, v226, v227\n"
    "v_cvt_pk_bf16_f32 v175, v228, v229\n"
    "s_nop 1\n"
    "v_mfma_f32_16x16x32_bf16 v[208:211], a[240:243], v[172:175], 0\n"
    "s_add_u32 s68, s68, 4\n"
    "s_addc_u32 s69, s69, 0\n"
    "s_nop 7\n"
    "s_setpc_b64 s[68:69]\n"
    "Lslow0_2:\n"
    "s_nop 7\n"
    "v_max3_f32 v224, v144, v145, v146\n"
    "v_max3_f32 v225, v147, v148, v149\n"
    "v_max3_f32 v224, v224, v150, v151\n"
    "v_max_f32 v224, v224, v225\n"
    "v_mov_b32 v225, v224\n"
    "s_nop 1\n"
    "v_permlane16_swap_b32 v224, v225\n"
    "v_max_f32 v224, v224, v225\n"
    "v_mov_b32 v225, v224\n"
    "s_nop 1\n"
    "v_permlane32_swap_b32 v224, v225\n"
    "v_max_f32 v224, v224, v225\n"
    "v_ceil_f32 v224, v224\n"
    "v_max_f32 v224, 0, v224\n"
    "v_sub_f32 v144, v144, v224\n"
    "v_sub_f32 v145, v145, v224\n"
    "v_sub_f32 v146, v146, v224\n"
    "v_sub_f32 v147, v147, v224\n"
    "v_sub_f32 v148, v148, v224\n"
    "v_sub_f32 v149, v149, v224\n"
    "v_sub_f32 v150, v150, v224\n"
    "v_sub_f32 v151, v151, v224\n"
    "v_sub_f32 v196, v196, v224\n"
    "v_sub_f32 v197, v197, v224\n"
    "v_sub_f32 v198, v198, v224\n"
    "v_sub_f32 v199, v199, v224\n"
    "v_sub_f32 v225, 0, v224\n"
    "v_exp_f32 v226, v225\n"
    "s_nop 0\n"
    "v_mul_f32 v214, v214, v226\n"
    "v_mul_f32 v64, v64, v226\n"
    "v_mul_f32 v65, v65, v226\n"
    "v_mul_f32 v66, v66, v226\n"
    "v_mul_f32 v67, v67, v226\n"
    "v_mul_f32 v68, v68, v226\n"
    "v_mul_f32 v69, v69, v226\n"
    "v_mul_f32 v70, v70, v226\n"
    "v_mul_f32 v71, v71, v226\n"
    "v_mul_f32 v72, v72, v226\n"
    "v_mul_f32 v73, v73, v226\n"
    "v_mul_f32 v74, v74, v226\n"
    "v_mul_f32 v75, v75, v226\n"
    "v_mul_f32 v76, v76, v226\n"
    "v_mul_f32 v77, v77, v226\n"
    "v_mul_f32 v78, v78, v226\n"
    "v_mul_f32 v79, v79, v226\n"
    "v_mul_f32 v80, v80, v226\n"
    "v_mul_f32 v81, v81, v226\n"
    "v_mul_f32 v82, v82, v226\n"
    "v_mul_f32 v83, v83, v226\n"
    "v_mul_f32 v84, v84, v226\n"
    "v_mul_f32 v85, v85, v226\n"
    "v_mul_f32 v86, v86, v226\n"
    "v_mul_f32 v87, v87, v226\n"
    "v_mul_f32 v88, v88, v226\n"
    "v_mul_f32 v89, v89, v226\n"
    "v_mul_f32 v90, v90, v226\n"
    "v_mul_f32 v91, v91, v226\n"
    "v_mul_f32 v92, v92, v226\n"
    "v_mul_f32 v93, v93, v226\n"
    "v_mul_f32 v94, v94, v226\n"
    "v_mul_f32 v95, v95, v226\n"
    "v_exp_f32 v226, v144\n"
    "v_exp_f32 v227, v145\n"
    "v_exp_f32 v228, v146\n"
    "v_exp_f32 v229, v147\n"
    "v_cvt_pk_bf16_f32 v176, v226, v227\n"
    "v_exp_f32 v226, v148\n"
    "v_exp_f32 v227, v149\n"
    "v_cvt_pk_bf16_f32 v177, v228, v229\n"
    "v_exp_f32 v228, v150\n"
    "v_exp_f32 v229, v151\n"
    "v_cvt_pk_bf16_f32 v178, v226, v227\n"
    "v_cvt_pk_bf16_f32 v179, v228, v229\n"
    "s_nop 1\n"
    "v_mfma_f32_16x16x32_bf16 v[208:211], a[240:243], v[176:179], 0\n"
    "s_add_u32 s68, s68, 4\n"
    "s_addc_u32 s69, s69, 0\n"
    "s_nop 7\n"
    "s_setpc_b64 s[68:69]\n"
    "Lslow0_3:\n"
    "s_nop 7\n"
    "v_max3_f32 v224, v152, v153, v154\n"
    "v_max3_f32 v225, v155, v156, v157\n"
    "v_max3_f32 v224, v224, v158, v159\n"
    "v_max_f32 v224, v224, v225\n"
    "v_mov_b32 v225, v224\n"
    "s_nop 1\n"
    "v_permlane16_swap_b32 v224, v225\n"
    "v_max_f32 v224, v224, v225\n"
    "v_mov_b32 v225, v224\n"
    "s_nop 1\n"
    "v_permlane32_swap_b32 v224, v225\n"
    "v_max_f32 v224, v224, v225\n"
    "v_ceil_f32 v224, v224\n"
    "v_max_f32 v224, 0, v224\n"
    "v_sub_f32 v152, v152, v224\n"
    "v_sub_f32 v153, v153, v224\n"
    "v_sub_f32 v154, v154, v224\n"
    "v_sub_f32 v155, v155, v224\n"
    "v_sub_f32 v156, v156, v224\n"
    "v_sub_f32 v157, v157, v224\n"
    "v_sub_f32 v158, v158, v224\n"
    "v_sub_f32 v159, v159, v224\n"
    "v_sub_f32 v200, v200, v224\n"
    "v_sub_f32 v201, v201, v224\n"
    "v_sub_f32 v202, v202, v224\n"
    "v_sub_f32 v203, v203, v224\n"
    "v_sub_f32 v225, 0, v224\n"
    "v_exp_f32 v226, v225\n"
    "s_nop 0\n"
    "v_mul_f32 v215, v215, v226\n"
    "v_mul_f32 v96, v96, v226\n"
    "v_mul_f32 v97, v97, v226\n"
    "v_mul_f32 v98, v98, v226\n"
    "v_mul_f32 v99, v99, v226\n"
    "v_mul_f32 v100, v100, v226\n"
    "v_mul_f32 v101, v101, v226\n"
    "v_mul_f32 v102, v102, v226\n"
    "v_mul_f32 v103, v103, v226\n"
    "v_mul_f32 v104, v104, v226\n"
    "v_mul_f32 v105, v105, v226\n"
    "v_mul_f32 v106, v106, v226\n"
    "v_mul_f32 v107, v107, v226\n"
    "v_mul_f32 v108, v108, v226\n"
    "v_mul_f32 v109, v109, v226\n"
    "v_mul_f32 v110, v110, v226\n"
    "v_mul_f32 v111, v111, v226\n"
    "v_mul_f32 v112, v112, v226\n"
    "v_mul_f32 v113, v113, v226\n"
    "v_mul_f32 v114, v114, v226\n"
    "v_mul_f32 v115, v115, v226\n"
    "v_mul_f32 v116, v116, v226\n"
    "v_mul_f32 v117, v117, v226\n"
    "v_mul_f32 v118, v118, v226\n"
    "v_mul_f32 v119, v119, v226\n"
    "v_mul_f32 v120, v120, v226\n"
    "v_mul_f32 v121, v121, v226\n"
    "v_mul_f32 v122, v122, v226\n"
    "v_mul_f32 v123, v123, v226\n"
    "v_mul_f32 v124, v124, v226\n"
    "v_mul_f32 v125, v125, v226\n"
    "v_mul_f32 v126, v126, v226\n"
    "v_mul_f32 v127, v127, v226\n"
    "v_exp_f32 v226, v152\n"
    "v_exp_f32 v227, v153\n"
    "v_exp_f32 v228, v154\n"
    "v_exp_f32 v229, v155\n"
    "v_cvt_pk_bf16_f32 v180, v226, v227\n"
    "v_exp_f32 v226, v156\n"
    "v_exp_f32 v227, v157\n"
    "v_cvt_pk_bf16_f32 v181, v228, v229\n"
    "v_exp_f32 v228, v158\n"
    "v_exp_f32 v229, v159\n"
    "v_cvt_pk_bf16_f32 v182, v226, v227\n"
    "v_cvt_pk_bf16_f32 v183, v228, v229\n"
    "s_nop 1\n"
    "v_mfma_f32_16x16x32_bf16 v[208:211], a[240:243], v[180:183], 0\n"
    "s_add_u32 s68, s68, 4\n"
    "s_addc_u32 s69, s69, 0\n"
    "s_nop 7\n"
    "s_setpc_b64 s[68:69]\n"
    "Lslow0_4:\n"
    "s_nop 7\n"
    "v_max3_f32 v224, v160, v161, v162\n"
    "v_max3_f32 v225, v163, v164, v165\n"
    "v_max3_f32 v224, v224, v166, v167\n"
    "v_max_f32 v224, v224, v225\n"
    "v_mov_b32 v225, v224\n"
    "s_nop 1\n"
    "v_permlane16_swap_b32 v224, v225\n"
    "v_max_f32 v224, v224, v225\n"
    "v_mov_b32 v225, v224\n"
    "s_nop 1\n"
    "v_permlane32_swap_b32 v224, v225\n"
    "v_max_f32 v224, v224, v225\n"
    "v_ceil_f32 v224, v224\n"
    "v_max_f32 v224, 0, v224\n"
    "v_sub_f32 v160, v160, v224\n"
    "v_sub_f32 v161, v161, v224\n"
    "v_sub_f32 v162, v162, v224\n"
    "v_sub_f32 v163, v163, v224\n"
    "v_sub_f32 v164, v164, v224\n"
    "v_sub_f32 v165, v165, v224\n"
    "v_sub_f32 v166, v166, v224\n"
    "v_sub_f32 v167, v167, v224\n"
    "v_sub_f32 v204, v204, v224\n"
    "v_sub_f32 v205, v205, v224\n"
    "v_sub_f32 v206, v206, v224\n"
    "v_sub_f32 v207, v207, v224\n"
    "v_sub_f32 v225, 0, v224\n"
    "v_exp_f32 v226, v225\n"
    "s_nop 0\n"
    "v_mul_f32 v216, v216, v226\n"
    "v_accvgpr_read_b32 v225, a208\n"
    "v_mul_f32 v225, v225, v226\n"
    "v_accvgpr_write_b32 a208, v225\n"
    "v_accvgpr_read_b32 v225, a209\n"
    "v_mul_f32 v225, v225, v226\n"
    "v_accvgpr_write_b32 a209, v225\n"
    "v_accvgpr_read_b32 v225, a210\n"
    "v_mul_f32 v225, v225, v226\n"
    "v_accvgpr_write_b32 a210, v225\n"
    "v_accvgpr_read_b32 v225, a211\n"
    "v_mul_f32 v225, v225, v226\n"
    "v_accvgpr_write_b32 a211, v225\n"
    "v_accvgpr_read_b32 v225, a212\n"
    "v_mul_f32 v225, v225, v226\n"
    "v_accvgpr_write_b32 a212, v225\n"
    "v_accvgpr_read_b32 v225, a213\n"
    "v_mul_f32 v225, v225, v226\n"
    "v_accvgpr_write_b32 a213, v225\n"
    "v_accvgpr_read_b32 v225, a214\n"
    "v_mul_f32 v225, v225, v226\n"
    "v_accvgpr_write_b32 a214, v225\n"
    "v_accvgpr_read_b32 v225, a215\n"
    "v_mul_f32 v225, v225, v226\n"
    "v_accvgpr_write_b32 a215, v225\n"
    "v_accvgpr_read_b32 v225, a216\n"
    "v_mul_f32 v225, v225, v226\n"
    "v_accvgpr_write_b32 a216, v225\n"
    "v_accvgpr_read_b32 v225, a217\n"
    "v_mul_f32 v225, v225, v226\n"
    "v_accvgpr_write_b32 a217, v225\n"
    "v_accvgpr_read_b32 v225, a218\n"
    "v_mul_f32 v225, v225, v226\n"
    "v_accvgpr_write_b32 a218, v225\n"
    "v_accvgpr_read_b32 v225, a219\n"
    "v_mul_f32 v225, v225, v226\n"
    "v_accvgpr_write_b32 a219, v225\n"
    "v_accvgpr_read_b32 v225, a220\n"
    "v_mul_f32 v225, v225, v226\n"
    "v_accvgpr_write_b32 a220, v225\n"
    "v_accvgpr_read_b32 v225, a221\n"
    "v_mul_f32 v225, v225, v226\n"
    "v_accvgpr_write_b32 a221, v225\n"
    "v_accvgpr_read_b32 v225, a222\n"
    "v_mul_f32 v225, v225, v226\n"
    "v_accvgpr_write_b32 a222, v225\n"
    "v_accvgpr_read_b32 v225, a223\n"
    "v_mul_f32 v225, v225, v226\n"
    "v_accvgpr_write_b32 a223, v225\n"
    "v_accvgpr_read_b32 v225, a224\n"
    "v_mul_f32 v225, v225, v226\n"
    "v_accvgpr_write_b32 a224, v225\n"
    "v_accvgpr_read_b32 v225, a225\n"
    "v_mul_f32 v225, v225, v226\n"
    "v_accvgpr_write_b32 a225, v225\n"
    "v_accvgpr_read_b32 v225, a226\n"
    "v_mul_f32 v225, v225, v226\n"
    "v_accvgpr_write_b32 a226, v225\n"
    "v_accvgpr_read_b32 v225, a227\n"
    "v_mul_f32 v225, v225, v226\n"
    "v_accvgpr_write_b32 a227, v225\n"
    "v_accvgpr_read_b32 v225, a228\n"
    "v_mul_f32 v225, v225, v226\n"
    "v_accvgpr_write_b32 a228, v225\n"
    "v_accvgpr_read_b32 v225, a229\n"
    "v_mul_f32 v225, v225, v226\n"
    "v_accvgpr_write_b32 a229, v225\n"
    "v_accvgpr_read_b32 v225, a230\n"
    "v_mul_f32 v225, v225, v226\n"
    "v_accvgpr_write_b32 a230, v225\n"
    "v_accvgpr_read_b32 v225, a231\n"
    "v_mul_f32 v225, v225, v226\n"
    "v_accvgpr_write_b32 a231, v225\n"
    "v_accvgpr_read_b32 v225, a232\n"
    "v_mul_f32 v225, v225, v226\n"
    "v_accvgpr_write_b32 a232, v225\n"
    "v_accvgpr_read_b32 v225, a233\n"
    "v_mul_f32 v225, v225, v226\n"
    "v_accvgpr_write_b32 a233, v225\n"
    "v_accvgpr_read_b32 v225, a234\n"
    "v_mul_f32 v225, v225, v226\n"
    "v_accvgpr_write_b32 a234, v225\n"
    "v_accvgpr_read_b32 v225, a235\n"
    "v_mul_f32 v225, v225, v226\n"
    "v_accvgpr_write_b32 a235, v225\n"
    "v_accvgpr_read_b32 v225, a236\n"
    "v_mul_f32 v225, v225, v226\n"
    "v_accvgpr_write_b32 a236, v225\n"
    "v_accvgpr_read_b32 v225, a237\n"
    "v_mul_f32 v225, v225, v226\n"
    "v_accvgpr_write_b32 a237, v225\n"
    "v_accvgpr_read_b32 v225, a238\n"
    "v_mul_f32 v225, v225, v226\n"
    "v_accvgpr_write_b32 a238, v225\n"
    "v_accvgpr_read_b32 v225, a239\n"
    "v_mul_f32 v225, v225, v226\n"
    "v_accvgpr_write_b32 a239, v225\n"
    "v_exp_f32 v226, v160\n"
    "v_exp_f32 v227, v161\n"
    "v_exp_f32 v228, v162\n"
    "v_exp_f32 v229, v163\n"
    "v_cvt_pk_bf16_f32 v184, v226, v227\n"
    "v_exp_f32 v226, v164\n"
    "v_exp_f32 v227, v165\n"
    "v_cvt_pk_bf16_f32 v185, v228, v229\n"
    "v_exp_f32 v228, v166\n"
    "v_exp_f32 v229, v167\n"
    "v_cvt_pk_bf16_f32 v186, v226, v227\n"
    "v_cvt_pk_bf16_f32 v187, v228, v229\n"
    "s_nop 1\n"
    "v_mfma_f32_16x16x32_bf16 v[208:211], a[240:243], v[184:187], 0\n"
    "s_add_u32 s68, s68, 4\n"
    "s_addc_u32 s69, s69, 0\n"
    "s_nop 7\n"
    "s_setpc_b64 s[68:69]\n"
    "Lstub1_b_1:\n"
    "s_getpc_b64 s[68:69]\n"
    "s_branch Lslow1_0\n"
    "s_branch Lback1_b_1\n"
    "Lstub1_b_2:\n"
    "s_getpc_b64 s[68:69]\n"
    "s_branch Lslow1_1\n"
    "s_branch Lback1_b_2\n"
    "Lstub1_b_3:\n"
    "s_getpc_b64 s[68:69]\n"
    "s_branch Lslow1_2\n"
    "s_branch Lback1_b_3\n"
    "Lstub1_2_0:\n"
    "s_getpc_b64 s[68:69]\n"
    "s_branch Lslow1_3\n"
    "s_branch Lback1_2_0\n"
    "Lstub1_2_1:\n"
    "s_getpc_b64 s[68:69]\n"
    "s_branch Lslow1_0\n"
    "s_branch Lback1_2_1\n"
    "Lstub1_2_2:\n"
    "s_getpc_b64 s[68:69]\n"
    "s_branch Lslow1_1\n"
    "s_branch Lback1_2_2\n"
    "Lstub1_2_3:\n"
    "s_getpc_b64 s[68:69]\n"
    "s_branch Lslow1_2\n"
    "s_branch Lback1_2_3\n"
    "Lstub1_3_0:\n"
    "s_getpc_b64 s[68:69]\n"
    "s_branch Lslow1_3\n"
    "s_branch Lback1_3_0\n"
    "Lstub1_3_1:\n"
    "s_getpc_b64 s[68:69]\n"
    "s_branch Lslow1_0\n"
    "s_branch Lback1_3_1\n"
    "Lstub1_3_2:\n"
    "s_getpc_b64 s[68:69]\n"
    "s_branch Lslow1_1\n"
    "s_branch Lback1_3_2\n"
    "Lstub1_3_3:\n"
    "s_getpc_b64 s[68:69]\n"
    "s_branch Lslow1_2\n"
    "s_branch Lback1_3_3\n"
    "Lstub1_0_0:\n"
    "s_getpc_b64 s[68:69]\n"
    "s_branch Lslow1_3\n"
    "s_branch Lback1_0_0\n"
    "Lstub1_0_1:\n"
    "s_getpc_b64 s[68:69]\n"
    "s_branch Lslow1_0\n"
    "s_branch Lback1_0_1\n"
    "Lstub1_0_2:\n"
    "s_getpc_b64 s[68:69]\n"
    "s_branch Lslow1_1\n"
    "s_branch Lback1_0_2\n"
    "Lstub1_0_3:\n"
    "s_getpc_b64 s[68:69]\n"
    "s_branch Lslow1_2\n"
    "s_branch Lback1_0_3\n"
    "Lstub1_1_0:\n"
    "s_getpc_b64 s[68:69]\n"
    "s_branch Lslow1_3\n"
    "s_branch Lback1_1_0\n"
    "Lstub1_1_1:\n"
    "s_getpc_b64 s[68:69]\n"
    "s_branch Lslow1_0\n"
    "s_branch Lback1_1_1\n"
    "Lstub1_1_2:\n"
    "s_getpc_b64 s[68:69]\n"
    "s_branch Lslow1_1\n"
    "s_branch Lback1_1_2\n"
    "Lstub1_1_3:\n"
    "s_getpc_b64 s[68:69]\n"
    "s_branch Lslow1_2\n"
    "s_branch Lback1_1_3\n"
    "Lstub1_c_0:\n"
    "s_getpc_b64 s[68:69]\n"
    "s_branch Lslow1_3\n"
    "s_branch Lback1_c_0\n"
    "Lstub1_c_1:\n"
    "s_getpc_b64 s[68:69]\n"
    "s_branch Lslow1_0\n"
    "s_branch Lback1_c_1\n"
    "Lstub1_c_2:\n"
    "s_getpc_b64 s[68:69]\n"
    "s_branch Lslow1_1\n"
    "s_branch Lback1_c_2\n"
    "Lstub1_c_3:\n"
    "s_getpc_b64 s[68:69]\n"
    "s_branch Lslow1_2\n"
    "s_branch Lback1_c_3\n"
    "Lstub1_d_0:\n"
    "s_getpc_b64 s[68:69]\n"
    "s_branch Lslow1_3\n"
    "s_branch Lback1_d_0\n"
    "Lstub1_d_1:\n"
    "s_getpc_b64 s[68:69]\n"
    "s_branch Lslow1_0\n"
    "s_branch Lback1_d_1\n"
    "Lstub1_d_2:\n"
    "s_getpc_b64 s[68:69]\n"
    "s_branch Lslow1_1\n"
    "s_branch Lback1_d_2\n"
    "Lstub1_d_3:\n"
    "s_getpc_b64 s[68:69]\n"
    "s_branch Lslow1_2\n"
    "s_branch Lback1_d_3\n"
    "Lstub1_e:\n"
    "s_getpc_b64 s[68:69]\n"
    "s_branch Lslow1_3\n"
    "s_branch Lback1_e\n"
    "Lslow1_0:\n"
    "s_nop 7\n"
    "v_max3_f32 v224, v128, v129, v130\n"
    "v_max3_f32 v225, v131, v132, v133\n"
    "v_max3_f32 v224, v224, v134, v135\n"
    "v_max_f32 v224, v224, v225\n"
    "v_mov_b32 v225, v224\n"
    "s_nop 1\n"
    "v_permlane16_swap_b32 v224, v225\n"
    "v_max_f32 v224, v224, v225\n"
    "v_mov_b32 v225, v224\n"
    "s_nop 1\n"
    "v_permlane32_swap_b32 v224, v225\n"
    "v_max_f32 v224, v224, v225\n"
    "v_ceil_f32 v224, v224\n"
    "v_max_f32 v224, 0, v224\n"
    "v_sub_f32 v128, v128, v224\n"
    "v_sub_f32 v129, v129, v224\n"
    "v_sub_f32 v130, v130, v224\n"
    "v_sub_f32 v131, v131, v224\n"
    "v_sub_f32 v132, v132, v224\n"
    "v_sub_f32 v133, v133, v224\n"
    "v_sub_f32 v134, v134, v224\n"
    "v_sub_f32 v135, v135, v224\n"
    "v_sub_f32 v188, v188, v224\n"
    "v_sub_f32 v189, v189, v224\n"
    "v_sub_f32 v190, v190, v224\n"
    "v_sub_f32 v191, v191, v224\n"
    "v_sub_f32 v225, 0, v224\n"
    "v_exp_f32 v226, v225\n"
    "s_nop 0\n"
    "v_mul_f32 v212, v212, v226\n"
    "v_mul_f32 v0, v0, v226\n"
    "v_mul_f32 v1, v1, v226\n"
    "v_mul_f32 v2, v2, v226\n"
    "v_mul_f32 v3, v3, v226\n"
    "v_mul_f32 v4, v4, v226\n"
    "v_mul_f32 v5, v5, v226\n"
    "v_mul_f32 v6, v6, v226\n"
    "v_mul_f32 v7, v7, v226\n"
    "v_mul_f32 v8, v8, v226\n"
    "v_mul_f32 v9, v9, v226\n"
    "v_mul_f32 v10, v10, v226\n"
    "v_mul_f32 v11, v11, v226\n"
    "v_mul_f32 v12, v12, v226\n"
    "v_mul_f32 v13, v13, v226\n"
    "v_mul_f32 v14, v14, v226\n"
    "v_mul_f32 v15, v15, v226\n"
    "v_mul_f32 v16, v16, v226\n"
    "v_mul_f32 v17, v17, v226\n"
    "v_mul_f32 v18, v18, v226\n"
    "v_mul_f32 v19, v19, v226\n"
    "v_mul_f32 v20, v20, v226\n"
    "v_mul_f32 v21, v21, v226\n"
    "v_mul_f32 v22, v22, v226\n"
    "v_mul_f32 v23, v23, v226\n"
    "v_mul_f32 v24, v24, v226\n"
    "v_mul_f32 v25, v25, v226\n"
    "v_mul_f32 v26, v26, v226\n"
    "v_mul_f32 v27, v27, v226\n"
    "v_mul_f32 v28, v28, v226\n"
    "v_mul_f32 v29, v29, v226\n"
    "v_mul_f32 v30, v30, v226\n"
    "v_mul_f32 v31, v31, v226\n"
    "v_exp_f32 v226, v128\n"
    "v_exp_f32 v227, v129\n"
    "v_exp_f32 v228, v130\n"
    "v_exp_f32 v229, v131\n"
    "v_cvt_pk_bf16_f32 v168, v226, v227\n"
    "v_exp_f32 v226, v132\n"
    "v_exp_f32 v227, v133\n"
    "v_cvt_pk_bf16_f32 v169, v228, v229\n"
    "v_exp_f32 v228, v134\n"
    "v_exp_f32 v229, v135\n"
    "v_cvt_pk_bf16_f32 v170, v226, v227\n"
    "v_cvt_pk_bf16_f32 v171, v228, v229\n"
    "s_nop 1\n"
    "v_mfma_f32_16x16x32_bf16 v[208:211], a[240:243], v[168:171], 0\n"
    "s_add_u32 s68, s68, 4\n"
    "s_addc_u32 s69, s69, 0\n"
    "s_nop 7\n"
    "s_setpc_b64 s[68:69]\n"
    "Lslow1_1:\n"
    "s_nop 7\n"
    "v_max3_f32 v224, v136, v137, v138\n"
    "v_max3_f32 v225, v139, v140, v141\n"
    "v_max3_f32 v224, v224, v142, v143\n"
    "v_max_f32 v224, v224, v225\n"
    "v_mov_b32 v225, v224\n"
    "s_nop 1\n"
    "v_permlane16_swap_b32 v224, v225\n"
    "v_max_f32 v224, v224, v225\n"
    "v_mov_b32 v225, v224\n"
    "s_nop 1\n"
    "v_permlane32_swap_b32 v224, v225\n"
    "v_max_f32 v224, v224, v225\n"
    "v_ceil_f32 v224, v224\n"
    "v_max_f32 v224, 0, v224\n"
    "v_sub_f32 v136, v136, v224\n"
    "v_sub_f32 v137, v137, v224\n"
    "v_sub_f32 v138, v138, v224\n"
    "v_sub_f32 v139, v139, v224\n"
    "v_sub_f32 v140, v140, v224\n"
    "v_sub_f32 v141, v141, v224\n"
    "v_sub_f32 v142, v142, v224\n"
    "v_sub_f32 v143, v143, v224\n"
    "v_sub_f32 v192, v192, v224\n"
    "v_sub_f32 v193, v193, v224\n"
    "v_sub_f32 v194, v194, v224\n"
    "v_sub_f32 v195, v195, v224\n"
    "v_sub_f32 v225, 0, v224\n"
    "v_exp_f32 v226, v225\n"
    "s_nop 0\n"
    "v_mul_f32 v213, v213, v226\n"
    "v_mul_f32 v32, v32, v226\n"
    "v_mul_f32 v33, v33, v226\n"
    "v_mul_f32 v34, v34, v226\n"
    "v_mul_f32 v35, v35, v226\n"
    "v_mul_f32 v36, v36, v226\n"
    "v_mul_f32 v37, v37, v226\n"
    "v_mul_f32 v38, v38, v226\n"
    "v_mul_f32 v39, v39, v226\n"
    "v_mul_f32 v40, v40, v226\n"
    "v_mul_f32 v41, v41, v226\n"
    "v_mul_f32 v42, v42, v226\n"
    "v_mul_f32 v43, v43, v226\n"
    "v_mul_f32 v44, v44, v226\n"
    "v_mul_f32 v45, v45, v226\n"
    "v_mul_f32 v46, v46, v226\n"
    "v_mul_f32 v47, v47, v226\n"
    "v_mul_f32 v48, v48, v226\n"
    "v_mul_f32 v49, v49, v226\n"
    "v_mul_f32 v50, v50, v226\n"
    "v_mul_f32 v51, v51, v226\n"
    "v_mul_f32 v52, v52, v226\n"
    "v_mul_f32 v53, v53, v226\n"
    "v_mul_f32 v54, v54, v226\n"
    "v_mul_f32 v55, v55, v226\n"
    "v_mul_f32 v56, v56, v226\n"
    "v_mul_f32 v57, v57, v226\n"
    "v_mul_f32 v58, v58, v226\n"
    "v_mul_f32 v59, v59, v226\n"
    "v_mul_f32 v60, v60, v226\n"
    "v_mul_f32 v61, v61, v226\n"
    "v_mul_f32 v62, v62, v226\n"
    "v_mul_f32 v63, v63, v226\n"
    "v_exp_f32 v226, v136\n"
    "v_exp_f32 v227, v137\n"
    "v_exp_f32 v228, v138\n"
    "v_exp_f32 v229, v139\n"
    "v_cvt_pk_bf16_f32 v172, v226, v227\n"
    "v_exp_f32 v226, v140\n"
    "v_exp_f32 v227, v141\n"
    "v_cvt_pk_bf16_f32 v173, v228, v229\n"
    "v_exp_f32 v228, v142\n"
    "v_exp_f32 v229, v143\n"
    "v_cvt_pk_bf16_f32 v174, v226, v227\n"
    "v_cvt_pk_bf16_f32 v175, v228, v229\n"
    "s_nop 1\n"
    "v_mfma_f32_16x16x32_bf16 v[208:211], a[240:243], v[172:175], 0\n"
    "s_add_u32 s68, s68, 4\n"
    "s_addc_u32 s69, s69, 0\n"
    "s_nop 7\n"
    "s_setpc_b64 s[68:69]\n"
    "Lslow1_2:\n"
    "s_nop 7\n"
    "v_max3_f32 v224, v144, v145, v146\n"
    "v_max3_f32 v225, v147, v148, v149\n"
    "v_max3_f32 v224, v224, v150, v151\n"
    "v_max_f32 v224, v224, v225\n"
    "v_mov_b32 v225, v224\n"
    "s_nop 1\n"
    "v_permlane16_swap_b32 v224, v225\n"
    "v_max_f32 v224, v224, v225\n"
    "v_mov_b32 v225, v224\n"
    "s_nop 1\n"
    "v_permlane32_swap_b32 v224, v225\n"
    "v_max_f32 v224, v224, v225\n"
    "v_ceil_f32 v224, v224\n"
    "v_max_f32 v224, 0, v224\n"
    "v_sub_f32 v144, v144, v224\n"
    "v_sub_f32 v145, v145, v224\n"
    "v_sub_f32 v146, v146, v224\n"
    "v_sub_f32 v147, v147, v224\n"
    "v_sub_f32 v148, v148, v224\n"
    "v_sub_f32 v149, v149, v224\n"
    "v_sub_f32 v150, v150, v224\n"
    "v_sub_f32 v151, v151, v224\n"
    "v_sub_f32 v196, v196, v224\n"
    "v_sub_f32 v197, v197, v224\n"
    "v_sub_f32 v198, v198, v224\n"
    "v_sub_f32 v199, v199, v224\n"
    "v_sub_f32 v225, 0, v224\n"
    "v_exp_f32 v226, v225\n"
    "s_nop 0\n"
    "v_mul_f32 v214, v214, v226\n"
    "v_mul_f32 v64, v64, v226\n"
    "v_mul_f32 v65, v65, v226\n"
    "v_mul_f32 v66, v66, v226\n"
    "v_mul_f32 v67, v67, v226\n"
    "v_mul_f32 v68, v68, v226\n"
    "v_mul_f32 v69, v69, v226\n"
    "v_mul_f32 v70, v70, v226\n"
    "v_mul_f32 v71, v71, v226\n"
    "v_mul_f32 v72, v72, v226\n"
    "v_mul_f32 v73, v73, v226\n"
    "v_mul_f32 v74, v74, v226\n"
    "v_mul_f32 v75, v75, v226\n"
    "v_mul_f32 v76, v76, v226\n"
    "v_mul_f32 v77, v77, v226\n"
    "v_mul_f32 v78, v78, v226\n"
    "v_mul_f32 v79, v79, v226\n"
    "v_mul_f32 v80, v80, v226\n"
    "v_mul_f32 v81, v81, v226\n"
    "v_mul_f32 v82, v82, v226\n"
    "v_mul_f32 v83, v83, v226\n"
    "v_mul_f32 v84, v84, v226\n"
    "v_mul_f32 v85, v85, v226\n"
    "v_mul_f32 v86, v86, v226\n"
    "v_mul_f32 v87, v87, v226\n"
    "v_mul_f32 v88, v88, v226\n"
    "v_mul_f32 v89, v89, v226\n"
    "v_mul_f32 v90, v90, v226\n"
    "v_mul_f32 v91, v91, v226\n"
    "v_mul_f32 v92, v92, v226\n"
    "v_mul_f32 v93, v93, v226\n"
    "v_mul_f32 v94, v94, v226\n"
    "v_mul_f32 v95, v95, v226\n"
    "v_exp_f32 v226, v144\n"
    "v_exp_f32 v227, v145\n"
    "v_exp_f32 v228, v146\n"
    "v_exp_f32 v229, v147\n"
    "v_cvt_pk_bf16_f32 v176, v226, v227\n"
    "v_exp_f32 v226, v148\n"
    "v_exp_f32 v227, v149\n"
    "v_cvt_pk_bf16_f32 v177, v228, v229\n"
    "v_exp_f32 v228, v150\n"
    "v_exp_f32 v229, v151\n"
    "v_cvt_pk_bf16_f32 v178, v226, v227\n"
    "v_cvt_pk_bf16_f32 v179, v228, v229\n"
    "s_nop 1\n"
    "v_mfma_f32_16x16x32_bf16 v[208:211], a[240:243], v[176:179], 0\n"
    "s_add_u32 s68, s68, 4\n"
    "s_addc_u32 s69, s69, 0\n"
    "s_nop 7\n"
    "s_setpc_b64 s[68:69]\n"
    "Lslow1_3:\n"
    "s_nop 7\n"
    "v_max3_f32 v224, v152, v153, v154\n"
    "v_max3_f32 v225, v155, v156, v157\n"
    "v_max3_f32 v224, v224, v158, v159\n"
    "v_max_f32 v224, v224, v225\n"
    "v_mov_b32 v225, v224\n"
    "s_nop 1\n"
    "v_permlane16_swap_b32 v224, v225\n"
    "v_max_f32 v224, v224, v225\n"
    "v_mov_b32 v225, v224\n"
    "s_nop 1\n"
    "v_permlane32_swap_b32 v224, v225\n"
    "v_max_f32 v224, v224, v225\n"
    "v_ceil_f32 v224, v224\n"
    "v_max_f32 v224, 0, v224\n"
    "v_sub_f32 v152, v152, v224\n"
    "v_sub_f32 v153, v153, v224\n"
    "v_sub_f32 v154, v154, v224\n"
    "v_sub_f32 v155, v155, v224\n"
    "v_sub_f32 v156, v156, v224\n"
    "v_sub_f32 v157, v157, v224\n"
    "v_sub_f32 v158, v158, v224\n"
    "v_sub_f32 v159, v159, v224\n"
    "v_sub_f32 v200, v200, v224\n"
    "v_sub_f32 v201, v201, v224\n"
    "v_sub_f32 v202, v202, v224\n"
    "v_sub_f32 v203, v203, v224\n"
    "v_sub_f32 v225, 0, v224\n"
    "v_exp_f32 v226, v225\n"
    "s_nop 0\n"
    "v_mul_f32 v215, v215, v226\n"
    "v_mul_f32 v96, v96, v226\n"
    "v_mul_f32 v97, v97, v226\n"
    "v_mul_f32 v98, v98, v226\n"
    "v_mul_f32 v99, v99, v226\n"
    "v_mul_f32 v100, v100, v226\n"
    "v_mul_f32 v101, v101, v226\n"
    "v_mul_f32 v102, v102, v226\n"
    "v_mul_f32 v103, v103, v226\n"
    "v_mul_f32 v104, v104, v226\n"
    "v_mul_f32 v105, v105, v226\n"
    "v_mul_f32 v106, v106, v226\n"
    "v_mul_f32 v107, v107, v226\n"
    "v_mul_f32 v108, v108, v226\n"
    "v_mul_f32 v109, v109, v226\n"
    "v_mul_f32 v110, v110, v226\n"
    "v_mul_f32 v111, v111, v226\n"
    "v_mul_f32 v112, v112, v226\n"
    "v_mul_f32 v113, v113, v226\n"
    "v_mul_f32 v114, v114, v226\n"
    "v_mul_f32 v115, v115, v226\n"
    "v_mul_f32 v116, v116, v226\n"
    "v_mul_f32 v117, v117, v226\n"
    "v_mul_f32 v118, v118, v226\n"
    "v_mul_f32 v119, v119, v226\n"
    "v_mul_f32 v120, v120, v226\n"
    "v_mul_f32 v121, v121, v226\n"
    "v_mul_f32 v122, v122, v226\n"
    "v_mul_f32 v123, v123, v226\n"
    "v_mul_f32 v124, v124, v226\n"
    "v_mul_f32 v125, v125, v226\n"
    "v_mul_f32 v126, v126, v226\n"
    "v_mul_f32 v127, v127, v226\n"
    "v_exp_f32 v226, v152\n"
    "v_exp_f32 v227, v153\n"
    "v_exp_f32 v228, v154\n"
    "v_exp_f32 v229, v155\n"
    "v_cvt_pk_bf16_f32 v180, v226, v227\n"
    "v_exp_f32 v226, v156\n"
    "v_exp_f32 v227, v157\n"
    "v_cvt_pk_bf16_f32 v181, v228, v229\n"
    "v_exp_f32 v228, v158\n"
    "v_exp_f32 v229, v159\n"
    "v_cvt_pk_bf16_f32 v182, v226, v227\n"
    "v_cvt_pk_bf16_f32 v183, v228, v229\n"
    "s_nop 1\n"
    "v_mfma_f32_16x16x32_bf16 v[208:211], a[240:243], v[180:183], 0\n"
    "s_add_u32 s68, s68, 4\n"
    "s_addc_u32 s69, s69, 0\n"
    "s_nop 7\n"
    "s_setpc_b64 s[68:69]\n"
    "Lend:\n"
    :
    : "s"(ka), "s"(id), "s"(wave), "s"(ldsb)
    :
      "v0", "v1", "v2", "v3", "v4", "v5", "v6", "v7", "v8", "v9", "v10", "v11", "v12", "v13", "v14", "v15", "v16", "v17", "v18", "v19", "v20", "v21", "v22", "v23",
      "v24", "v25", "v26", "v27", "v28", "v29", "v30", "v31", "v32", "v33", "v34", "v35", "v36", "v37", "v38", "v39", "v40", "v41", "v42", "v43", "v44", "v45", "v46", "v47",
      "v48", "v49", "v50", "v51", "v52", "v53", "v54", "v55", "v56", "v57", "v58", "v59", "v60", "v61", "v62", "v63", "v64", "v65", "v66", "v67", "v68", "v69", "v70", "v71",
      "v72", "v73", "v74", "v75", "v76", "v77", "v78", "v79", "v80", "v81", "v82", "v83", "v84", "v85", "v86", "v87", "v88", "v89", "v90", "v91", "v92", "v93", "v94", "v95",
      "v96", "v97", "v98", "v99", "v100", "v101", "v102", "v103", "v104", "v105", "v106", "v107", "v108", "v109", "v110", "v111", "v112", "v113", "v114", "v115", "v116", "v117", "v118", "v119",
      "v120", "v121", "v122", "v123", "v124", "v125", "v126", "v127", "v128", "v129", "v130", "v131", "v132", "v133", "v134", "v135", "v136", "v137", "v138", "v139", "v140", "v141", "v142", "v143",
      "v144", "v145", "v146", "v147", "v148", "v149", "v150", "v151", "v152", "v153", "v154", "v155", "v156", "v157", "v158", "v159", "v160", "v161", "v162", "v163", "v164", "v165", "v166", "v167",
      "v168", "v169", "v170", "v171", "v172", "v173", "v174", "v175", "v176", "v177", "v178", "v179", "v180", "v181", "v182", "v183", "v184", "v185", "v186", "v187", "v188", "v189", "v190", "v191",
      "v192", "v193", "v194", "v195", "v196", "v197", "v198", "v199", "v200", "v201", "v202", "v203", "v204", "v205", "v206", "v207", "v208", "v209", "v210", "v211", "v212", "v213", "v214", "v215",
      "v216", "v217", "v218", "v219", "v220", "v221", "v222", "v223", "v224", "v225", "v226", "v227", "v228", "v229", "v230", "v231", "v232", "v233", "v234", "v235", "v236", "v237", "v238", "v239",
      "v240", "v241", "v242", "v243", "v244", "v245", "v246", "v247", "v248", "v249", "v250", "v251", "v252", "v253", "v254", "v255", "a0", "a1", "a2", "a3", "a4", "a5", "a6", "a7",
      "a8", "a9", "a10", "a11", "a12", "a13", "a14", "a15", "a16", "a17", "a18", "a19", "a20", "a21", "a22", "a23", "a24", "a25", "a26", "a27", "a28", "a29", "a30", "a31",
      "a32", "a33", "a34", "a35", "a36", "a37", "a38", "a39", "a40", "a41", "a42", "a43", "a44", "a45", "a46", "a47", "a48", "a49", "a50", "a51", "a52", "a53", "a54", "a55",
      "a56", "a57", "a58", "a59", "a60", "a61", "a62", "a63", "a64", "a65", "a66", "a67", "a68", "a69", "a70", "a71", "a72", "a73", "a74", "a75", "a76", "a77", "a78", "a79",
      "a80", "a81", "a82", "a83", "a84", "a85", "a86", "a87", "a88", "a89", "a90", "a91", "a92", "a93", "a94", "a95", "a96", "a97", "a98", "a99", "a100", "a101", "a102", "a103",
      "a104", "a105", "a106", "a107", "a108", "a109", "a110", "a111", "a112", "a113", "a114", "a115", "a116", "a117", "a118", "a119", "a120", "a121", "a122", "a123", "a124", "a125", "a126", "a127",
      "a128", "a129", "a130", "a131", "a132", "a133", "a134", "a135", "a136", "a137", "a138", "a139", "a140", "a141", "a142", "a143", "a144", "a145", "a146", "a147", "a148", "a149", "a150", "a151",
      "a152", "a153", "a154", "a155", "a156", "a157", "a158", "a159", "a160", "a161", "a162", "a163", "a164", "a165", "a166", "a167", "a168", "a169", "a170", "a171", "a172", "a173", "a174", "a175",
      "a176", "a177", "a178", "a179", "a180", "a181", "a182", "a183", "a184", "a185", "a186", "a187", "a188", "a189", "a190", "a191", "a192", "a193", "a194", "a195", "a196", "a197", "a198", "a199",
      "a200", "a201", "a202", "a203", "a204", "a205", "a206", "a207", "a208", "a209", "a210", "a211", "a212", "a213", "a214", "a215", "a216", "a217", "a218", "a219", "a220", "a221", "a222", "a223",
      "a224", "a225", "a226", "a227", "a228", "a229", "a230", "a231", "a232", "a233", "a234", "a235", "a236", "a237", "a238", "a239", "a240", "a241", "a242", "a243", "a244", "a245", "a246", "a247",
      "a248", "a249", "a250", "a251", "a252", "a253", "a254", "a255", "s5", "s6", "s7", "s8", "s9", "s10", "s11", "s12", "s13", "s14", "s15", "s16", "s17", "s18", "s19", "s20",
      "s21", "s22", "s23", "s24", "s25", "s26", "s27", "s28", "s29", "s30", "s31", "s32", "s33", "s34", "s35", "s36", "s37", "s38", "s39", "s40", "s41", "s42", "s43", "s44",
      "s45", "s46", "s47", "s48", "s49", "s50", "s51", "s52", "s53", "s54", "s55", "s56", "s57", "s58", "s59", "s60", "s61", "s62", "s63", "s64", "s65", "s66", "s67", "s68",
      "s69", "s70", "s71", "s72", "s73", "s74", "s75", "s76", "s77", "s78", "s79", "s80", "s81", "s82", "s83", "s84", "s85", "s86", "s87", "s88", "s89", "s90", "s91", "s92",
      "s93", "s94", "s95", "s96", "s97", "s98", "s99", "vcc", "scc", "memory");
}

extern "C" void kernel_launch(void* const* d_in, const int* in_sizes, int n_in, void* d_out, int out_size, void* d_ws, size_t ws_size, hipStream_t stream) {
  fused_k<<<256, 256, 0, stream>>>((const char*)d_in[0], (const char*)d_in[1], (const char*)d_in[2], (const char*)d_in[3], (const char*)d_in[4],
                                   (const char*)d_in[5], (const char*)d_in[6], (const char*)d_in[7], (const char*)d_in[8], (const char*)d_in[9], (char*)d_ws, (char*)d_out);
}
```
